```python
import torch

def _softmax(x, axis=-1):
    return torch.softmax(x, dim=axis)
def _gelu(x):
    return torch.nn.functional.gelu(x, approximate="tanh")
def _silu(x):
    return torch.nn.functional.silu(x)
def _sigmoid(x):
    return torch.sigmoid(x)
def _layernorm(x, axis=-1, eps=1e-5):
    m = x.mean(dim=axis, keepdim=True)
    v = x.var(dim=axis, keepdim=True, unbiased=False)
    return (x - m) / torch.sqrt(v + eps)
def _scatter_add(tgt, idx, src, axis=0):
    return tgt.clone().index_add_(axis, idx, src)
def _segment_sum(src, seg, n):
    out = torch.zeros((n,) + src.shape[1:], dtype=src.dtype)
    return out.index_add_(0, seg, src)
def _pad(x, pw):
    flat = tuple(v for pair in reversed(pw) for v in pair)
    return torch.nn.functional.pad(x, flat)

kernel_name = 'sequential_preLN__spectral_fnet__moe_arch_mod__prompt'

def setup_inputs():

    _w = (lambda a: (lambda t: t.to(torch.bfloat16) if t.is_floating_point() else t)(torch.as_tensor(a)))
    def _A_setup():
        B, S, D = 1, 2048, 1024
        ns = torch.arange(S)[:, None] * torch.arange(S)[None, :]
        nd = torch.arange(D)[:, None] * torch.arange(D)[None, :]
        Ws_re = torch.cos(-2.0 * torch.pi * ns / S).to(float)
        Ws_im = torch.sin(-2.0 * torch.pi * ns / S).to(float)
        Wd_re = torch.cos(-2.0 * torch.pi * nd / D).to(float)
        Wd_im = torch.sin(-2.0 * torch.pi * nd / D).to(float)
        return {
            "x": _w((lambda *s: torch.randn(*s, dtype=torch.bfloat16) * 0.05)(B, S, D)),
            "dft_seq_re": _w(Ws_re),
            "dft_seq_im": _w(Ws_im),
            "dft_hid_re": _w(Wd_re),
            "dft_hid_im": _w(Wd_im),
        }
    _A_in = _A_setup()

    def _B_setup():
        B, S, D, F, C = 1, 2048, 1024, 3584, 1
        return {
            "x": _w((lambda *s: torch.randn(*s, dtype=torch.bfloat16) * 0.05)(B, S, D)),
            "w_r": _w((lambda *s: torch.randn(*s, dtype=torch.bfloat16) * 1.6)(D)),
            "w_gate": _w((lambda *s: torch.randn(*s, dtype=torch.bfloat16) * 0.4)(D, F)),
            "w_up": _w((lambda *s: torch.randn(*s, dtype=torch.bfloat16) * 0.4)(D, F)),
            "w_down": _w((lambda *s: torch.randn(*s, dtype=torch.bfloat16) * 0.4)(F, D)),
        }
    _B_in = _B_setup()

    return {
        "A_x": _A_in["x"],
        "A_dft_seq_re": _A_in["dft_seq_re"],
        "A_dft_seq_im": _A_in["dft_seq_im"],
        "A_dft_hid_re": _A_in["dft_hid_re"],
        "A_dft_hid_im": _A_in["dft_hid_im"],
        "B_x": _B_in["x"],
        "B_w_r": _B_in["w_r"],
        "B_w_gate": _B_in["w_gate"],
        "B_w_up": _B_in["w_up"],
        "B_w_down": _B_in["w_down"],
    }

def reference(A_x, A_dft_seq_re, A_dft_seq_im, A_dft_hid_re, A_dft_hid_im, B_x, B_w_r, B_w_gate, B_w_up, B_w_down):
    def _A_ref(x, dft_seq_re, dft_seq_im, dft_hid_re, dft_hid_im):
        a = torch.einsum("bsd,ed->bse", x, dft_hid_re)
        b = torch.einsum("bsd,ed->bse", x, dft_hid_im)
        y = torch.einsum("ts,bse->bte", dft_seq_re, a) - torch.einsum("ts,bse->bte", dft_seq_im, b)
        return y

    def _B_ref(x, w_r, w_gate, w_up, w_down):
        B, S, D = x.shape
        C = 1
        r = x @ w_r
        idx = torch.argsort(-r, axis=-1)[:, :C]
        idx_e = (lambda x, n, axis: torch.repeat_interleave(x, n, dim=axis))(idx.reshape(B, C, 1), D, axis=2)
        x_sel = (lambda x, i, axis: torch.take_along_dim(x, i, dim=axis))(x, idx_e, axis=1)
        g = _silu(x_sel @ w_gate)
        u = x_sel @ w_up
        y = (g * u) @ w_down
        r_sel = _sigmoid((lambda x, i, axis: torch.take_along_dim(x, i, dim=axis))(r, idx, axis=1))
        y = y * r_sel.reshape(B, C, 1)
        b_off = (torch.arange(B) * S).reshape(B, 1)
        flat_idx = (idx + b_off).reshape(B * C)
        zero = (x * 0.0).reshape(B * S, D)
        upd = _scatter_add(zero, flat_idx, y.reshape(B * C, D), axis=0)
        return x + upd.reshape(B, S, D)

    x = A_x
    h = x + _A_ref(_layernorm(x, axis=-1), A_dft_seq_re, A_dft_seq_im, A_dft_hid_re, A_dft_hid_im)
    h2 = h + _B_ref(_layernorm(h, axis=-1), B_w_r, B_w_gate, B_w_up, B_w_down)
    return h2
```

```cpp
#include <hip/hip_runtime.h>
#include <hip/hip_bf16.h>
#include <stdint.h>

typedef unsigned short bf16_t;
typedef short bf16x8 __attribute__((ext_vector_type(8)));
typedef short bf16x4 __attribute__((ext_vector_type(4)));
typedef float f32x4 __attribute__((ext_vector_type(4)));
typedef unsigned u32x4 __attribute__((ext_vector_type(4)));
typedef unsigned u32x2 __attribute__((ext_vector_type(2)));
typedef unsigned u32x3 __attribute__((ext_vector_type(3)));
typedef unsigned long long u64;
#define LAS __attribute__((address_space(3)))
#define GAS __attribute__((address_space(1)))

constexpr int S_LEN = 2048, D_LEN = 1024, F_LEN = 3584;
constexpr unsigned SENT = 0xFFFFFFFFu;
constexpr int SPIN_CAP = 1 << 17;

constexpr size_t WS_KEYS = 0;
constexpr size_t WS_FLAGS = 16384;
constexpr size_t WS_XRD = 524288;
constexpr size_t WS_FLAG1 = 262144;
constexpr size_t WS_TAG = 131072;
constexpr size_t WS_VOTE = WS_TAG + 4096;
constexpr size_t WS_PROBE = (size_t)20 << 20;
constexpr size_t WS_AT = (size_t)4 << 20;
constexpr size_t WS_Y = (size_t)12 << 20;
constexpr size_t WS_STATS = (size_t)16 << 20;
constexpr size_t WS_PART = WS_STATS + (512u << 10);
constexpr size_t WS_CAND = WS_STATS + (1024u << 10);
constexpr size_t WS_PRE = WS_STATS + (1152u << 10);
constexpr size_t FILL_BYTES = (size_t)1408 << 10;

__device__ __forceinline__ float bflo(unsigned v) { return __uint_as_float(v << 16); }
__device__ __forceinline__ float bfhi(unsigned v) { return __uint_as_float(v & 0xffff0000u); }
__device__ __forceinline__ unsigned pk_bf16(float lo, float hi) {
    typedef __bf16 bf2 __attribute__((ext_vector_type(2)));
    typedef float f2 __attribute__((ext_vector_type(2)));
    f2 v = {lo, hi};
    bf2 r = __builtin_convertvector(v, bf2);
    return __builtin_bit_cast(unsigned, r);
}
__device__ __forceinline__ float rbf(float x) { return bflo(pk_bf16(x, 0.f) & 0xffffu); }

template <int CTRL> __device__ __forceinline__ unsigned dppu(unsigned v) {
    return (unsigned)__builtin_amdgcn_update_dpp(0, (int)v, CTRL, 0xf, 0xf, true);
}
template <int CTRL> __device__ __forceinline__ float dppf(float v) { return __uint_as_float(dppu<CTRL>(__float_as_uint(v))); }
__device__ __forceinline__ void swap16(unsigned v, unsigned& a, unsigned& b) {
    auto r = __builtin_amdgcn_permlane16_swap(v, v, false, false);
    a = r[0]; b = r[1];
}
__device__ __forceinline__ void swap32(unsigned v, unsigned& a, unsigned& b) {
    auto r = __builtin_amdgcn_permlane32_swap(v, v, false, false);
    a = r[0]; b = r[1];
}
__device__ __forceinline__ float sum_rows(float v) {
    unsigned a, b;
    swap16(__float_as_uint(v), a, b); v = __uint_as_float(a) + __uint_as_float(b);
    swap32(__float_as_uint(v), a, b); v = __uint_as_float(a) + __uint_as_float(b);
    return v;
}
__device__ __forceinline__ float wave_sum(float v) {
    v += dppf<0xB1>(v);
    v += dppf<0x4E>(v);
    v += dppf<0x141>(v);
    v += dppf<0x140>(v);
    return sum_rows(v);
}
__device__ __forceinline__ unsigned wave_max_u32(unsigned v) {
    v = max(v, dppu<0xB1>(v));
    v = max(v, dppu<0x4E>(v));
    v = max(v, dppu<0x141>(v));
    v = max(v, dppu<0x140>(v));
    unsigned a, b;
    swap16(v, a, b); v = max(a, b);
    swap32(v, a, b); v = max(a, b);
    return v;
}
__device__ __forceinline__ float sel4(int g, float a, float b, float c, float d) {
    const float t0 = (g & 1) ? b : a, t1 = (g & 1) ? d : c;
    return (g & 2) ? t1 : t0;
}
__device__ __forceinline__ unsigned umax4(u32x4 v) { return max(max(v[0], v[1]), max(v[2], v[3])); }

__device__ __forceinline__ void touch_line(const void* p, unsigned lds_dst) {
    unsigned keep;
    asm volatile("s_mov_b32 %0, m0\n\ts_mov_b32 m0, %2\n\ts_nop 0\n\tglobal_load_lds_dword %1, off\n\ts_mov_b32 m0, %0"
                 : "=&s"(keep) : "v"(p), "s"(lds_dst) : "memory");
}
__device__ __forceinline__ __amdgpu_buffer_rsrc_t mk_rsrc(const void* p, unsigned bytes) {
    return __builtin_amdgcn_make_buffer_rsrc((void*)p, 0, (int)bytes, 0x00020000);
}
#define LDS_LOAD(w, v, o, pol) "buffer_load_" w " " v ", %0, %2 offen" o pol " lds"
#define LDS_M0 "s_mov_b32 m0, %1\n\ts_nop 0\n\t"
__device__ __forceinline__ void dma_nt(__amdgpu_buffer_rsrc_t r, unsigned dst, int voff, int soff) {
    asm volatile(LDS_M0 LDS_LOAD("dwordx4", "%3", "", " nt") :: "s"(r), "s"(dst), "s"(soff), "v"(voff) : "memory", "m0");
}
template <bool SC1> __device__ __forceinline__ void dma_word(__amdgpu_buffer_rsrc_t r, unsigned dst, int voff) {
    if constexpr (SC1) asm volatile(LDS_M0 LDS_LOAD("dword", "%3", "", " sc1") :: "s"(r), "s"(dst), "n"(0), "v"(voff) : "memory", "m0");
    else asm volatile(LDS_M0 LDS_LOAD("dword", "%3", "", "") :: "s"(r), "s"(dst), "n"(0), "v"(voff) : "memory", "m0");
}
__device__ __forceinline__ void dma_2k(__amdgpu_buffer_rsrc_t r, unsigned dst, int v0, int v1, int soff) {
    asm volatile(LDS_M0 LDS_LOAD("dwordx4", "%3", "", " sc1") "\n\t" LDS_LOAD("dwordx4", "%4", " offset:1024", " sc1")
                 :: "s"(r), "s"(dst), "s"(soff), "v"(v0), "v"(v1) : "memory", "m0");
}
template <bool SC1> __device__ __forceinline__ void dma_4k(__amdgpu_buffer_rsrc_t r, unsigned dst, const int (&v)[4], int soff) {
#define FOUR(pol) asm volatile(LDS_M0 LDS_LOAD("dwordx4", "%3", "", pol) "\n\t" LDS_LOAD("dwordx4", "%4", " offset:1024", pol) "\n\t" \
        LDS_LOAD("dwordx4", "%5", " offset:2048", pol) "\n\t" LDS_LOAD("dwordx4", "%6", " offset:3072", pol) \
        :: "s"(r), "s"(dst), "s"(soff), "v"(v[0]), "v"(v[1]), "v"(v[2]), "v"(v[3]) : "memory", "m0")
    if constexpr (SC1) FOUR(" sc1"); else FOUR("");
#undef FOUR
}
#define CODE_BYTES 25984
__device__ __forceinline__ __amdgpu_buffer_rsrc_t code_rsrc() {
    unsigned long long p; unsigned d;
    asm volatile("s_getpc_b64 %0\n1:\n\ts_mov_b32 %1, 1b-_Z12fused_kernel4Args" : "=s"(p), "=s"(d));
    return __builtin_amdgcn_make_buffer_rsrc((void*)(p - d), 0, CODE_BYTES, 0x00020000);
}
#define LD16_SC1(rs, off) __builtin_amdgcn_raw_buffer_load_b128(rs, off, 0, 16)
#define ST16_SC1(v, rs, off) __builtin_amdgcn_raw_buffer_store_b128(v, rs, off, 0, 16)

constexpr int BT = 128, BK = 64, NSTAGE = 4;
constexpr int TILE_BYTES = BT * BK * 2;
constexpr int STAGE_BYTES = 2 * TILE_BYTES;
constexpr int RING_BYTES = NSTAGE * STAGE_BYTES;
constexpr int L_DUMMY = RING_BYTES;
constexpr int L_CTL = RING_BYTES + 256;
constexpr int L_RED = RING_BYTES + 512;

#define WAIT_VM(n) asm volatile("s_waitcnt vmcnt(" #n ")" ::: "memory")
#define WAIT_VM0() do { asm volatile("" ::: "memory"); __builtin_amdgcn_s_waitcnt(0x0F70); asm volatile("s_waitcnt vmcnt(0)" ::: "memory"); } while (0)
#define WAIT_LGKM0() asm volatile("s_waitcnt lgkmcnt(0)" ::: "memory")
#define BARRIER() do { asm volatile("" ::: "memory"); __builtin_amdgcn_s_barrier(); asm volatile("" ::: "memory"); } while (0)

__device__ __forceinline__ int prow(int rho) {
    const int w = rho >> 6, m = (rho >> 4) & 3, q = (rho >> 2) & 3, r = rho & 3;
    return w * 64 + (m >> 1) * 32 + q * 8 + (m & 1) * 4 + r;
}
__device__ __forceinline__ unsigned r2key(float r, int row) {
    const unsigned u = pk_bf16(r, 0.f) & 0xffffu;
    const unsigned o = (u & 0x8000u) ? (~u & 0xffffu) : (u | 0x8000u);
    return (o << 16) | (0xffffu - (unsigned)row);
}
__device__ __forceinline__ float key2r(unsigned k) {
    const unsigned o = k >> 16;
    return bflo((o & 0x8000u) ? (o & 0x7fffu) : (~o & 0xffffu));
}
#define RESET_T 2
#define RESET_U 3
#define VOTE_T 4
#define BUBBLE 8
#define FIN_PRIO 3
#define W_PRE 2
#define W_PRE_SL 20
#define BQ 1
#define POLL_T 12
#define AUX_SLEEP 6
#define WD_EARLY 4
#define C2_START 0
#define C2_STAG 7
#define TAIL_PRIO 3
#define STAT_DELAY 14
#define CODE_WG 1
#define AUX_STAY 1
#define T_DELAY 0
#define WS_END 1
#define CATCH1 4
#define CPF_MASK1 0x1080u
#define CPF_MASK2 0x1080u
#define CPF_STAG 5
#define CPF_FIX 0x1000u
#define OUT_AUX 0
#define POLL_SLEEP 4
__device__ __forceinline__ float dot2bf(unsigned a, unsigned b, float c) {
    typedef __bf16 bf2 __attribute__((ext_vector_type(2)));
    return __builtin_amdgcn_fdot2_f32_bf16(__builtin_bit_cast(bf2, a), __builtin_bit_cast(bf2, b), c, false);
}

#define STAMP(i) do { } while (0)
#define CSTAMP(i) do { } while (0)
#define FLUSH() do { } while (0)
#define TSV do { } while (0)

#define KSHIFT 4

constexpr int L_HT = ((32 + KSHIFT) % NSTAGE) * STAGE_BYTES;
constexpr int L_WD0 = ((33 + KSHIFT) % NSTAGE) * STAGE_BYTES;
constexpr int L_WD1 = ((34 + KSHIFT) % NSTAGE) * STAGE_BYTES;
constexpr int L_YB = RING_BYTES;
constexpr int L_ATAB = L_YB;
constexpr int LDS_BYTES = RING_BYTES + 32768;

#define SB() __builtin_amdgcn_sched_barrier(0)
template <int N> __device__ __forceinline__ void wait_vm() { asm volatile("s_waitcnt vmcnt(%0)" :: "n"(N) : "memory"); }
__device__ __forceinline__ int at_tile(int c, int par) { return ((c << 8) & 0x78000) | (((c << 2) & 0x2000) ^ par); }
__device__ __forceinline__ int at_panel(int ep) { return ep << 19; }
__device__ __forceinline__ int pre_off(int slot) { const int s_ = slot >= 56, l = slot - 56 * s_; return (l >> 2) * 16384 + s_ * 8192 + (l & 3) * 2048; }
static_assert(KSHIFT >= 3 && KSHIFT % 2 == 0, "flag samples must have replaced older contents of their slot; even tile counts");
struct Ring {
    __amdgpu_buffer_rsrc_t rP, rQ, rF;
    int voP[4], voQ[4], voF;
    LAS unsigned char* fl;
    unsigned lb;
    int sp, sq;
    int poff, par;
    int tq, ti;
    bool img;
    template <int LD, bool PIMG> __device__ __forceinline__ void setup(int lw, int lane) {
        const int r = lane >> 3, v0 = (PIMG ? r : (r & 3) + (r & 4) * 2) * (LD * 2) + (((lane & 7) ^ (lane >> 4)) << 4);
#pragma unroll
        for (int j = 0; j < 4; ++j) {
            voQ[j] = (v0 ^ ((j & 1) * 64)) + (PIMG ? j * 8 : (j & 1) * 16 + (j >> 1) * 4) * (LD * 2) - j * 1024;
            voP[j] = PIMG ? (lw >> 1) * 16384 + (lw & 1) * 4096 + lane * 16 : voQ[j];
        }
        img = PIMG;
    }
    __device__ __forceinline__ void seek(int col, int pof) {
        sq = col; poff = pof;
        sp = (img ? at_tile(sq, par) : sq) + poff;
    }
    __device__ __forceinline__ void step() {
        sq = (sq + BK * 2) & 4095;
        sp = (img ? at_tile(sq, par) : sq) + poff;
        asm volatile("" : "+s"(sq), "+s"(sp));
    }
    __device__ __forceinline__ void sample() { dma_word<true>(rF, (unsigned)(size_t)fl, voF); }
    __device__ __forceinline__ unsigned peek(unsigned addr) {
        unsigned v;
        asm volatile("ds_read_b32 %0, %1\n\ts_waitcnt lgkmcnt(0)" : "=v"(v) : "v"(addr) : "memory");
        return v;
    }
    __device__ __forceinline__ bool seen() { return __builtin_amdgcn_readfirstlane(peek((unsigned)(size_t)fl)) == 1u; }
    __device__ __forceinline__ void gate(unsigned want, unsigned sa, unsigned need, unsigned& have) {
#pragma unroll 1
        for (int spin = 0; spin <= SPIN_CAP && (need & ~have) != 0u; ++spin) {
            if (spin) { sample(); WAIT_VM(0); __builtin_amdgcn_s_sleep(1); }
            const unsigned long long m = __ballot(peek(sa) == want);
            have = (unsigned)m & (unsigned)(m >> 32);
        }
    }
    template <int AUX> __device__ __forceinline__ void issueP(LAS unsigned char* base) { dma_4k<AUX != 0>(rP, (unsigned)(size_t)base, voP, sp); }
    __device__ __forceinline__ void issueQ(LAS unsigned char* base) { dma_4k<false>(rQ, (unsigned)(size_t)base + TILE_BYTES, voQ, sq); }
    template <bool P2> __device__ __forceinline__ void issue(LAS unsigned char* lds) {
        issueP<P2 ? 16 : 0>(lds + lb);
        issueQ(lds + lb);
        SB();
        if (P2) sample();
        lb = (lb + STAGE_BYTES) & (RING_BYTES - 1);
        asm volatile("" : "+s"(lb));
        if (P2) step();
        SB();
    }
    __device__ __forceinline__ void next1() {
        ++ti;
        sp = sq = __builtin_amdgcn_readlane(tq, ti);
        asm volatile("s_nop 4" : "+s"(sq), "+s"(sp), "+s"(ti));
        SB();
    }
    template <int CAT> __device__ __forceinline__ void ramp(LAS unsigned char* lds) {
#pragma unroll 1
        for (int i = 1; i <= CAT + 1; ++i) {
            issue<false>(lds);
            next1();
            if (i == CAT + 1) continue;
            WAIT_VM(8);
            BARRIER();
        }
    }
    template <bool P2, bool SAMP, int W = 16> __device__ __forceinline__ void steady(int cnt) {
        int t, u;
#define LD(v, r, s, o) "buffer_load_dwordx4 " v ", " r ", " s " offen" o " lds\n\t"
        if constexpr (P2) {
#define BODY(SMP) asm volatile("s_mov_b32 m0, %0\n\ts_branch 2f\n1:\n\ts_waitcnt vmcnt(%17)\n\ts_barrier\n2:\n\t" \
            LD("%8", "%6", "%2", " sc1") LD("%8", "%6", "%2", " offset:1024 sc1") LD("%8", "%6", "%2", " offset:2048 sc1") LD("%8", "%6", "%2", " offset:3072 sc1") \
            SMP "s_mov_b32 %4, %1\n\ts_add_i32 %1, %1, 0x80\n\ts_add_i32 m0, %0, 0x4000\n\ts_and_b32 %1, %1, 0xfff\n\t" \
            LD("%9", "%7", "%4", "") "s_lshl_b32 %2, %1, 8\n\ts_lshl_b32 %5, %1, 2\n\ts_and_b32 %2, %2, 0x78000\n\t" \
            LD("%10", "%7", "%4", " offset:1024") "s_and_b32 %5, %5, 0x2000\n\ts_xor_b32 %5, %5, %13\n\ts_or_b32 %2, %2, %5\n\t" \
            LD("%11", "%7", "%4", " offset:2048") "s_add_i32 %0, %0, 0x8000\n\ts_and_b32 %0, %0, 0x1ffff\n\ts_add_i32 %3, %3, -1\n\ts_cmp_lg_u32 %3, 0\n\t" \
            LD("%12", "%7", "%4", " offset:3072") "s_mov_b32 m0, %0\n\ts_cbranch_scc1 1b\n\ts_waitcnt vmcnt(%17)\n\ts_barrier" \
            : "+s"(lb), "+s"(sq), "+s"(sp), "+s"(cnt), "=&s"(t), "=&s"(u) \
            : "s"(rP), "s"(rQ), "v"(voP[0]), "v"(voQ[0]), "v"(voQ[1]), "v"(voQ[2]), "v"(voQ[3]), "s"(par | poff), "s"(rF), "v"(voF), "s"((unsigned)(size_t)fl), "n"(W) \
            : "memory", "scc", "m0")
            if constexpr (SAMP) BODY("s_mov_b32 m0, %16\n\ts_nop 0\n\tbuffer_load_dword %15, %14, 0 offen sc1 lds\n\t");
            else BODY("");
#undef BODY
        } else {
            asm volatile("s_mov_b32 m0, %0\n\ts_branch 2f\n1:\n\ts_waitcnt vmcnt(16)\n\ts_barrier\n2:\n\t"
                LD("%6", "%4", "%1", "") LD("%7", "%4", "%1", " offset:1024") LD("%8", "%4", "%1", " offset:2048") LD("%9", "%4", "%1", " offset:3072")
                "s_add_i32 m0, %0, 0x4000\n\ts_mov_b32 %3, %1\n\t"
                LD("%6", "%5", "%3", "") "s_add_i32 %2, %2, 1\n\ts_add_i32 %0, %0, 0x8000\n\t"
                LD("%7", "%5", "%3", " offset:1024") "s_and_b32 %0, %0, 0x1ffff\n\tv_readlane_b32 %1, %10, %2\n\t"
                LD("%8", "%5", "%3", " offset:2048") "s_cmp_lg_u32 %2, %11\n\t"
                LD("%9", "%5", "%3", " offset:3072") "s_mov_b32 m0, %0\n\ts_cbranch_scc1 1b\n\ts_waitcnt vmcnt(16)\n\ts_barrier"
                : "+s"(lb), "+s"(sq), "+s"(ti), "=&s"(t)
                : "s"(rP), "s"(rQ), "v"(voQ[0]), "v"(voQ[1]), "v"(voQ[2]), "v"(voQ[3]), "v"(tq), "n"(D_LEN / BK)
                : "memory", "scc", "m0");
            (void)cnt;
            (void)u;
        }
#undef LD
    }
};

template <bool PLIN, bool FIRST, class Pre>
__device__ __forceinline__ void compute_loop(const LAS unsigned char* lds, f32x4 (&acc)[4][4], int NT, int wp, int wq, int lane, Pre pre) {
    const int fr = lane & 15, fq = lane >> 4, key = (fr >> 1) & 7;
    const unsigned l0 = (unsigned)(size_t)lds;
    const unsigned oP0 = l0 + (PLIN ? wp * 8192 + lane * 16 : (wp * 64 + fr) * 128 + ((fq ^ key) * 16));
    const unsigned oP1 = l0 + (PLIN ? wp * 8192 + 1024 + lane * 16 : (wp * 64 + fr) * 128 + (((4 + fq) ^ key) * 16));
    const unsigned oQ0 = l0 + TILE_BYTES + (wq * 64 + fr) * 128 + ((fq ^ key) * 16);
    const unsigned oQ1 = l0 + TILE_BYTES + (wq * 64 + fr) * 128 + (((4 + fq) ^ key) * 16);
    unsigned aP0 = oP0, aQ0 = oQ0, aP1 = oP1, aQ1 = oQ1;
    bf16x8 p0[4], p1[4], r0[4], r1[4], q0[4], q1[4];
#pragma unroll
    for (int i = 0; i < 16; ++i) asm volatile("" : "+v"(acc[i >> 2][i & 3]));
    asm volatile("" : "+v"(aP0), "+v"(aQ0), "+v"(aP1), "+v"(aQ1));
#define MM(pp, qq, m, n) do { acc[m][n] = __builtin_amdgcn_mfma_f32_16x16x32_bf16(pp[m], qq[n], acc[m][n], 0, 0, 0); SB(); } while (0)
#define RD(dst, i, a, st) do { dst[i] = *(const LAS bf16x8*)((const LAS unsigned char*)(size_t)(a) + (st) * STAGE_BYTES + (i) * 2048); SB(); } while (0)
#define ADR(a) do { a ^= 2 * STAGE_BYTES; asm volatile("" : "+v"(a)); SB(); } while (0)
    __builtin_amdgcn_s_waitcnt(0xC07F);
    SB();
    if (FIRST) BARRIER();
    SB();
    RD(p0, 0, aP0, 0); RD(q0, 0, aQ0, 0); RD(p1, 0, aP1, 0); RD(q1, 0, aQ1, 0); RD(p1, 1, aP1, 0); RD(p0, 1, aP0, 0);
    RD(p0, 2, aP0, 0); RD(p1, 2, aP1, 0); RD(p1, 3, aP1, 0); RD(p0, 3, aP0, 0);
    pre();
    SB();
    int t = NT;
#pragma unroll 1
    do {
        MM(p0, q0, 0, 0); MM(p1, q1, 0, 0); RD(q0, 1, aQ0, 0);
        MM(p1, q1, 1, 0); MM(p0, q0, 1, 0); RD(q1, 1, aQ1, 0);
        MM(p0, q0, 2, 0); MM(p1, q1, 2, 0); RD(q0, 2, aQ0, 0);
        MM(p1, q1, 3, 0); MM(p0, q0, 3, 0); RD(q1, 2, aQ1, 0);
        MM(p0, q0, 3, 1); MM(p1, q1, 3, 1); RD(q0, 3, aQ0, 0);
        MM(p1, q1, 2, 1); MM(p0, q0, 2, 1); RD(q1, 3, aQ1, 0);
        MM(p0, q0, 1, 1); MM(p1, q1, 1, 1);
        MM(p1, q1, 0, 1); MM(p0, q0, 0, 1);
        MM(p0, q0, 0, 2); MM(p1, q1, 0, 2); __builtin_amdgcn_s_waitcnt(0xC07F); SB(); BARRIER(); SB();
        MM(p1, q1, 1, 2); RD(r0, 0, aP0, 1); MM(p0, q0, 1, 2); RD(q0, 0, aQ0, 1);
        MM(p0, q0, 2, 2); RD(r1, 0, aP1, 1); MM(p1, q1, 2, 2); RD(q1, 0, aQ1, 1);
        MM(p1, q1, 3, 2); MM(p0, q0, 3, 2); RD(r1, 1, aP1, 1);
        MM(p0, q0, 3, 3); RD(r0, 1, aP0, 1); MM(p1, q1, 3, 3);
        MM(p1, q1, 2, 3); RD(r0, 2, aP0, 1); MM(p0, q0, 2, 3); RD(r1, 2, aP1, 1);
        MM(p0, q0, 1, 3); MM(p1, q1, 1, 3); RD(r1, 3, aP1, 1);
        MM(p1, q1, 0, 3); RD(r0, 3, aP0, 1); MM(p0, q0, 0, 3);

        MM(r0, q0, 0, 0); MM(r1, q1, 0, 0); RD(q0, 1, aQ0, 1);
        MM(r1, q1, 1, 0); MM(r0, q0, 1, 0); RD(q1, 1, aQ1, 1);
        MM(r0, q0, 2, 0); MM(r1, q1, 2, 0); RD(q0, 2, aQ0, 1);
        MM(r1, q1, 3, 0); MM(r0, q0, 3, 0); RD(q1, 2, aQ1, 1);
        MM(r0, q0, 3, 1); MM(r1, q1, 3, 1); RD(q0, 3, aQ0, 1);
        MM(r1, q1, 2, 1); MM(r0, q0, 2, 1); RD(q1, 3, aQ1, 1);
        MM(r0, q0, 1, 1); t -= 2; asm volatile("" : "+s"(t)); SB(); MM(r1, q1, 1, 1); ADR(aP0);
        MM(r1, q1, 0, 1); ADR(aP1); MM(r0, q0, 0, 1); ADR(aQ0);
        MM(r0, q0, 0, 2); ADR(aQ1); MM(r1, q1, 0, 2); __builtin_amdgcn_s_waitcnt(0xC07F); SB(); BARRIER(); SB();
        MM(r1, q1, 1, 2); RD(p0, 0, aP0, 0); MM(r0, q0, 1, 2); RD(q0, 0, aQ0, 0);
        MM(r0, q0, 2, 2); RD(p1, 0, aP1, 0); MM(r1, q1, 2, 2); RD(q1, 0, aQ1, 0);
        MM(r1, q1, 3, 2); MM(r0, q0, 3, 2); RD(p1, 1, aP1, 0);
        MM(r0, q0, 3, 3); RD(p0, 1, aP0, 0); MM(r1, q1, 3, 3);
        MM(r1, q1, 2, 3); RD(p0, 2, aP0, 0); MM(r0, q0, 2, 3); RD(p1, 2, aP1, 0);
        MM(r0, q0, 1, 3); MM(r1, q1, 1, 3); RD(p1, 3, aP1, 0);
        MM(r1, q1, 0, 3); RD(p0, 3, aP0, 0); MM(r0, q0, 0, 3);
    } while (t != 0);
    SB();
#undef MM
#undef RD
#undef ADR
}

struct Args {
    const bf16_t* x; const bf16_t* wre; const bf16_t* wim; char* ws; const bf16_t* cr; const bf16_t* ci;
    const bf16_t* w_r; const bf16_t* wg; const bf16_t* wu; const bf16_t* wd; bf16_t* out;
};

__device__ __forceinline__ unsigned poll_winner(const char* ws, int rep, int lane, unsigned& mine) {
    GAS unsigned* kp = (GAS unsigned*)(ws + WS_KEYS + (size_t)(rep >> 2) * 8192 + (rep & 3) * 256) + (lane & 31);
    unsigned kw;
    for (int spin = 0;; ++spin) {
            asm volatile("" ::: "memory");
        kw = __hip_atomic_load(kp, __ATOMIC_RELAXED, __HIP_MEMORY_SCOPE_AGENT);
        if (__all(kw != 0u) || spin > SPIN_CAP) break;
        __builtin_amdgcn_s_sleep(POLL_SLEEP);
    }
    mine = kw;
    return __builtin_amdgcn_readfirstlane(wave_max_u32(kw));
}

__device__ __forceinline__ void stage2(char* w2, int slot, int lw, LAS unsigned char* img0, LAS unsigned char* img1, LAS unsigned char* abuf, int lane) {
    const int g = lane >> 4, n = lane & 15;
    const __amdgpu_buffer_rsrc_t rPi = mk_rsrc(w2 + WS_PRE + pre_off(slot), 2048);
    unsigned pp[8];
    auto request = [&]() {
        asm volatile("" ::: "memory");
#pragma unroll
        for (int k = 0; k < 8; ++k) pp[k] = __builtin_amdgcn_raw_buffer_load_b32(rPi, k * 256 + lane * 4, 0, 16);
        SB();
    };
    request();
    bf16x8 wf[16];
    {
        const int q = (lane >> 2) & 3, ky = q | ((g & 1) << 2);
#pragma unroll
        for (int j = 0; j < 16; ++j) {
            LAS unsigned char* t = (g < 2 ? img0 : img1) + (8 * (g & 1) + q) * 512 + (((j & 7) ^ ky) + (j & 8)) * 32 + (lane & 3) * 8;
            const bf16x4 w0 = __builtin_amdgcn_ds_read_tr16_b64_v4i16((LAS bf16x4*)t);
            const bf16x4 w1 = __builtin_amdgcn_ds_read_tr16_b64_v4i16((LAS bf16x4*)(t + 2048));
            wf[j] = __builtin_shufflevector(w0, w1, 0, 1, 2, 3, 4, 5, 6, 7);
        }
        SB();
#pragma unroll
        for (int j = 0; j < 16; ++j) asm volatile("" : "+v"(wf[j]));
    }
    unsigned aa[5], pto = lane * 16;
#pragma unroll
    for (int m = 0; m < 4; ++m) aa[m] = (unsigned)(size_t)abuf + ((n >> 2) == m ? 0 : 64) + g * 16;
    aa[4] = (unsigned)(size_t)abuf + (lane & 31) * 2;
    __amdgpu_buffer_rsrc_t rPt = mk_rsrc(w2 + WS_PART + __builtin_amdgcn_readfirstlane((slot >> 2) * 16384 + (lw >> 1) * 8192 + (slot & 3) * 2048 + (lw & 1) * 1024), 1024);
    asm volatile("" : "+v"(aa[0]), "+v"(aa[1]), "+v"(aa[2]), "+v"(aa[3]), "+v"(aa[4]), "+v"(pto));
    asm volatile("" : "+s"(rPt));
    SB();
#pragma unroll 1
    for (int spin = 0; spin <= SPIN_CAP && !__all(max(max(max(pp[0], pp[1]), max(pp[2], pp[3])), max(max(pp[4], pp[5]), max(pp[6], pp[7]))) != SENT); ++spin) request();
    const float sv = ((__uint_as_float(pp[0]) + __uint_as_float(pp[1])) + (__uint_as_float(pp[2]) + __uint_as_float(pp[3]))) +
                     ((__uint_as_float(pp[4]) + __uint_as_float(pp[5])) + (__uint_as_float(pp[6]) + __uint_as_float(pp[7])));
    unsigned ug, uu;
    swap32(__float_as_uint(sv), ug, uu);
    const float gg = rbf(__uint_as_float(ug)), up = rbf(__uint_as_float(uu));
    const unsigned act = pk_bf16(rbf(gg * __builtin_amdgcn_rcpf(1.0f + __expf(-gg))) * up, 0.f);
    *(volatile LAS unsigned short*)(size_t)aa[4] = (unsigned short)act;
    bf16x8 af[4];
#pragma unroll
    for (int m = 0; m < 4; ++m) af[m] = *(volatile LAS bf16x8*)(size_t)aa[m];
    u32x4 pv;
#pragma unroll
    for (int q = 0; q < 4; ++q) {
        f32x4 d = {0.f, 0.f, 0.f, 0.f};
#pragma unroll
        for (int m = 0; m < 4; ++m) d = __builtin_amdgcn_mfma_f32_16x16x32_bf16(af[m], wf[4 * q + m], d, 0, 0, 0);
        const float e = d[0];
        pv[q] = min(__float_as_uint(e), SENT - 1u);
    }
    ST16_SC1(pv, rPt, pto);
}
__device__ __forceinline__ int wd_off(int lw, int i, int lane) {
    const int r = 2 * (i & 5) + (lane >> 5), ky = (r & 3) | ((r >> 1) & 4), c = lane & 31;
    return (lane >> 5) * (D_LEN * 2) + lw * 512 + (((c >> 1) ^ ky) * 2 + (c & 1)) * 16;
}

__global__ __launch_bounds__(768, 3) void fused_kernel(const Args a) {
    extern __shared__ __attribute__((aligned(16))) unsigned char smem[];
    constexpr int K = S_LEN, NT1 = D_LEN / BK;
    const int tid = threadIdx.x, wid = __builtin_amdgcn_readfirstlane(tid >> 6), lane = tid & 63;
    const int xcd = blockIdx.x & 7, idx = blockIdx.x >> 3;
    TSV;
    LAS unsigned char* lds = (LAS unsigned char*)smem;
    if (idx < CODE_WG && wid < 4) dma_word<false>(code_rsrc(), (unsigned)(size_t)lds + L_DUMMY, tid * 128);
    SB();
    const int h = xcd & 1, xq = xcd >> 1, rt = xq * 4 + (idx & 3), ct = idx >> 2;
    volatile LAS unsigned* ctl1 = (volatile LAS unsigned*)(lds + L_CTL);
#define NBR(k) ((rt + (k)) & 3)
    unsigned kx;
    Ring ring;
    if (__builtin_expect((wid & 12) == 4, 1)) {
        __builtin_amdgcn_s_setprio(3);
        ring.template setup<D_LEN, false>(wid - 4, lane);
        ring.lb = (wid - 4) * 4096;
        auto ord1 = [&](int i) {
            const int q = (i - idx - (idx >> 2)) & 3;
            return ((xq * 4 + h * 8 + i + 4 - (q == 0 ? 0 : q == 2 ? 4 : 2)) & 15) * 128;
        };
        ring.poff = 0;
        ring.sp = ring.sq = ord1(0);
        unsigned ox = (unsigned)(rt * 4 + wid - 4) * (32 * D_LEN * 2), ow = (unsigned)(ct * 4 + wid - 4) * (32 * D_LEN * 2);
        asm volatile("" : "+s"(ring.sq), "+s"(ring.lb), "+s"(ox), "+s"(ow), "+v"(ring.voQ[0]), "+v"(ring.voQ[1]), "+v"(ring.voQ[2]), "+v"(ring.voQ[3]));
        SB();
        ring.sp = ring.sq;
#pragma unroll
        for (int j = 0; j < 4; ++j) ring.voP[j] = ring.voQ[j];
        ring.rP = mk_rsrc((const char*)a.x + ox, 32 * D_LEN * 2);
        ring.rQ = mk_rsrc((const char*)(h == 0 ? a.wre : a.wim) + ow, 32 * D_LEN * 2);
        ring.template issue<false>(lds);
        __builtin_amdgcn_s_setprio(0);
        ring.tq = ord1(lane);
        ring.ti = 0;
        ring.next1();
        ring.par = (ct & 4) << 11;
        ring.template ramp<CATCH1>(lds);
        if (BUBBLE) __builtin_amdgcn_s_sleep(BUBBLE);
        ring.template steady<false, false>(NT1 - 2 - CATCH1);
        ring.template setup<K, true>(wid - 4, lane);
        ring.rQ = mk_rsrc((h == 0 ? a.cr : a.ci) + (size_t)(rt * 4 + wid - 4) * 32 * K, 32 * K * 2);
        ring.seek(rt * 256, 0);
        LAS unsigned char* l0 = lds + (wid - 4) * 4096;
        ring.issueQ(l0); ring.step();
        WAIT_VM(12); BARRIER();
        ring.issueQ(l0 + STAGE_BYTES);
        ring.seek((xq * 4 + NBR(1)) * 256, 0);
        WAIT_VM(8); BARRIER();
        ring.issueQ(l0 + 2 * STAGE_BYTES);
        ring.seek((xq * 4 + NBR(2)) * 256, 0);
        BARRIER();
        ring.issueQ(l0 + 3 * STAGE_BYTES);
        kx = (unsigned)__builtin_amdgcn_readfirstlane((int)ctl1[4]);
        SB();
    }
    char* ws = a.ws + ((0ull - (unsigned long long)a.ws) & 0x1fffffull);
    const int g = lane >> 4, n = lane & 15;
    LAS float* tab = (LAS float*)(lds + L_RED);
    if (wid >= 8) {
        const int aw = wid - 8;
        GAS u64* tagp = (GAS u64*)(ws + WS_TAG);
        if (AUX_SLEEP) __builtin_amdgcn_s_sleep(AUX_SLEEP);
        u64 tag0 = __hip_atomic_load(tagp, __ATOMIC_RELAXED, __HIP_MEMORY_SCOPE_AGENT);
        SB();
        const int rho = (aw < 2 ? ct : (xq & 1) * 4 + (idx & 3)) * 16 + (aw & 1) * 8 + (n >> 1);
        unsigned fa = (unsigned)(size_t)lds + (aw >> 1) * TILE_BYTES + rho * 128 + ((((n & 1) * 4 + g) ^ ((rho >> 1) & 7)) * 16);
        constexpr int NIL = 1 << 30;
        const int at = tid - 512;
        __amdgpu_buffer_rsrc_t rZ = mk_rsrc(ws, 18u << 20);
        int z0 = (int)WS_STATS + blockIdx.x * (int)(FILL_BYTES / 256) + at * 16;
        int z1 = aw < 2 ? (at < (int)(FILL_BYTES / 4096) - 256 ? z0 + 4096 : NIL) : aw == 2 ? (lane < 10 ? blockIdx.x * 160 + lane * 16 : NIL)
               : lane < 32 ? (int)WS_FLAG1 + (lane >> 4) * 65536 + h * 32768 + (rt >> 3) * 16384 + ((lane >> 3) & 1) * 8192 + ct * 1024 + (rt & 7) * 128 + (lane & 7) * 16
               : NIL;
        u32x4 zv = {SENT, SENT, SENT, SENT};
        asm volatile("" : "+v"(fa), "+v"(z0), "+v"(z1), "+v"(zv), "+s"(rZ));
        f32x4 d1 = {0.f, 0.f, 0.f, 0.f}, d2 = {0.f, 0.f, 0.f, 0.f};
        u32x4 ones = {0x3F803F80u, 0x3F803F80u, 0x3F803F80u, 0x3F803F80u};
        asm volatile("" : "+v"(ones), "+v"(d1), "+v"(d2));
        if (aw == 0) {
            const int j = lane < 4 ? ct * 4 + lane : (idx & 3) * 8 + lane - 4;
            dma_word<true>(rZ, (unsigned)(size_t)lds + L_DUMMY, lane >= 12 ? NIL : (int)WS_XRD + (j >> 4) * 65536 + (lane < 4 ? rt : 16 + h * 8 + ct) * 2048 + (j & 15) * 128);
        }
        const char* cb;
        __amdgpu_buffer_rsrc_t rR, rS, rV;
        unsigned mask, tb, wl, wh;
        int ti, ro, o0, o1;
        auto warm = [&](int t) { if (t >= 4 && ((mask >> t) & 1)) { touch_line(cb + (ti & 7) * 512, L_DUMMY); ti += 2; } };
        u32x4 w0v, w1v;
        float wsr = 0.f;
#pragma unroll
        for (int t = 0; t < 8; ++t) {
            SB();
            BARRIER(); SB();
            const bf16x8 f = *(const LAS bf16x8*)((const LAS unsigned char*)(size_t)fa + (t & 1) * STAGE_BYTES);
            SB();
            if (aw == 1) { STAMP(40 + t); if (!(t & 1)) CSTAMP(57 + t / 2); }
            if (t == 0) {
                rR = mk_rsrc(a.w_r, aw == 3 && h == 0 ? D_LEN * 2 : 0);
                rS = mk_rsrc(ws + WS_XRD, 131072);
                rV = mk_rsrc(ws + WS_VOTE, 512);
                asm volatile("" : "+s"(rR), "+s"(rS), "+s"(rV));
            }
            if (t == 1) {
                cb = (const char*)(h ? a.ci : a.cr) + (size_t)rt * (512u << 10) + ct * 65536 + (lane >> 2) * 4096 + (lane & 3) * 128;
                mask = aw == 1 ? CPF_MASK1 : aw == 2 ? CPF_MASK2 : 0u;
                mask = (mask & CPF_FIX) | ((mask & ~CPF_FIX) << (idx % CPF_STAG));
                ti = 2 * xq + aw - 1;
                asm volatile("" : "+v"(cb), "+s"(mask), "+s"(ti));
            }
            if (t == 2) {
                w0v = __builtin_amdgcn_raw_buffer_load_b128(rR, lane * 16, 0, 0); w1v = __builtin_amdgcn_raw_buffer_load_b128(rR, lane * 16, 1024, 0);
                ro = (n | 2) != g * 4 + 2 ? 131072 : (aw < 2 ? h * 65536 + rt * 2048 : (xq >> 1) * 65536 + (16 + h * 8 + ct) * 2048) + rho * 16;
                asm volatile("" : "+v"(ro));
            }
            if (t == 3) {
                const int hi = aw == 3 ? 64 : 0;
                o0 = rt * 2048 + (hi + lane) * 16;
                o1 = o0 + (16 + h * 8 + ct - rt) * 2048;
                tb = (unsigned)(size_t)(tab + prow(hi + lane));
                asm volatile("" : "+v"(o0), "+v"(o1), "+v"(tb));
            }
            if (t == RESET_T) ST16_SC1(zv, rZ, z0);
            if (t == RESET_U) { if (aw >= 2) zv = (u32x4){0u, 0u, 0u, 0u}; ST16_SC1(zv, rZ, z1); }
            warm(t);
            if (t & 1) fa ^= 2 * STAGE_BYTES;
            d1 = __builtin_amdgcn_mfma_f32_16x16x32_bf16(f, __builtin_bit_cast(bf16x8, ones), d1, 0, 0, 0);
            d2 = __builtin_amdgcn_mfma_f32_16x16x32_bf16(f, f, d2, 0, 0, 0);
            if (t == 5) {
#pragma unroll
                for (int i = 0; i < 4; ++i) { wsr = dot2bf(w0v[i], 0x3F803F80u, wsr); wsr = dot2bf(w1v[i], 0x3F803F80u, wsr); }
                asm volatile("" : "+v"(wsr));
            }
            if (t == 1) {
                asm volatile("" : "+v"(tag0));
                const u64 want = tag0 * 6364136223846793005ull + 1442695040888963407ull;
                wl = (unsigned)want; wh = (unsigned)(want >> 32);
                asm volatile("" : "+v"(wl), "+v"(wh));
            }
            if (t == VOTE_T && aw == 3 && idx < 8) __builtin_amdgcn_raw_buffer_store_b64((u32x2){ctl1[5], wl}, rV, blockIdx.x * 8 + lane * 512, 0, 16);
            if (t == 6) WAIT_VM0();
            asm volatile("" : "+v"(d1), "+v"(d2), "+v"(fa));
        }
        SB(); BARRIER(); SB();
        {
            const float sa = d1[0] + d1[1], sb = d1[2] + d1[3];
            float sq = sel4(n, d2[0], d2[1], d2[2], d2[3]);
            sq += dppf<0xB1>(sq);
            const u32x4 rec = {__float_as_uint((n & 2) ? sb : sa), wl, __float_as_uint(sq), wh};
            ST16_SC1(rec, rS, ro);
        }
        if (aw == 1) { STAMP(48); CSTAMP(61); }
        if (aw == 1 || aw == 2) {
            warm(8);
#pragma unroll 1
            for (int t = 9; t <= NT1 + AUX_STAY; ++t) {
                BARRIER();
                if (aw == 1 && t <= 16) { STAMP(40 + t); if (!(t & 1)) CSTAMP(t <= 12 ? 57 + t / 2 : 25 + t / 2); }
                warm(t);
            }
            FLUSH();
            return;
        }
        SB(); BARRIER(); SB();
        if (aw == 3) {
            wsr = wave_sum(wsr);
            if (lane == 0) ctl1[1] = __float_as_uint(wsr);
        } else {
            if (lane == 0) { ctl1[0] = wl | 2u; ctl1[2] = wl; ctl1[3] = wh; }
            *(LAS u32x2*)(lds + L_YB + 12288 + 256 + lane * 8) = (u32x2){0u, 0u};
        }
#pragma unroll
        for (int t = 10; t <= POLL_T; ++t) BARRIER();
        SB();
        u32x4 q0, q1, q2, q3;
        u32x2 qv;
        auto request = [&]() {
            asm volatile("" ::: "memory");
            q0 = LD16_SC1(rS, o0); q1 = LD16_SC1(rS, o0 + 65536); q2 = LD16_SC1(rS, o1); q3 = LD16_SC1(rS, o1 + 65536);
            qv = __builtin_amdgcn_raw_buffer_load_b64(rV, lane * 8, 0, 16);
            SB();
        };
        if (FIN_PRIO) __builtin_amdgcn_s_setprio(FIN_PRIO);
        request();
        int b = POLL_T + 1;
        for (int spin = 0; spin <= SPIN_CAP; ++spin) {
            if (b < NT1) { BARRIER(); ++b; }
            SB();
            unsigned d = ((q0[1] ^ wl) | (q1[1] ^ wl)) | ((q2[1] ^ wl) | (q3[1] ^ wl)) | ((q0[3] ^ wh) | (q1[3] ^ wh)) | ((q2[3] ^ wh) | (q3[3] ^ wh)) | (qv[1] ^ wl);
            asm volatile("" : "+v"(d));
            if (__all(d == 0u)) break;
            request();
        }
        {
            const unsigned a0 = q0[0], a1 = q1[0], b0 = q0[2], b1 = q1[2], c0 = q2[0], c1_ = q3[0];
            const float mean = (__uint_as_float(a0) + __uint_as_float(a1)) * (1.0f / D_LEN);
            const float var = fmaxf((__uint_as_float(b0) + __uint_as_float(b1)) * (1.0f / D_LEN) - mean * mean, 0.f);
            float rden = __builtin_amdgcn_rcpf(rbf(__builtin_amdgcn_sqrtf(rbf(rbf(var) + 1e-5f))));
            if (h != 0) rden = -rden;
            float mr = rbf(mean) * rden;
            float wv = __uint_as_float(c0) + __uint_as_float(c1_);
            const unsigned vt = qv[0];
            unsigned kv = __popcll(__ballot(vt == 2u)) > __popcll(__ballot(vt == 1u)) ? 8192u : 0u;
            asm volatile("" : "+v"(rden), "+v"(mr), "+v"(wv), "+v"(kv));
            SB();
            if (b < NT1) BARRIER();
            SB();
            LAS float* tp = (LAS float*)(size_t)tb;
            tp[0] = rden; tp[128] = mr; tp[256] = wv;
            ctl1[4] = kv;
        }
        WAIT_LGKM0();
        return;
    }
#define CTL(i) ((unsigned)__builtin_amdgcn_readfirstlane((int)ctl[i]))
    const int wp = wid & 1, wq = (wid >> 1) & 1;
#define Y_OFF ((int)WS_Y + ((rt & 7) * 8 + ct) * 65536 + (wid & 3) * 16384 + (((rt >> 3) ^ (rt >> 2)) & 1) * 8192)
#define P2_DEFS \
    char* w2 = ws + kx; \
    const int sidx = idx < 28 ? xq * 28 + idx : 112 + xq * 4 + idx - 28;     \
    volatile LAS unsigned* ctl = (volatile LAS unsigned*)(lds + (h == 0 ? L_ATAB : L_CTL)); \
    const int NT = h == 0 ? 32 + KSHIFT : 32 - KSHIFT; \
    LAS float* rstat = (LAS float*)(lds + L_ATAB + 256);     \
    const int hp_ = rt * 2 + wq;                             \
    (void)sidx; (void)ctl; (void)NT; (void)rstat; (void)hp_; (void)w2
    if (__builtin_expect(wid < 4, 1)) {
        if (wid == 3 && idx < 8) {
            const unsigned r = (((unsigned)__builtin_amdgcn_s_memrealtime() * 2654435761u + blockIdx.x * 40503u) >> 9) & 0x1fdf80u;
            const unsigned first = (idx & 1) << 13;
            const char* pb = ws + WS_PROBE;
            unsigned va, vb, left;
            asm volatile("s_waitcnt lgkmcnt(0)\n\ts_load_dword %0, %3, %4\n\ts_load_dword %1, %3, %5\n\ts_waitcnt lgkmcnt(1)\n\t"
                         "s_nop 15\n\ts_getreg_b32 %2, hwreg(HW_REG_IB_STS, 8, 4)\n\ts_waitcnt lgkmcnt(0)"
                         : "=&s"(va), "=&s"(vb), "=&s"(left) : "s"(pb), "s"(r | first), "s"(r | (first ^ 8192u)) : "memory");
            const unsigned nearbit = (left != 0u ? first : first ^ 8192u) >> 13;
            if (lane == 0) ctl1[5] = nearbit == (unsigned)(xcd >> 2) ? 1u : 2u;
        }
        f32x4 acc[4][4];
#pragma unroll
        for (int i = 0; i < 16; ++i) acc[i >> 2][i & 3] = (f32x4){0.f, 0.f, 0.f, 0.f};
        unsigned tc = (unsigned)(size_t)(tab + wp * 64 + g * 8), tw = (unsigned)(size_t)(tab + 256 + prow(wq * 64 + n));
        unsigned ta = (unsigned)(size_t)lds + wp * STAGE_BYTES + wq * 8192 + lane * 16;
        constexpr int NIL = 1 << 30;
        __amdgpu_buffer_rsrc_t rW = mk_rsrc(ws, 18u << 20);
        const int hpan = rt * 2 + wq;
        int sk = (int)WS_AT + at_panel(h * 8 + ct) + at_tile((rt * 2 + wp) * 128, (ct & 4) << 11) + wq * 16384 + lane * 16;
        int st = lane != 0 ? NIL : (int)WS_FLAG1 + (1 - wp) * 65536 + h * 32768 + (rt >> 3) * 24576 + ct * 1024 + (rt & 7) * 128 + wq * 4;
        int so = h != 0 ? Y_OFF + lane * 16 : g != (n >> 2) ? NIL
               : (int)WS_STATS + (hpan & 15) * 32768 + (ct >> 2) * 16384 + (hpan >> 4) * 8192 + ((ct & 3) * 2 + wp) * 1024 + n * 16;
        int sf = lane != 0 ? NIL : (int)WS_FLAGS + (rt * 8 + ct) * 128 + wid * 4;
        u32x4 sel[2], aw[2];
        unsigned one[2];
#pragma unroll
        for (int i = 0; i < 4; ++i) {
            one[i & 1] = g == (n >> 2) && (i & 1) == ((n >> 1) & 1) ? ((n & 1) ? 0x3F800000u : 0x00003F80u) : 0u;
            aw[0][i] = aw[1][i] = (n & 3) == 0 ? 0x3F803F80u : 0u;
        }
        if ((n & 3) == 1) {
            const bf16_t* wr = a.w_r + ct * BT + wp * 64 + g * 8;
            aw[0] = *(const u32x4*)wr; aw[1] = *(const u32x4*)(wr + 32);
        }
        unsigned ea[2];
#pragma unroll
        for (int mp = 0; mp < 2; ++mp)
            ea[mp] = (unsigned)(size_t)lds + L_YB + wq * 16384 + (h == 0 ? wp * 2048 + mp * 1024 + lane * 16 : n * 256 + (((wp * 8 + mp * 4 + g) ^ n) * 16));
        asm volatile("" : "+v"(tc), "+v"(tw), "+v"(ta), "+v"(sk), "+v"(st), "+v"(so), "+v"(sf), "+v"(one[0]), "+v"(one[1]), "+v"(ea[0]), "+v"(ea[1]));
        asm volatile("" : "+s"(rW));
        unsigned tg;
        if (wid == 0) STAMP(0);
        compute_loop<false, true>(lds, acc, NT1, wp, wq, lane, [&]() { if (wid == 0) STAMP(24); if (wid == 0) STAMP(31); });
        if (wid == 0) STAMP(25);
        __builtin_amdgcn_s_waitcnt(0x0F70);
        {
            f32x4 rd[4], mr[4];
            float wsm[4];
#pragma unroll
            for (int m = 0; m < 4; ++m) {
                rd[m] = *(const LAS f32x4*)((const LAS float*)(size_t)tc + (m >> 1) * 32 + (m & 1) * 4);
                mr[m] = *(const LAS f32x4*)((const LAS float*)(size_t)tc + 128 + (m >> 1) * 32 + (m & 1) * 4);
                wsm[m] = *((const LAS float*)(size_t)tw + (m >> 1) * 32 + (m & 1) * 4);
            }
            const unsigned kv = ctl1[4];
            tg = ctl1[0];
            SB();
            sk += (int)kv; st ^= (int)kv; so += (int)kv; sf += (int)kv;
            kx = (unsigned)__builtin_amdgcn_readfirstlane((int)kv);
#pragma unroll
            for (int nn = 0; nn < 4; ++nn)
#pragma unroll
                for (int mp = 0; mp < 2; ++mp) {
                    const f32x4 u = acc[2 * mp][nn] * rd[2 * mp] - mr[2 * mp] * wsm[nn];
                    const f32x4 v = acc[2 * mp + 1][nn] * rd[2 * mp + 1] - mr[2 * mp + 1] * wsm[nn];
                    u32x4 w;
                    w[0] = pk_bf16(u[0], u[1]); w[1] = pk_bf16(u[2], u[3]);
                    w[2] = pk_bf16(v[0], v[1]); w[3] = pk_bf16(v[2], v[3]);
                    ST16_SC1(w, rW, sk + (nn * 2 + mp) * 1024);
                    *(LAS u32x4*)((LAS unsigned char*)(size_t)ta + (nn * 2 + mp) * 1024) = w;
                }
        }
        if (wid == 0) STAMP(26);
        WAIT_LGKM0();
        BARRIER();
        SB();
        {
#pragma unroll
            for (int i = 0; i < 16; ++i) { acc[i >> 2][i & 3] = (f32x4){0.f, 0.f, 0.f, 0.f}; asm volatile("" : "+v"(acc[i >> 2][i & 3])); }
            sel[0] = (u32x4){one[0], one[1], 0u, 0u}; sel[1] = (u32x4){0u, 0u, one[0], one[1]};
            asm volatile("" : "+v"(st), "+v"(so), "+v"(sf), "+v"(tg), "+v"(sel[0]), "+v"(sel[1]));
            SB();
            WAIT_VM(0);
            __builtin_amdgcn_raw_buffer_store_b32(tg, rW, st, 0, 16);
            SB();
            if (wid == 0) STAMP(27);
        }
        compute_loop<true, false>(lds, acc, h == 0 ? 32 + KSHIFT : 32 - KSHIFT, wp, wq, lane, [&]() {
            if (wid == 0) { STAMP(h == 0 ? 1 : 19); CSTAMP(22); }
        });
        P2_DEFS;
        if (wid == 0) { STAMP(h == 0 ? 2 : 12); CSTAMP(23); }
        u32x4 hq[4][2];
        {
            bf16x8 ef[4][2];
#pragma unroll
            for (int i = 0; i < 8; ++i) ef[i >> 1][i & 1] = *(const LAS bf16x8*)((const LAS unsigned char*)(size_t)ea[i & 1] + (i >> 1) * 4096);
            SB();
#pragma unroll
            for (int i = 0; i < 16; ++i)
                acc[i & 3][i >> 2] = __builtin_amdgcn_mfma_f32_16x16x32_bf16(__builtin_bit_cast(bf16x8, sel[i & 1]), ef[i >> 2][(i >> 1) & 1], acc[i & 3][i >> 2], 0, 0, 0);
#pragma unroll
            for (int nn = 0; nn < 4; ++nn)
#pragma unroll
                for (int mp = 0; mp < 2; ++mp)
#pragma unroll
                    for (int i = 0; i < 4; ++i) {
                        const f32x4 c = acc[2 * mp + (i >> 1)][nn];
                        hq[nn][mp][i] = pk_bf16(c[(i & 1) * 2], c[(i & 1) * 2 + 1]);
                    }
        }
        if (__builtin_expect(h == 0, 1)) {
            f32x4 d1[4], d2[4];
#pragma unroll
            for (int nn = 0; nn < 4; ++nn) {
                d1[nn] = d2[nn] = (f32x4){0.f, 0.f, 0.f, 0.f};
#pragma unroll
                for (int mp = 0; mp < 2; ++mp) {
                    const bf16x8 hf = __builtin_bit_cast(bf16x8, hq[nn][mp]);
                    d1[nn] = __builtin_amdgcn_mfma_f32_16x16x32_bf16(__builtin_bit_cast(bf16x8, aw[mp]), hf, d1[nn], 0, 0, 0);
                    d2[nn] = __builtin_amdgcn_mfma_f32_16x16x32_bf16(hf, hf, d2[nn], 0, 0, 0);
                }
            }
#pragma unroll
            for (int nn = 0; nn < 4; ++nn) {
                u32x3 sv;
                sv[0] = min(__float_as_uint(d1[nn][0]), SENT - 1u);
                sv[1] = min(__float_as_uint(sel4(n, d2[nn][0], d2[nn][1], d2[nn][2], d2[nn][3])), SENT - 1u);
                sv[2] = min(__float_as_uint(d1[nn][1]), SENT - 1u);
                __builtin_amdgcn_raw_buffer_store_b96(sv, rW, so + nn * 256, 0, 16);
            }
            if (wid == 0) STAMP(3);
#pragma unroll
            for (int nn = 0; nn < 4; ++nn)
#pragma unroll
                for (int mp = 0; mp < 2; ++mp)
                    *(LAS u32x4*)(lds + L_HT + (wq * 64 + nn * 16 + n) * 256 + (((wp * 8 + mp * 4 + g) ^ n) * 16)) = hq[nn][mp];
            WAIT_LGKM0();
            BARRIER();
            BARRIER();
            if (wid == 0) STAMP(6);
            const int crow = (int)CTL(2 + wq) + wq * 64;
            const __amdgpu_buffer_rsrc_t rOut = mk_rsrc(a.out + (size_t)rt * BT * D_LEN, BT * D_LEN * 2);
#pragma unroll 2
            for (int j = 0; j < 8; ++j) {
                const int r = wq * 64 + wp * 32 + j * 4 + g;
                const float m_ = rstat[r * 2], d_ = rstat[r * 2 + 1];
                const u32x4 hh = *(const LAS u32x4*)(lds + L_HT + r * 256 + ((n ^ (r & 15)) * 16));
                u32x4 ov;
#pragma unroll
                for (int i = 0; i < 4; ++i) {
                    const unsigned u = hh[i];
                    const float h0 = bflo(u), h1 = bfhi(u);
                    const unsigned dd = pk_bf16(h0 - m_, h1 - m_);
                    const unsigned q = pk_bf16(bflo(dd) * d_, bfhi(dd) * d_);
                    ov[i] = pk_bf16(h0 + bflo(q), h1 + bfhi(q));
                }
                __builtin_amdgcn_raw_buffer_store_b128(ov, rOut, r == crow ? BT * D_LEN * 2 : (r * D_LEN + ct * BT + n * 8) * 2, 0, OUT_AUX);
            }
            FLUSH();
            return;
        }
#pragma unroll
        for (int i = 0; i < 8; ++i) __builtin_amdgcn_raw_buffer_store_b128(hq[i >> 1][i & 1], rW, so, i * 1024, 16);
        WAIT_VM0();
        __builtin_amdgcn_raw_buffer_store_b32(1u, rW, sf, 0, 16);
        if (wid == 0) STAMP(13);
    } else {
        LAS unsigned char* l0 = lds + (wid - 4) * 4096;
        P2_DEFS;
        const __amdgpu_buffer_rsrc_t rD = mk_rsrc(a.wd + (size_t)sidx * 32 * D_LEN, h == 0 && sidx < 112 && wid < 8 - BQ ? 32 * D_LEN * 2 : 0);
        auto load_wd = [&](int i) {
            dma_nt(rD, (unsigned)(size_t)lds + (i < 8 ? L_WD0 : L_WD1) + (wid & 3) * 8192 + (i & 7) * 1024, wd_off(wid & 3, i, lane), i * (4 * D_LEN));
        };
        const int lw = wid - 4;
        constexpr int NIL = 1 << 30;
        ring.rP = ring.rF = mk_rsrc(w2, (unsigned)WS_STATS);
        ring.voP[0] += (int)WS_AT + at_panel(h * 8 + ct);
        ring.voP[1] = ring.voP[2] = ring.voP[3] = ring.voP[0];
#define FLINE(k) (((k) >> 3) * (24576 - 2 * (int)kx) + ((k) & 7) * 128)
        const int ft = (g & 1) * 65536 + (g >> 1) * 4 + (int)WS_FLAG1 + (lw == 0 ? ct * 1024 : NIL);
        int vall = FLINE(n) + ft + h * 32768;
        ring.voF = (n >> 2) == xq ? vall : NIL;
        ring.fl = lds + L_YB + wq * 16384 + 12288 + wp * 2048 + 256;
        unsigned sa = (unsigned)(size_t)lds + L_YB + 12288 + 256 + lane * 4;
        const unsigned tg = (unsigned)__builtin_amdgcn_readfirstlane((int)ctl1[0]);
        float wsum = __uint_as_float(ctl1[1]);
        ring.lb = lw * 4096;
        int cq[3], cp[3];
        unsigned mk[3];
#pragma unroll
        for (int k = 0; k < 3; ++k) {
            const int nb = xq * 4 + NBR(k + 1);
            cq[k] = nb * 256; cp[k] = at_tile(cq[k], ring.par); mk[k] = 0x10000u << nb;
            asm volatile("" : "+s"(cq[k]), "+s"(cp[k]), "+s"(mk[k]));
        }
        unsigned m2 = (mk[0] | mk[1] | mk[2]) >> 16;
        asm volatile("" : "+s"(m2), "+s"(ring.rP));
        asm volatile("" : "+v"(vall), "+v"(ring.voF), "+v"(ring.voP[0]), "+v"(sa), "+v"(wsum));
        ring.rF = ring.rP;
        ring.voP[1] = ring.voP[2] = ring.voP[3] = ring.voP[0];
        WAIT_VM(8);
        BARRIER();
        SB();
        __amdgpu_buffer_rsrc_t rE = mk_rsrc(h == 0 ? (const void*)w2 : (const void*)a.x, (unsigned)WS_STATS);
        int ve[2];
        auto fetch = [&]() {
            if (wid == 4 && h == 0) STAMP(21);
            LAS unsigned char* base = lds + L_YB + wq * 16384 + wp * 2048;
            const int s2 = h == 0 ? 2048 : 16 * D_LEN * 2;
#pragma unroll
            for (int k = 0; k < 4; ++k) dma_2k(rE, (unsigned)(size_t)base + k * 4096, ve[0], ve[1], k * s2);
        };
#pragma unroll
        for (int j = 0; j < 2; ++j)
            ve[j] = h == 0 ? Y_OFF + lane * 16 : ((rt * BT + wq * 64 + wp * 8 + j * 4 + g) * D_LEN + ct * BT + ((n ^ (wp * 8 + j * 4 + g)) * 8)) * 2 - j * 1024;
        __amdgpu_buffer_rsrc_t rQ2 = mk_rsrc(a.ci + (size_t)(rt * 4 + lw) * 32 * K, 32 * K * 2);
        int vC2 = (ct * 4 + g) * (K * 2) + ((xq * 1024 + 2048 + n * 128) & 4095);
        int vT2 = h == 0 && n < (KSHIFT + 1) / 2 ? FLINE((xq * 4 - 1 - n) & 15) + ft + 32768 : NIL;
        int vY = lane == 0 ? (int)WS_FLAGS + (rt * 8 + ct) * 128 + lw * 4 : NIL;
        int n0 = C2_START + idx % C2_STAG, n1 = (h == 0 ? 32 : NT) - 11 - n0;
        int c8 = (xq * 1024 + 1024) & 4095, p8 = at_tile(c8, ring.par);
        int cK = (xq * 1024 + (32 - KSHIFT) * 128) & 4095, pK = at_tile(cK, ring.par) + at_panel(8);
        asm volatile("" : "+s"(rE), "+s"(rQ2), "+s"(n0), "+s"(n1), "+s"(c8), "+s"(p8), "+s"(cK), "+s"(pK));
        asm volatile("" : "+v"(ve[0]), "+v"(ve[1]), "+v"(vC2), "+v"(vT2), "+v"(vY));
        unsigned have = 0u;
        SB();
        if (T_DELAY) __builtin_amdgcn_s_sleep(T_DELAY);
#define GATE(m) do { if (__builtin_expect(((m) & ~have) != 0u, 0)) ring.gate(tg, sa, m, have); SB(); } while (0)
#define TILE(k, o, st) do { ring.sp = cp[k] + (o) * 256; ring.sq = cq[k] + (o); ring.template issueP<16>(l0 + (st) * STAGE_BYTES); \
                            ring.issueQ(l0 + (st) * STAGE_BYTES); ring.sample(); SB(); } while (0)
        ring.gate(tg, sa, mk[0], have);
        if (wid == 4) STAMP(28);
        ring.sp = cp[0]; ring.template issueP<16>(l0 + 2 * STAGE_BYTES); ring.sample();
        BARRIER();
        GATE(mk[1]);
        ring.sp = cp[1]; ring.template issueP<16>(l0 + 3 * STAGE_BYTES); ring.sample();
        WAIT_VM(6); BARRIER();
        if (wid == 4) STAMP(30);
        GATE(mk[2]);
        TILE(2, 0, 0);
        GATE(m2);
        ring.voF = vall;
        TILE(0, 128, 1);
        WAIT_VM(19); BARRIER();
        TILE(1, 128, 2);
        WAIT_VM(19); BARRIER();
        TILE(2, 128, 3);
        WAIT_VM(19); BARRIER();
#undef GATE
#undef TILE
        if (wid == 4) STAMP(29);
        if (have != 0xffffffffu) ring.gate(tg, sa, 0xffffffffu, have);
        ring.fl -= 256;
        ring.sq = c8; ring.sp = p8; ring.poff = 0;
        if (n0 > 0) ring.template steady<true, false>(n0);
        ring.voF = vC2;
        ring.rF = ring.rQ;
        ring.template issue<true>(lds);
        if (__builtin_expect(blockIdx.x == 255, 0) && tid == 256) {
            const u64 want = ((u64)ctl1[3] << 32) | ctl1[2];
            __hip_atomic_store((GAS u64*)(ws + WS_TAG), want, __ATOMIC_RELAXED, __HIP_MEMORY_SCOPE_AGENT);
        }
        ring.voF = vT2;
        ring.rF = ring.rP;
        ring.fl += 512;
        ring.sample();
        WAIT_VM(18); BARRIER();
        if (wid == 4) { CSTAMP(34); STAMP(36); }
        ring.template steady<true, false, 18>(2);
        ring.template steady<true, false>(n1);
        if (wid == 4) { CSTAMP(35); STAMP(37); }
        if (h == 0) {
            have = 0u;
            ring.gate(tg, sa + 256, ((1u << ((KSHIFT + 1) / 2)) - 1u) * 0x10001u, have);
            ring.voF = vY;
            ring.fl -= 512;
            ring.rQ = rQ2;
            ring.sq = cK; ring.sp = pK; ring.poff = at_panel(8);
            ring.template steady<true, true>(KSHIFT);
        }
        if (h != 0 || ring.seen()) {
            fetch();
            WAIT_VM(16); BARRIER();
            WAIT_VM(8); BARRIER();
        } else {
            ring.sample();
            WAIT_VM(10); BARRIER();
            const bool have = ring.seen();
            if (have) { fetch(); WAIT_VM(9); } else { ring.sample(); WAIT_VM(2); }
            BARRIER();
            if (!have) {
#pragma unroll 1
                for (int spin = 0; spin <= SPIN_CAP && !ring.seen(); ++spin) { ring.sample(); WAIT_VM(0); }
                fetch();
            }
        }
        if (wid == 4 && h == 0) STAMP(20);
        WAIT_VM0();
        BARRIER();
        if (h == 0) {
            const int slot = sidx;
            LAS unsigned char* abuf = lds + L_ATAB + 2048 + lw * 128;
#pragma unroll
            for (int i = 0; i < WD_EARLY; ++i) load_wd(i);
            SB();
            BARRIER();
            __builtin_amdgcn_s_setprio(TAIL_PRIO);
            if (lane < 16) *(LAS unsigned*)(abuf + 64 + lane * 4) = 0u;
            __builtin_amdgcn_s_sleep(STAT_DELAY);
            if (wp == 0) {
                const __amdgpu_buffer_rsrc_t rS = mk_rsrc(w2 + WS_STATS + (hp_ & 15) * 32768 + (hp_ >> 4) * 8192, 24576);
                __amdgpu_buffer_rsrc_t rC = mk_rsrc(w2 + WS_CAND + (size_t)hp_ * 4096, 4096);
                __amdgpu_buffer_rsrc_t rKy = mk_rsrc(w2 + WS_KEYS + hp_ * 4, ct == 0 ? 8192 + 3 * 256 + 4 : 0);
                asm volatile("" : "+s"(rC), "+s"(rKy));
                float s1, s2, s3;
#pragma unroll 1
                for (int spin = 0;; ++spin) {
                    asm volatile("" ::: "memory");
                    u32x3 sv[16];
#pragma unroll
                    for (int j = 0; j < 16; ++j) sv[j] = __builtin_amdgcn_raw_buffer_load_b96(rS, lane * 16, (j >> 3) * 16384 + (j & 7) * 1024, 16);
                    SB();
                    s1 = 0.f; s2 = 0.f; s3 = 0.f;
#pragma unroll
                    for (int j = 0; j < 16; ++j) {
                        const unsigned u0 = sv[j][0], u1 = sv[j][1], u2 = sv[j][2];
                        s1 += __uint_as_float(u0); s2 += __uint_as_float(u1); s3 += __uint_as_float(u2);
                    }
                    const float z = (s1 - s1) + (s2 - s2) + (s3 - s3);
                    if (__builtin_expect(__all(z == 0.f), 1) || spin > SPIN_CAP) break;
                    unsigned mx = 0u;
#pragma unroll
                    for (int j = 0; j < 16; ++j) mx = max(max(mx, sv[j][0]), max(sv[j][1], sv[j][2]));
                    if (__all(mx != SENT)) break;
                }
                if (wid == 4) STAMP(4);
                const float mean = s1 * (1.0f / D_LEN);
                const float var = fmaxf(s2 * (1.0f / D_LEN) - mean * mean, 0.f);
                const float mb = rbf(mean), rden = __builtin_amdgcn_rcpf(rbf(__builtin_amdgcn_sqrtf(rbf(rbf(var) + 1e-5f))));
                const unsigned kb = wave_max_u32(r2key((s3 - mb * wsum) * rden, hp_ * 64 + lane));
                __builtin_amdgcn_raw_buffer_store_b32(kb, rKy, (lane >> 2) * 8192 + (lane & 3) * 256, 0, 16);
                {
                    const int cr_ = __builtin_amdgcn_readfirstlane((int)((0xffffu - (kb & 0xffffu)) & 63u));
                    const float m_ = __int_as_float(__builtin_amdgcn_readlane(__float_as_int(mb), cr_));
                    const float d_ = __int_as_float(__builtin_amdgcn_readlane(__float_as_int(rden), cr_));
                    u32x2 c = *(const LAS u32x2*)(lds + L_HT + (wq * 64 + cr_) * 256 + (((lane & 31) * 8) ^ ((cr_ & 15) * 16)));
#pragma unroll
                    for (int i = 0; i < 2; ++i) {
                        const unsigned u = c[i];
                        const unsigned dd = pk_bf16(bflo(u) - m_, bfhi(u) - m_);
                        const unsigned q = pk_bf16(bflo(dd) * d_, bfhi(dd) * d_);
                        c[i] = min(lane < 32 ? q : u, SENT - 1u);
                    }
                    __builtin_amdgcn_raw_buffer_store_b64(c, rC, (lane < 32 ? 2048 : 0) + ct * 256 + (lane & 31) * 8, 0, 16);
                }
                rstat[(wq * 64 + lane) * 2] = mb; rstat[(wq * 64 + lane) * 2 + 1] = rden;
                if (lane == 0) ctl[2 + wq] = (0xffffu - (kb & 0xffffu)) & 63u;
                WAIT_LGKM0();
                if (wid == 4) STAMP(5);
            }
            BARRIER();
#pragma unroll
            for (int i = WD_EARLY; i < 16; ++i) load_wd(i);
            if (slot < 112 && wid < 8 - BQ) {
                WAIT_VM0();
                if (wid == 4) STAMP(7);
                stage2(w2, slot, lw, lds + L_WD0 + lw * 8192, lds + L_WD1 + lw * 8192, abuf, lane);
                if (wid == 4) STAMP(11);
            }
            FLUSH();
            return;
        }
    }

    P2_DEFS;
    LAS float* red = (LAS float*)(lds + L_RED);
    if (sidx < 112) {
        const int slot = sidx, grp = slot >> 3, kq = slot & 7;
        const __amdgpu_buffer_rsrc_t rG = mk_rsrc(a.wg, D_LEN * F_LEN * 2), rU = mk_rsrc(a.wu, D_LEN * F_LEN * 2);
        auto pair = [&](int i) {
            const int d = wid * 16 + i * 2 + (lane >> 5), ky = (d & 3) | ((d >> 1) & 4), c = lane & 31;
            const int vgu = ((kq * 128 + d) * F_LEN + grp * 256 + (((c >> 1) ^ ky) * 16) + (c & 1) * 8) * 2;
            dma_nt(rG, (unsigned)(size_t)lds + wid * 8192 + i * 1024, vgu, 0);
            dma_nt(rU, (unsigned)(size_t)lds + 65536 + wid * 8192 + i * 1024, vgu ^ 512, 0);
        };
        if (wid >= 4) {
            __builtin_amdgcn_s_sleep(W_PRE_SL);
#pragma unroll
            for (int i = 0; i < W_PRE; ++i) pair(i);
        }
        BARRIER();
        if (wid < 4) {
#pragma unroll
            for (int i = 0; i < W_PRE; ++i) pair(i);
        }
#pragma unroll
        for (int i = W_PRE; i < 8; ++i) pair(i);
        if (BQ && wid == 7) {
            const __amdgpu_buffer_rsrc_t rD = mk_rsrc(a.wd + (size_t)slot * 32 * D_LEN, 32 * D_LEN * 2);
#pragma unroll
            for (int i = 0; i < 16; ++i)
                dma_nt(rD, (unsigned)(size_t)lds + L_YB + 16384 + i * 1024, wd_off(3, i, lane), i * (4 * D_LEN));
            if (lane < 16) *(LAS unsigned*)(lds + L_YB + 4096 + 64 + lane * 4) = 0u;
        }
        const __amdgpu_buffer_rsrc_t rK = mk_rsrc(w2, wid == 0 ? (unsigned)(WS_CAND + 32 * 4096) : 0u);
        const int ok_ = lane < 40 ? (int)WS_KEYS + (xcd >> 2) * 8192 + (xcd & 3) * 256 + (lane - 32) * 16 : (int)(WS_CAND + 32 * 4096);
        const int oc = (int)WS_CAND + 2048 + kq * 256 + n * 16;
        int best = 0;
        u32x4 v = {0u, 0u, 0u, 0u};
        auto request = [&]() {
            asm volatile("" ::: "memory");
            v = LD16_SC1(rK, lane < 32 ? oc + best * 4096 : ok_);
            SB();
        };
        auto settled = [&]() {
            const unsigned mx = umax4(v), mn = min(min(v[0], v[1]), min(v[2], v[3]));
            unsigned fv = lane < 32 ? ~mx : mn | (unsigned)(lane >> 3 ^ 4);
            asm volatile("" : "+v"(fv));
            const bool fine = fv != 0u;
            unsigned km = max(mx, dppu<0xB1>(mx));
            km = max(km, dppu<0x4E>(km));
            km = max(km, dppu<0x141>(km));
            const int nb = (int)(((0xffffu - ((unsigned)__builtin_amdgcn_readlane((int)km, 32) & 0xffffu)) >> 6) & 31u);
            const bool same = nb == best;
            best = nb;
            return __all(fine) && same;
        };
        if (wid == 0) {
            request();
            WAIT_VM(0);
            settled();
            request();
        } else if (BQ && wid == 7) WAIT_VM(16);
        else WAIT_VM(0);
        BARRIER();
        if (wid == 0) STAMP(8);
        bf16x8 wfr[4][4];
        {
            const int q = (lane >> 2) & 3, ky = q | ((g & 1) << 2);
#pragma unroll
            for (int u = 0; u < 4; ++u) {
                const int tro = (u >> 1) * 65536 + (8 * g + q) * 512 + (((wid * 2 + (u & 1)) ^ ky) * 32) + (lane & 3) * 8;
#pragma unroll
                for (int ks = 0; ks < 4; ++ks) {
                    const bf16x4 w0 = __builtin_amdgcn_ds_read_tr16_b64_v4i16((LAS bf16x4*)(lds + tro + ks * 16384));
                    const bf16x4 w1 = __builtin_amdgcn_ds_read_tr16_b64_v4i16((LAS bf16x4*)(lds + tro + ks * 16384 + 2048));
                    wfr[u][ks] = __builtin_shufflevector(w0, w1, 0, 1, 2, 3, 4, 5, 6, 7);
                }
            }
            SB();
#pragma unroll
            for (int i = 0; i < 16; ++i) asm volatile("" : "+v"(wfr[i >> 2][i & 3]));
        }
        const __amdgpu_buffer_rsrc_t rPo = mk_rsrc(w2 + WS_PRE, 112 * 2048);
        int po = n < 4 ? pre_off((grp ^ (n >> 1)) * 8 + wid) + kq * 256 + n * 64 + g * 16 : 112 * 2048;
        asm volatile("" : "+v"(po));
        if (wid == 0) {
#pragma unroll 1
            for (int spin = 0; spin <= SPIN_CAP && !settled(); ++spin) {
                __builtin_amdgcn_s_sleep(1);
                request();
            }
            __builtin_amdgcn_s_waitcnt(0x0F70);
            if (lane < 16) *(LAS u32x4*)(lds + L_RED + lane * 16) = v;
            WAIT_LGKM0();
            STAMP(9);
        }
        BARRIER();
        {
            f32x4 ac[4];
            bf16x8 xf[4];
#pragma unroll
            for (int ks = 0; ks < 4; ++ks) xf[ks] = *(const LAS bf16x8*)(lds + L_RED + ks * 64 + g * 16);
            SB();
#pragma unroll
            for (int u = 0; u < 4; ++u) ac[u] = (f32x4){0.f, 0.f, 0.f, 0.f};
#pragma unroll
            for (int ks = 0; ks < 4; ++ks)
#pragma unroll
                for (int u = 0; u < 4; ++u) ac[u] = __builtin_amdgcn_mfma_f32_16x16x32_bf16(wfr[u][ks], xf[ks], ac[u], 0, 0, 0);
            u32x4 sv;
#pragma unroll
            for (int r = 0; r < 4; ++r) sv[r] = min(__float_as_uint(sel4(n, ac[0][r], ac[1][r], ac[2][r], ac[3][r])), SENT - 1u);
            ST16_SC1(sv, rPo, po);
            SB();
            asm volatile("" :: "v"(v));
        }
        if (wid == 0) STAMP(14);
        if (BQ && wid == 7) {
            WAIT_VM0();
            stage2(w2, slot, 3, lds + L_YB + 16384, lds + L_YB + 24576, lds + L_YB + 4096, lane);
        }
    } else {
        const int rj = sidx - 112;
        if (wid == 4) {
            unsigned kk;
            const unsigned k = poll_winner(w2, xcd, lane, kk);
            if (lane == 0) ctl[4] = k;
            if (lane < 32) ctl[8 + lane] = kk;
            WAIT_LGKM0();
        }
        BARRIER();
        const unsigned kw = CTL(4);
        if (wid == 0) STAMP(16);
        {
            const unsigned kc = ctl[8 + wid * 4 + g];
            const __amdgpu_buffer_rsrc_t rL = mk_rsrc(w2 + WS_CAND + (size_t)wid * 16384, 16384);
            u32x2 hv2, nv2;
            for (int spin = 0;; ++spin) {
                asm volatile("" ::: "memory");
                hv2 = __builtin_amdgcn_raw_buffer_load_b64(rL, g * 4096 + (rj * 64 + n * 4) * 2, 0, 16);
                nv2 = __builtin_amdgcn_raw_buffer_load_b64(rL, g * 4096 + 2048 + (rj * 64 + n * 4) * 2, 0, 16);
                if (__all(max(max(hv2[0], hv2[1]), max(nv2[0], nv2[1])) != SENT) || spin > SPIN_CAP) break;
                __builtin_amdgcn_s_sleep(1);
            }
            u32x2 ov;
#pragma unroll
            for (int i = 0; i < 2; ++i) {
                const unsigned u = hv2[i], q = nv2[i];
                ov[i] = pk_bf16(bflo(u) + bflo(q), bfhi(u) + bfhi(q));
            }
            const int crow = (int)(0xffffu - (kc & 0xffffu)) & (S_LEN - 1);
            if (kc != kw) *(u32x2*)(a.out + (size_t)crow * D_LEN + rj * 64 + n * 4) = ov;
        }
        const int wrow = (int)(0xffffu - (kw & 0xffffu)) & (S_LEN - 1);
        const __amdgpu_buffer_rsrc_t rC = mk_rsrc(w2 + WS_CAND + (size_t)(wrow >> 6) * 4096, 4096);
        const __amdgpu_buffer_rsrc_t rPt = mk_rsrc(w2 + WS_PART, 112 * 4096);
        const int ocol = (rj >> 2) * 256 + ((lane >> 3) & 3) * 64 + (rj & 3) * 16 + (lane & 7) * 2, opos = (lane & 7) * 8 + ((lane >> 3) & 3);
        unsigned hv, hnv;
        for (int spin = 0;; ++spin) {
            asm volatile("" ::: "memory");
            hv = __builtin_amdgcn_raw_buffer_load_b32(rC, ocol * 2, 0, 16);
            hnv = __builtin_amdgcn_raw_buffer_load_b32(rC, 2048 + ocol * 2, 0, 16);
            if (__all(max(hv, hnv) != SENT) || spin > SPIN_CAP) break;
        }
        float sgm = rbf(1.0f / (1.0f + __expf(-key2r(kw))));
        GAS unsigned* optr = (GAS unsigned*)(a.out + (size_t)wrow * D_LEN + ocol);
        asm volatile("" : "+v"(sgm), "+v"(optr));
        u32x4 pv[4];
#pragma unroll 1
        for (int spin = 0;; ++spin) {
            asm volatile("" ::: "memory");
#pragma unroll
            for (int i = 0; i < 4; ++i) pv[i] = LD16_SC1(rPt, (wid + 8 * i) * 16384 + (rj >> 3) * 8192 + g * 2048 + ((rj & 7) * 64 + n * 4) * 4);
            if (__all(max(max(umax4(pv[0]), umax4(pv[1])), max(umax4(pv[2]), umax4(pv[3]))) != SENT) || spin > SPIN_CAP) break;
            __builtin_amdgcn_s_sleep(1);
        }
        if (wid == 0) STAMP(17);
        f32x4 sm;
#pragma unroll
        for (int j = 0; j < 4; ++j) {
            const unsigned u0 = pv[0][j], u1 = pv[1][j], u2 = pv[2][j], u3 = pv[3][j];
            sm[j] = sum_rows((__uint_as_float(u0) + __uint_as_float(u1)) + (__uint_as_float(u2) + __uint_as_float(u3)));
        }
        if (lane < 16) *(LAS f32x4*)(red + wid * 64 + lane * 4) = sm;
        WAIT_LGKM0();
        BARRIER();
        if (wid == 0) {
            float t0 = 0.f, t1 = 0.f;
#pragma unroll
            for (int w = 0; w < 8; ++w) { t0 += red[w * 64 + opos]; t1 += red[w * 64 + opos + 4]; }
            const float y0 = rbf(rbf(t0) * sgm), y1 = rbf(rbf(t1) * sgm);
            const unsigned o = pk_bf16(bflo(hv) + rbf(bflo(hnv) + y0), bfhi(hv) + rbf(bfhi(hnv) + y1));
            if (lane < 32) *optr = o;
            STAMP(18);
        }
        __builtin_amdgcn_s_waitcnt(0x0F70);
    }
    FLUSH();
}

extern "C" void kernel_launch(void* const* d_in, const int* in_sizes, int n_in, void* d_out, int out_size, void* d_ws,
                              size_t ws_size, hipStream_t stream) {
    Args a;
    a.x = (const bf16_t*)d_in[0];
    a.cr = (const bf16_t*)d_in[1];
    a.ci = (const bf16_t*)d_in[2];
    a.wre = (const bf16_t*)d_in[3];
    a.wim = (const bf16_t*)d_in[4];
    a.w_r = (const bf16_t*)d_in[6];
    a.wg = (const bf16_t*)d_in[7];
    a.wu = (const bf16_t*)d_in[8];
    a.wd = (const bf16_t*)d_in[9];
    a.out = (bf16_t*)d_out;
    a.ws = (char*)d_ws + (WS_END ? ((ws_size - ((size_t)24 << 20)) & ~(size_t)255) : 0);
    (void)hipFuncSetAttribute((const void*)fused_kernel, hipFuncAttributeMaxDynamicSharedMemorySize, LDS_BYTES);
    fused_kernel<<<256, 768, LDS_BYTES, stream>>>(a);
}
```

```cpp
#include <hip/hip_runtime.h>
#include <hip/hip_bf16.h>
#include <stdint.h>

typedef unsigned short bf16_t;
typedef short bf16x8 __attribute__((ext_vector_type(8)));
typedef short bf16x4 __attribute__((ext_vector_type(4)));
typedef float f32x4 __attribute__((ext_vector_type(4)));
typedef unsigned u32x4 __attribute__((ext_vector_type(4)));
typedef unsigned u32x2 __attribute__((ext_vector_type(2)));
typedef unsigned u32x3 __attribute__((ext_vector_type(3)));
typedef unsigned long long u64;
#define LAS __attribute__((address_space(3)))
#define GAS __attribute__((address_space(1)))

constexpr int S_LEN = 2048, D_LEN = 1024, F_LEN = 3584;
constexpr unsigned SENT = 0xFFFFFFFFu;
constexpr int SPIN_CAP = 1 << 17;

constexpr size_t WS_KEYS = 0;
constexpr size_t WS_FLAGS = 16384;
constexpr size_t WS_XRD = 524288;
constexpr size_t WS_FLAG1 = 262144;
constexpr size_t WS_TAG = 131072;
constexpr size_t WS_VOTE = WS_TAG + 4096;
constexpr size_t WS_PROBE = (size_t)20 << 20;
constexpr size_t WS_AT = (size_t)4 << 20;
constexpr size_t WS_Y = (size_t)12 << 20;
constexpr size_t WS_STATS = (size_t)16 << 20;
constexpr size_t WS_PART = WS_STATS + (512u << 10);
constexpr size_t WS_CAND = WS_STATS + (1024u << 10);
constexpr size_t WS_PRE = WS_STATS + (1152u << 10);
constexpr size_t FILL_BYTES = (size_t)1408 << 10;

__device__ __forceinline__ float bflo(unsigned v) { return __uint_as_float(v << 16); }
__device__ __forceinline__ float bfhi(unsigned v) { return __uint_as_float(v & 0xffff0000u); }
__device__ __forceinline__ unsigned pk_bf16(float lo, float hi) {
    typedef __bf16 bf2 __attribute__((ext_vector_type(2)));
    typedef float f2 __attribute__((ext_vector_type(2)));
    f2 v = {lo, hi};
    bf2 r = __builtin_convertvector(v, bf2);
    return __builtin_bit_cast(unsigned, r);
}
__device__ __forceinline__ float rbf(float x) { return bflo(pk_bf16(x, 0.f) & 0xffffu); }

template <int CTRL> __device__ __forceinline__ unsigned dppu(unsigned v) {
    return (unsigned)__builtin_amdgcn_update_dpp(0, (int)v, CTRL, 0xf, 0xf, true);
}
template <int CTRL> __device__ __forceinline__ float dppf(float v) { return __uint_as_float(dppu<CTRL>(__float_as_uint(v))); }
__device__ __forceinline__ void swap16(unsigned v, unsigned& a, unsigned& b) {
    auto r = __builtin_amdgcn_permlane16_swap(v, v, false, false);
    a = r[0]; b = r[1];
}
__device__ __forceinline__ void swap32(unsigned v, unsigned& a, unsigned& b) {
    auto r = __builtin_amdgcn_permlane32_swap(v, v, false, false);
    a = r[0]; b = r[1];
}
__device__ __forceinline__ float sum_rows(float v) {
    unsigned a, b;
    swap16(__float_as_uint(v), a, b); v = __uint_as_float(a) + __uint_as_float(b);
    swap32(__float_as_uint(v), a, b); v = __uint_as_float(a) + __uint_as_float(b);
    return v;
}
__device__ __forceinline__ float wave_sum(float v) {
    v += dppf<0xB1>(v);
    v += dppf<0x4E>(v);
    v += dppf<0x141>(v);
    v += dppf<0x140>(v);
    return sum_rows(v);
}
__device__ __forceinline__ unsigned wave_max_u32(unsigned v) {
    v = max(v, dppu<0xB1>(v));
    v = max(v, dppu<0x4E>(v));
    v = max(v, dppu<0x141>(v));
    v = max(v, dppu<0x140>(v));
    unsigned a, b;
    swap16(v, a, b); v = max(a, b);
    swap32(v, a, b); v = max(a, b);
    return v;
}
__device__ __forceinline__ float sel4(int g, float a, float b, float c, float d) {
    const float t0 = (g & 1) ? b : a, t1 = (g & 1) ? d : c;
    return (g & 2) ? t1 : t0;
}
__device__ __forceinline__ unsigned umax4(u32x4 v) { return max(max(v[0], v[1]), max(v[2], v[3])); }

__device__ __forceinline__ void touch_line(const void* p, unsigned lds_dst) {
    unsigned keep;
    asm volatile("s_mov_b32 %0, m0\n\ts_mov_b32 m0, %2\n\ts_nop 0\n\tglobal_load_lds_dword %1, off\n\ts_mov_b32 m0, %0"
                 : "=&s"(keep) : "v"(p), "s"(lds_dst) : "memory");
}
__device__ __forceinline__ __amdgpu_buffer_rsrc_t mk_rsrc(const void* p, unsigned bytes) {
    return __builtin_amdgcn_make_buffer_rsrc((void*)p, 0, (int)bytes, 0x00020000);
}
#define LDS_LOAD(w, v, o, pol) "buffer_load_" w " " v ", %0, %2 offen" o pol " lds"
#define LDS_M0 "s_mov_b32 m0, %1\n\ts_nop 0\n\t"
__device__ __forceinline__ void dma_nt(__amdgpu_buffer_rsrc_t r, unsigned dst, int voff, int soff) {
    asm volatile(LDS_M0 LDS_LOAD("dwordx4", "%3", "", " nt") :: "s"(r), "s"(dst), "s"(soff), "v"(voff) : "memory", "m0");
}
template <bool SC1> __device__ __forceinline__ void dma_word(__amdgpu_buffer_rsrc_t r, unsigned dst, int voff) {
    if constexpr (SC1) asm volatile(LDS_M0 LDS_LOAD("dword", "%3", "", " sc1") :: "s"(r), "s"(dst), "n"(0), "v"(voff) : "memory", "m0");
    else asm volatile(LDS_M0 LDS_LOAD("dword", "%3", "", "") :: "s"(r), "s"(dst), "n"(0), "v"(voff) : "memory", "m0");
}
__device__ __forceinline__ void dma_2k(__amdgpu_buffer_rsrc_t r, unsigned dst, int v0, int v1, int soff) {
    asm volatile(LDS_M0 LDS_LOAD("dwordx4", "%3", "", " sc1") "\n\t" LDS_LOAD("dwordx4", "%4", " offset:1024", " sc1")
                 :: "s"(r), "s"(dst), "s"(soff), "v"(v0), "v"(v1) : "memory", "m0");
}
template <bool SC1> __device__ __forceinline__ void dma_4k(__amdgpu_buffer_rsrc_t r, unsigned dst, const int (&v)[4], int soff) {
#define FOUR(pol) asm volatile(LDS_M0 LDS_LOAD("dwordx4", "%3", "", pol) "\n\t" LDS_LOAD("dwordx4", "%4", " offset:1024", pol) "\n\t" \
        LDS_LOAD("dwordx4", "%5", " offset:2048", pol) "\n\t" LDS_LOAD("dwordx4", "%6", " offset:3072", pol) \
        :: "s"(r), "s"(dst), "s"(soff), "v"(v[0]), "v"(v[1]), "v"(v[2]), "v"(v[3]) : "memory", "m0")
    if constexpr (SC1) FOUR(" sc1"); else FOUR("");
#undef FOUR
}
#define CODE_BYTES 26112
__device__ __forceinline__ __amdgpu_buffer_rsrc_t code_rsrc() {
    unsigned long long p; unsigned d;
    asm volatile("s_getpc_b64 %0\n1:\n\ts_mov_b32 %1, 1b-_Z12fused_kernel4Args" : "=s"(p), "=s"(d));
    return __builtin_amdgcn_make_buffer_rsrc((void*)(p - d), 0, CODE_BYTES, 0x00020000);
}
#define LD16_SC1(rs, off) __builtin_amdgcn_raw_buffer_load_b128(rs, off, 0, 16)
#define ST16_SC1(v, rs, off) __builtin_amdgcn_raw_buffer_store_b128(v, rs, off, 0, 16)

constexpr int BT = 128, BK = 64, NSTAGE = 4;
constexpr int TILE_BYTES = BT * BK * 2;
constexpr int STAGE_BYTES = 2 * TILE_BYTES;
constexpr int RING_BYTES = NSTAGE * STAGE_BYTES;
constexpr int L_DUMMY = RING_BYTES;
constexpr int L_CTL = RING_BYTES + 256;
constexpr int L_RED = RING_BYTES + 512;

#define WAIT_VM(n) asm volatile("s_waitcnt vmcnt(" #n ")" ::: "memory")
#define WAIT_VM0() do { asm volatile("" ::: "memory"); __builtin_amdgcn_s_waitcnt(0x0F70); asm volatile("s_waitcnt vmcnt(0)" ::: "memory"); } while (0)
#define WAIT_LGKM0() asm volatile("s_waitcnt lgkmcnt(0)" ::: "memory")
#define BARRIER() do { asm volatile("" ::: "memory"); __builtin_amdgcn_s_barrier(); asm volatile("" ::: "memory"); } while (0)

__device__ __forceinline__ int prow(int rho) {
    const int w = rho >> 6, m = (rho >> 4) & 3, q = (rho >> 2) & 3, r = rho & 3;
    return w * 64 + (m >> 1) * 32 + q * 8 + (m & 1) * 4 + r;
}
__device__ __forceinline__ unsigned r2key(float r, int row) {
    const unsigned u = pk_bf16(r, 0.f) & 0xffffu;
    const unsigned o = (u & 0x8000u) ? (~u & 0xffffu) : (u | 0x8000u);
    return (o << 16) | (0xffffu - (unsigned)row);
}
__device__ __forceinline__ float key2r(unsigned k) {
    const unsigned o = k >> 16;
    return bflo((o & 0x8000u) ? (o & 0x7fffu) : (~o & 0xffffu));
}
#define RESET_T 2
#define RESET_U 3
#define VOTE_T 4
#define PSM 0x024600
#define FIN_PRIO 3
#define W_PRE 2
#define W_PRE_SL 20
#define BQ 1
#define POLL_T 12
#define AUX_SLEEP 6
#define WD_EARLY 4
#define C2_START 0
#define C2_STAG 7
#define TAIL_PRIO 3
#define STAT_DELAY 14
#define CODE_WG 1
#define AUX_STAY 1
#define T_DELAY 0
#define WS_END 1
#define CATCH1 4
#define CPF_MASK1 0x1080u
#define CPF_MASK2 0x1080u
#define CPF_STAG 5
#define CPF_FIX 0x1000u
#define OUT_AUX 0
#define POLL_SLEEP 4
__device__ __forceinline__ float dot2bf(unsigned a, unsigned b, float c) {
    typedef __bf16 bf2 __attribute__((ext_vector_type(2)));
    return __builtin_amdgcn_fdot2_f32_bf16(__builtin_bit_cast(bf2, a), __builtin_bit_cast(bf2, b), c, false);
}

#define STAMP(i) do { } while (0)
#define CSTAMP(i) do { } while (0)
#define FLUSH() do { } while (0)
#define TSV do { } while (0)

#define KSHIFT 4

constexpr int L_HT = ((32 + KSHIFT) % NSTAGE) * STAGE_BYTES;
constexpr int L_WD0 = ((33 + KSHIFT) % NSTAGE) * STAGE_BYTES;
constexpr int L_WD1 = ((34 + KSHIFT) % NSTAGE) * STAGE_BYTES;
constexpr int L_YB = RING_BYTES;
constexpr int L_ATAB = L_YB;
constexpr int LDS_BYTES = RING_BYTES + 32768;

#define SB() __builtin_amdgcn_sched_barrier(0)
template <int N> __device__ __forceinline__ void wait_vm() { asm volatile("s_waitcnt vmcnt(%0)" :: "n"(N) : "memory"); }
__device__ __forceinline__ int at_tile(int c, int par) { return ((c << 8) & 0x78000) | (((c << 2) & 0x2000) ^ par); }
__device__ __forceinline__ int at_panel(int ep) { return ep << 19; }
__device__ __forceinline__ int pre_off(int slot) { const int s_ = slot >= 56, l = slot - 56 * s_; return (l >> 2) * 16384 + s_ * 8192 + (l & 3) * 2048; }
static_assert(KSHIFT >= 3 && KSHIFT % 2 == 0, "flag samples must have replaced older contents of their slot; even tile counts");
struct Ring {
    __amdgpu_buffer_rsrc_t rP, rQ, rF;
    int voP[4], voQ[4], voF;
    LAS unsigned char* fl;
    unsigned lb;
    int sp, sq;
    int poff, par;
    int tq, ti;
    bool img;
    template <int LD, bool PIMG> __device__ __forceinline__ void setup(int lw, int lane) {
        const int r = lane >> 3, v0 = (PIMG ? r : (r & 3) + (r & 4) * 2) * (LD * 2) + (((lane & 7) ^ (lane >> 4)) << 4);
#pragma unroll
        for (int j = 0; j < 4; ++j) {
            voQ[j] = (v0 ^ ((j & 1) * 64)) + (PIMG ? j * 8 : (j & 1) * 16 + (j >> 1) * 4) * (LD * 2) - j * 1024;
            voP[j] = PIMG ? (lw >> 1) * 16384 + (lw & 1) * 4096 + lane * 16 : voQ[j];
        }
        img = PIMG;
    }
    __device__ __forceinline__ void seek(int col, int pof) {
        sq = col; poff = pof;
        sp = (img ? at_tile(sq, par) : sq) + poff;
    }
    __device__ __forceinline__ void step() {
        sq = (sq + BK * 2) & 4095;
        sp = (img ? at_tile(sq, par) : sq) + poff;
        asm volatile("" : "+s"(sq), "+s"(sp));
    }
    __device__ __forceinline__ void sample() { dma_word<true>(rF, (unsigned)(size_t)fl, voF); }
    __device__ __forceinline__ unsigned peek(unsigned addr) {
        unsigned v;
        asm volatile("ds_read_b32 %0, %1\n\ts_waitcnt lgkmcnt(0)" : "=v"(v) : "v"(addr) : "memory");
        return v;
    }
    __device__ __forceinline__ bool seen() { return __builtin_amdgcn_readfirstlane(peek((unsigned)(size_t)fl)) == 1u; }
    __device__ __forceinline__ void gate(unsigned want, unsigned sa, unsigned need, unsigned& have) {
#pragma unroll 1
        for (int spin = 0; spin <= SPIN_CAP && (need & ~have) != 0u; ++spin) {
            if (spin) { sample(); WAIT_VM(0); __builtin_amdgcn_s_sleep(1); }
            const unsigned long long m = __ballot(peek(sa) == want);
            have = (unsigned)m & (unsigned)(m >> 32);
        }
    }
    template <int AUX> __device__ __forceinline__ void issueP(LAS unsigned char* base) { dma_4k<AUX != 0>(rP, (unsigned)(size_t)base, voP, sp); }
    __device__ __forceinline__ void issueQ(LAS unsigned char* base) { dma_4k<false>(rQ, (unsigned)(size_t)base + TILE_BYTES, voQ, sq); }
    template <bool P2> __device__ __forceinline__ void issue(LAS unsigned char* lds) {
        issueP<P2 ? 16 : 0>(lds + lb);
        issueQ(lds + lb);
        SB();
        if (P2) sample();
        lb = (lb + STAGE_BYTES) & (RING_BYTES - 1);
        asm volatile("" : "+s"(lb));
        if (P2) step();
        SB();
    }
    __device__ __forceinline__ void next1() {
        ++ti;
        sp = sq = __builtin_amdgcn_readlane(tq, ti);
        asm volatile("s_nop 4" : "+s"(sq), "+s"(sp), "+s"(ti));
        SB();
    }
    template <int CAT> __device__ __forceinline__ void ramp(LAS unsigned char* lds) {
#pragma unroll 1
        for (int i = 1; i <= CAT + 1; ++i) {
            for (int z = (PSM >> (4 * i)) & 15; z > 0; --z) __builtin_amdgcn_s_sleep(1);
            issue<false>(lds);
            next1();
            if (i == CAT + 1) continue;
            WAIT_VM(8);
            BARRIER();
        }
    }
    template <bool P2, bool SAMP, int W = 16> __device__ __forceinline__ void steady(int cnt) {
        int t, u;
#define LD(v, r, s, o) "buffer_load_dwordx4 " v ", " r ", " s " offen" o " lds\n\t"
        if constexpr (P2) {
#define BODY(SMP) asm volatile("s_mov_b32 m0, %0\n\ts_branch 2f\n1:\n\ts_waitcnt vmcnt(%17)\n\ts_barrier\n2:\n\t" \
            LD("%8", "%6", "%2", " sc1") LD("%8", "%6", "%2", " offset:1024 sc1") LD("%8", "%6", "%2", " offset:2048 sc1") LD("%8", "%6", "%2", " offset:3072 sc1") \
            SMP "s_mov_b32 %4, %1\n\ts_add_i32 %1, %1, 0x80\n\ts_add_i32 m0, %0, 0x4000\n\ts_and_b32 %1, %1, 0xfff\n\t" \
            LD("%9", "%7", "%4", "") "s_lshl_b32 %2, %1, 8\n\ts_lshl_b32 %5, %1, 2\n\ts_and_b32 %2, %2, 0x78000\n\t" \
            LD("%10", "%7", "%4", " offset:1024") "s_and_b32 %5, %5, 0x2000\n\ts_xor_b32 %5, %5, %13\n\ts_or_b32 %2, %2, %5\n\t" \
            LD("%11", "%7", "%4", " offset:2048") "s_add_i32 %0, %0, 0x8000\n\ts_and_b32 %0, %0, 0x1ffff\n\ts_add_i32 %3, %3, -1\n\ts_cmp_lg_u32 %3, 0\n\t" \
            LD("%12", "%7", "%4", " offset:3072") "s_mov_b32 m0, %0\n\ts_cbranch_scc1 1b\n\ts_waitcnt vmcnt(%17)\n\ts_barrier" \
            : "+s"(lb), "+s"(sq), "+s"(sp), "+s"(cnt), "=&s"(t), "=&s"(u) \
            : "s"(rP), "s"(rQ), "v"(voP[0]), "v"(voQ[0]), "v"(voQ[1]), "v"(voQ[2]), "v"(voQ[3]), "s"(par | poff), "s"(rF), "v"(voF), "s"((unsigned)(size_t)fl), "n"(W) \
            : "memory", "scc", "m0")
            if constexpr (SAMP) BODY("s_mov_b32 m0, %16\n\ts_nop 0\n\tbuffer_load_dword %15, %14, 0 offen sc1 lds\n\t");
            else BODY("");
#undef BODY
        } else {
            asm volatile("s_mov_b32 m0, %0\n\ts_branch 2f\n1:\n\ts_waitcnt vmcnt(16)\n\ts_barrier\n2:\n\t"
                LD("%6", "%4", "%1", "") LD("%7", "%4", "%1", " offset:1024") LD("%8", "%4", "%1", " offset:2048") LD("%9", "%4", "%1", " offset:3072")
                "s_add_i32 m0, %0, 0x4000\n\ts_mov_b32 %3, %1\n\t"
                LD("%6", "%5", "%3", "") "s_add_i32 %2, %2, 1\n\ts_add_i32 %0, %0, 0x8000\n\t"
                LD("%7", "%5", "%3", " offset:1024") "s_and_b32 %0, %0, 0x1ffff\n\tv_readlane_b32 %1, %10, %2\n\t"
                LD("%8", "%5", "%3", " offset:2048") "s_cmp_lg_u32 %2, %11\n\t"
                LD("%9", "%5", "%3", " offset:3072") "s_mov_b32 m0, %0\n\ts_cbranch_scc1 1b\n\ts_waitcnt vmcnt(16)\n\ts_barrier"
                : "+s"(lb), "+s"(sq), "+s"(ti), "=&s"(t)
                : "s"(rP), "s"(rQ), "v"(voQ[0]), "v"(voQ[1]), "v"(voQ[2]), "v"(voQ[3]), "v"(tq), "n"(D_LEN / BK)
                : "memory", "scc", "m0");
            (void)cnt;
            (void)u;
        }
#undef LD
    }
};

template <bool PLIN, bool FIRST, class Pre>
__device__ __forceinline__ void compute_loop(const LAS unsigned char* lds, f32x4 (&acc)[4][4], int NT, int wp, int wq, int lane, Pre pre) {
    const int fr = lane & 15, fq = lane >> 4, key = (fr >> 1) & 7;
    const unsigned l0 = (unsigned)(size_t)lds;
    const unsigned oP0 = l0 + (PLIN ? wp * 8192 + lane * 16 : (wp * 64 + fr) * 128 + ((fq ^ key) * 16));
    const unsigned oP1 = l0 + (PLIN ? wp * 8192 + 1024 + lane * 16 : (wp * 64 + fr) * 128 + (((4 + fq) ^ key) * 16));
    const unsigned oQ0 = l0 + TILE_BYTES + (wq * 64 + fr) * 128 + ((fq ^ key) * 16);
    const unsigned oQ1 = l0 + TILE_BYTES + (wq * 64 + fr) * 128 + (((4 + fq) ^ key) * 16);
    unsigned aP0 = oP0, aQ0 = oQ0, aP1 = oP1, aQ1 = oQ1;
    bf16x8 p0[4], p1[4], r0[4], r1[4], q0[4], q1[4];
#pragma unroll
    for (int i = 0; i < 16; ++i) asm volatile("" : "+v"(acc[i >> 2][i & 3]));
    asm volatile("" : "+v"(aP0), "+v"(aQ0), "+v"(aP1), "+v"(aQ1));
#define MM(pp, qq, m, n) do { acc[m][n] = __builtin_amdgcn_mfma_f32_16x16x32_bf16(pp[m], qq[n], acc[m][n], 0, 0, 0); SB(); } while (0)
#define RD(dst, i, a, st) do { dst[i] = *(const LAS bf16x8*)((const LAS unsigned char*)(size_t)(a) + (st) * STAGE_BYTES + (i) * 2048); SB(); } while (0)
#define ADR(a) do { a ^= 2 * STAGE_BYTES; asm volatile("" : "+v"(a)); SB(); } while (0)
    __builtin_amdgcn_s_waitcnt(0xC07F);
    SB();
    if (FIRST) BARRIER();
    SB();
    RD(p0, 0, aP0, 0); RD(q0, 0, aQ0, 0); RD(p1, 0, aP1, 0); RD(q1, 0, aQ1, 0); RD(p1, 1, aP1, 0); RD(p0, 1, aP0, 0);
    RD(p0, 2, aP0, 0); RD(p1, 2, aP1, 0); RD(p1, 3, aP1, 0); RD(p0, 3, aP0, 0);
    pre();
    SB();
    int t = NT;
#pragma unroll 1
    do {
        MM(p0, q0, 0, 0); MM(p1, q1, 0, 0); RD(q0, 1, aQ0, 0);
        MM(p1, q1, 1, 0); MM(p0, q0, 1, 0); RD(q1, 1, aQ1, 0);
        MM(p0, q0, 2, 0); MM(p1, q1, 2, 0); RD(q0, 2, aQ0, 0);
        MM(p1, q1, 3, 0); MM(p0, q0, 3, 0); RD(q1, 2, aQ1, 0);
        MM(p0, q0, 3, 1); MM(p1, q1, 3, 1); RD(q0, 3, aQ0, 0);
        MM(p1, q1, 2, 1); MM(p0, q0, 2, 1); RD(q1, 3, aQ1, 0);
        MM(p0, q0, 1, 1); MM(p1, q1, 1, 1);
        MM(p1, q1, 0, 1); MM(p0, q0, 0, 1);
        MM(p0, q0, 0, 2); MM(p1, q1, 0, 2); __builtin_amdgcn_s_waitcnt(0xC07F); SB(); BARRIER(); SB();
        MM(p1, q1, 1, 2); RD(r0, 0, aP0, 1); MM(p0, q0, 1, 2); RD(q0, 0, aQ0, 1);
        MM(p0, q0, 2, 2); RD(r1, 0, aP1, 1); MM(p1, q1, 2, 2); RD(q1, 0, aQ1, 1);
        MM(p1, q1, 3, 2); MM(p0, q0, 3, 2); RD(r1, 1, aP1, 1);
        MM(p0, q0, 3, 3); RD(r0, 1, aP0, 1); MM(p1, q1, 3, 3);
        MM(p1, q1, 2, 3); RD(r0, 2, aP0, 1); MM(p0, q0, 2, 3); RD(r1, 2, aP1, 1);
        MM(p0, q0, 1, 3); MM(p1, q1, 1, 3); RD(r1, 3, aP1, 1);
        MM(p1, q1, 0, 3); RD(r0, 3, aP0, 1); MM(p0, q0, 0, 3);

        MM(r0, q0, 0, 0); MM(r1, q1, 0, 0); RD(q0, 1, aQ0, 1);
        MM(r1, q1, 1, 0); MM(r0, q0, 1, 0); RD(q1, 1, aQ1, 1);
        MM(r0, q0, 2, 0); MM(r1, q1, 2, 0); RD(q0, 2, aQ0, 1);
        MM(r1, q1, 3, 0); MM(r0, q0, 3, 0); RD(q1, 2, aQ1, 1);
        MM(r0, q0, 3, 1); MM(r1, q1, 3, 1); RD(q0, 3, aQ0, 1);
        MM(r1, q1, 2, 1); MM(r0, q0, 2, 1); RD(q1, 3, aQ1, 1);
        MM(r0, q0, 1, 1); t -= 2; asm volatile("" : "+s"(t)); SB(); MM(r1, q1, 1, 1); ADR(aP0);
        MM(r1, q1, 0, 1); ADR(aP1); MM(r0, q0, 0, 1); ADR(aQ0);
        MM(r0, q0, 0, 2); ADR(aQ1); MM(r1, q1, 0, 2); __builtin_amdgcn_s_waitcnt(0xC07F); SB(); BARRIER(); SB();
        MM(r1, q1, 1, 2); RD(p0, 0, aP0, 0); MM(r0, q0, 1, 2); RD(q0, 0, aQ0, 0);
        MM(r0, q0, 2, 2); RD(p1, 0, aP1, 0); MM(r1, q1, 2, 2); RD(q1, 0, aQ1, 0);
        MM(r1, q1, 3, 2); MM(r0, q0, 3, 2); RD(p1, 1, aP1, 0);
        MM(r0, q0, 3, 3); RD(p0, 1, aP0, 0); MM(r1, q1, 3, 3);
        MM(r1, q1, 2, 3); RD(p0, 2, aP0, 0); MM(r0, q0, 2, 3); RD(p1, 2, aP1, 0);
        MM(r0, q0, 1, 3); MM(r1, q1, 1, 3); RD(p1, 3, aP1, 0);
        MM(r1, q1, 0, 3); RD(p0, 3, aP0, 0); MM(r0, q0, 0, 3);
    } while (t != 0);
    SB();
#undef MM
#undef RD
#undef ADR
}

struct Args {
    const bf16_t* x; const bf16_t* wre; const bf16_t* wim; char* ws; const bf16_t* cr; const bf16_t* ci;
    const bf16_t* w_r; const bf16_t* wg; const bf16_t* wu; const bf16_t* wd; bf16_t* out;
};

__device__ __forceinline__ unsigned poll_winner(const char* ws, int rep, int lane, unsigned& mine) {
    GAS unsigned* kp = (GAS unsigned*)(ws + WS_KEYS + (size_t)(rep >> 2) * 8192 + (rep & 3) * 256) + (lane & 31);
    unsigned kw;
    for (int spin = 0;; ++spin) {
            asm volatile("" ::: "memory");
        kw = __hip_atomic_load(kp, __ATOMIC_RELAXED, __HIP_MEMORY_SCOPE_AGENT);
        if (__all(kw != 0u) || spin > SPIN_CAP) break;
        __builtin_amdgcn_s_sleep(POLL_SLEEP);
    }
    mine = kw;
    return __builtin_amdgcn_readfirstlane(wave_max_u32(kw));
}

__device__ __forceinline__ void stage2(char* w2, int slot, int lw, LAS unsigned char* img0, LAS unsigned char* img1, LAS unsigned char* abuf, int lane) {
    const int g = lane >> 4, n = lane & 15;
    const __amdgpu_buffer_rsrc_t rPi = mk_rsrc(w2 + WS_PRE + pre_off(slot), 2048);
    unsigned pp[8];
    auto request = [&]() {
        asm volatile("" ::: "memory");
#pragma unroll
        for (int k = 0; k < 8; ++k) pp[k] = __builtin_amdgcn_raw_buffer_load_b32(rPi, k * 256 + lane * 4, 0, 16);
        SB();
    };
    request();
    bf16x8 wf[16];
    {
        const int q = (lane >> 2) & 3, ky = q | ((g & 1) << 2);
#pragma unroll
        for (int j = 0; j < 16; ++j) {
            LAS unsigned char* t = (g < 2 ? img0 : img1) + (8 * (g & 1) + q) * 512 + (((j & 7) ^ ky) + (j & 8)) * 32 + (lane & 3) * 8;
            const bf16x4 w0 = __builtin_amdgcn_ds_read_tr16_b64_v4i16((LAS bf16x4*)t);
            const bf16x4 w1 = __builtin_amdgcn_ds_read_tr16_b64_v4i16((LAS bf16x4*)(t + 2048));
            wf[j] = __builtin_shufflevector(w0, w1, 0, 1, 2, 3, 4, 5, 6, 7);
        }
        SB();
#pragma unroll
        for (int j = 0; j < 16; ++j) asm volatile("" : "+v"(wf[j]));
    }
    unsigned aa[5], pto = lane * 16;
#pragma unroll
    for (int m = 0; m < 4; ++m) aa[m] = (unsigned)(size_t)abuf + ((n >> 2) == m ? 0 : 64) + g * 16;
    aa[4] = (unsigned)(size_t)abuf + (lane & 31) * 2;
    __amdgpu_buffer_rsrc_t rPt = mk_rsrc(w2 + WS_PART + __builtin_amdgcn_readfirstlane((slot >> 2) * 16384 + (lw >> 1) * 8192 + (slot & 3) * 2048 + (lw & 1) * 1024), 1024);
    asm volatile("" : "+v"(aa[0]), "+v"(aa[1]), "+v"(aa[2]), "+v"(aa[3]), "+v"(aa[4]), "+v"(pto));
    asm volatile("" : "+s"(rPt));
    SB();
#pragma unroll 1
    for (int spin = 0; spin <= SPIN_CAP && !__all(max(max(max(pp[0], pp[1]), max(pp[2], pp[3])), max(max(pp[4], pp[5]), max(pp[6], pp[7]))) != SENT); ++spin) request();
    const float sv = ((__uint_as_float(pp[0]) + __uint_as_float(pp[1])) + (__uint_as_float(pp[2]) + __uint_as_float(pp[3]))) +
                     ((__uint_as_float(pp[4]) + __uint_as_float(pp[5])) + (__uint_as_float(pp[6]) + __uint_as_float(pp[7])));
    unsigned ug, uu;
    swap32(__float_as_uint(sv), ug, uu);
    const float gg = rbf(__uint_as_float(ug)), up = rbf(__uint_as_float(uu));
    const unsigned act = pk_bf16(rbf(gg * __builtin_amdgcn_rcpf(1.0f + __expf(-gg))) * up, 0.f);
    *(volatile LAS unsigned short*)(size_t)aa[4] = (unsigned short)act;
    bf16x8 af[4];
#pragma unroll
    for (int m = 0; m < 4; ++m) af[m] = *(volatile LAS bf16x8*)(size_t)aa[m];
    u32x4 pv;
#pragma unroll
    for (int q = 0; q < 4; ++q) {
        f32x4 d = {0.f, 0.f, 0.f, 0.f};
#pragma unroll
        for (int m = 0; m < 4; ++m) d = __builtin_amdgcn_mfma_f32_16x16x32_bf16(af[m], wf[4 * q + m], d, 0, 0, 0);
        const float e = d[0];
        pv[q] = min(__float_as_uint(e), SENT - 1u);
    }
    ST16_SC1(pv, rPt, pto);
}
__device__ __forceinline__ int wd_off(int lw, int i, int lane) {
    const int r = 2 * (i & 5) + (lane >> 5), ky = (r & 3) | ((r >> 1) & 4), c = lane & 31;
    return (lane >> 5) * (D_LEN * 2) + lw * 512 + (((c >> 1) ^ ky) * 2 + (c & 1)) * 16;
}

__global__ __launch_bounds__(768, 3) void fused_kernel(const Args a) {
    extern __shared__ __attribute__((aligned(16))) unsigned char smem[];
    constexpr int K = S_LEN, NT1 = D_LEN / BK;
    const int tid = threadIdx.x, wid = __builtin_amdgcn_readfirstlane(tid >> 6), lane = tid & 63;
    const int xcd = blockIdx.x & 7, idx = blockIdx.x >> 3;
    TSV;
    LAS unsigned char* lds = (LAS unsigned char*)smem;
    if (idx < CODE_WG && wid < 4) dma_word<false>(code_rsrc(), (unsigned)(size_t)lds + L_DUMMY, tid * 128);
    SB();
    const int h = xcd & 1, xq = xcd >> 1, rt = xq * 4 + (idx & 3), ct = idx >> 2;
    volatile LAS unsigned* ctl1 = (volatile LAS unsigned*)(lds + L_CTL);
#define NBR(k) ((rt + (k)) & 3)
    unsigned kx;
    Ring ring;
    if (__builtin_expect((wid & 12) == 4, 1)) {
        __builtin_amdgcn_s_setprio(3);
        ring.template setup<D_LEN, false>(wid - 4, lane);
        ring.lb = (wid - 4) * 4096;
        auto ord1 = [&](int i) {
            const int q = (i - idx - (idx >> 2)) & 3;
            return ((xq * 4 + h * 8 + i + 4 - (q == 0 ? 0 : q == 2 ? 4 : 2)) & 15) * 128;
        };
        ring.poff = 0;
        ring.sp = ring.sq = ord1(0);
        unsigned ox = (unsigned)(rt * 4 + wid - 4) * (32 * D_LEN * 2), ow = (unsigned)(ct * 4 + wid - 4) * (32 * D_LEN * 2);
        asm volatile("" : "+s"(ring.sq), "+s"(ring.lb), "+s"(ox), "+s"(ow), "+v"(ring.voQ[0]), "+v"(ring.voQ[1]), "+v"(ring.voQ[2]), "+v"(ring.voQ[3]));
        SB();
        ring.sp = ring.sq;
#pragma unroll
        for (int j = 0; j < 4; ++j) ring.voP[j] = ring.voQ[j];
        ring.rP = mk_rsrc((const char*)a.x + ox, 32 * D_LEN * 2);
        ring.rQ = mk_rsrc((const char*)(h == 0 ? a.wre : a.wim) + ow, 32 * D_LEN * 2);
        ring.template issue<false>(lds);
        __builtin_amdgcn_s_setprio(0);
        ring.tq = ord1(lane);
        ring.ti = 0;
        ring.next1();
        ring.par = (ct & 4) << 11;
        ring.template ramp<CATCH1>(lds);
        ring.template steady<false, false>(NT1 - 2 - CATCH1);
        ring.template setup<K, true>(wid - 4, lane);
        ring.rQ = mk_rsrc((h == 0 ? a.cr : a.ci) + (size_t)(rt * 4 + wid - 4) * 32 * K, 32 * K * 2);
        ring.seek(rt * 256, 0);
        LAS unsigned char* l0 = lds + (wid - 4) * 4096;
        ring.issueQ(l0); ring.step();
        WAIT_VM(12); BARRIER();
        ring.issueQ(l0 + STAGE_BYTES);
        ring.seek((xq * 4 + NBR(1)) * 256, 0);
        WAIT_VM(8); BARRIER();
        ring.issueQ(l0 + 2 * STAGE_BYTES);
        ring.seek((xq * 4 + NBR(2)) * 256, 0);
        BARRIER();
        ring.issueQ(l0 + 3 * STAGE_BYTES);
        kx = (unsigned)__builtin_amdgcn_readfirstlane((int)ctl1[4]);
        SB();
    }
    char* ws = a.ws + ((0ull - (unsigned long long)a.ws) & 0x1fffffull);
    const int g = lane >> 4, n = lane & 15;
    LAS float* tab = (LAS float*)(lds + L_RED);
    if (wid >= 8) {
        const int aw = wid - 8;
        GAS u64* tagp = (GAS u64*)(ws + WS_TAG);
        if (AUX_SLEEP) __builtin_amdgcn_s_sleep(AUX_SLEEP);
        u64 tag0 = __hip_atomic_load(tagp, __ATOMIC_RELAXED, __HIP_MEMORY_SCOPE_AGENT);
        SB();
        const int rho = (aw < 2 ? ct : (xq & 1) * 4 + (idx & 3)) * 16 + (aw & 1) * 8 + (n >> 1);
        unsigned fa = (unsigned)(size_t)lds + (aw >> 1) * TILE_BYTES + rho * 128 + ((((n & 1) * 4 + g) ^ ((rho >> 1) & 7)) * 16);
        constexpr int NIL = 1 << 30;
        const int at = tid - 512;
        __amdgpu_buffer_rsrc_t rZ = mk_rsrc(ws, 18u << 20);
        int z0 = (int)WS_STATS + blockIdx.x * (int)(FILL_BYTES / 256) + at * 16;
        int z1 = aw < 2 ? (at < (int)(FILL_BYTES / 4096) - 256 ? z0 + 4096 : NIL) : aw == 2 ? (lane < 10 ? blockIdx.x * 160 + lane * 16 : NIL)
               : lane < 32 ? (int)WS_FLAG1 + (lane >> 4) * 65536 + h * 32768 + (rt >> 3) * 16384 + ((lane >> 3) & 1) * 8192 + ct * 1024 + (rt & 7) * 128 + (lane & 7) * 16
               : NIL;
        u32x4 zv = {SENT, SENT, SENT, SENT};
        asm volatile("" : "+v"(fa), "+v"(z0), "+v"(z1), "+v"(zv), "+s"(rZ));
        f32x4 d1 = {0.f, 0.f, 0.f, 0.f}, d2 = {0.f, 0.f, 0.f, 0.f};
        u32x4 ones = {0x3F803F80u, 0x3F803F80u, 0x3F803F80u, 0x3F803F80u};
        asm volatile("" : "+v"(ones), "+v"(d1), "+v"(d2));
        if (aw == 0) {
            const int j = lane < 4 ? ct * 4 + lane : (idx & 3) * 8 + lane - 4;
            dma_word<true>(rZ, (unsigned)(size_t)lds + L_DUMMY, lane >= 12 ? NIL : (int)WS_XRD + (j >> 4) * 65536 + (lane < 4 ? rt : 16 + h * 8 + ct) * 2048 + (j & 15) * 128);
        }
        const char* cb;
        __amdgpu_buffer_rsrc_t rR, rS, rV;
        unsigned mask, tb, wl, wh;
        int ti, ro, o0, o1;
        auto warm = [&](int t) { if (t >= 4 && ((mask >> t) & 1)) { touch_line(cb + (ti & 7) * 512, L_DUMMY); ti += 2; } };
        u32x4 w0v, w1v;
        float wsr = 0.f;
#pragma unroll
        for (int t = 0; t < 8; ++t) {
            SB();
            BARRIER(); SB();
            const bf16x8 f = *(const LAS bf16x8*)((const LAS unsigned char*)(size_t)fa + (t & 1) * STAGE_BYTES);
            SB();
            if (aw == 1) { STAMP(40 + t); if (!(t & 1)) CSTAMP(57 + t / 2); }
            if (t == 0) {
                rR = mk_rsrc(a.w_r, aw == 3 && h == 0 ? D_LEN * 2 : 0);
                rS = mk_rsrc(ws + WS_XRD, 131072);
                rV = mk_rsrc(ws + WS_VOTE, 512);
                asm volatile("" : "+s"(rR), "+s"(rS), "+s"(rV));
            }
            if (t == 1) {
                cb = (const char*)(h ? a.ci : a.cr) + (size_t)rt * (512u << 10) + ct * 65536 + (lane >> 2) * 4096 + (lane & 3) * 128;
                mask = aw == 1 ? CPF_MASK1 : aw == 2 ? CPF_MASK2 : 0u;
                mask = (mask & CPF_FIX) | ((mask & ~CPF_FIX) << (idx % CPF_STAG));
                ti = 2 * xq + aw - 1;
                asm volatile("" : "+v"(cb), "+s"(mask), "+s"(ti));
            }
            if (t == 2) {
                w0v = __builtin_amdgcn_raw_buffer_load_b128(rR, lane * 16, 0, 0); w1v = __builtin_amdgcn_raw_buffer_load_b128(rR, lane * 16, 1024, 0);
                ro = (n | 2) != g * 4 + 2 ? 131072 : (aw < 2 ? h * 65536 + rt * 2048 : (xq >> 1) * 65536 + (16 + h * 8 + ct) * 2048) + rho * 16;
                asm volatile("" : "+v"(ro));
            }
            if (t == 3) {
                const int hi = aw == 3 ? 64 : 0;
                o0 = rt * 2048 + (hi + lane) * 16;
                o1 = o0 + (16 + h * 8 + ct - rt) * 2048;
                tb = (unsigned)(size_t)(tab + prow(hi + lane));
                asm volatile("" : "+v"(o0), "+v"(o1), "+v"(tb));
            }
            if (t == RESET_T) ST16_SC1(zv, rZ, z0);
            if (t == RESET_U) { if (aw >= 2) zv = (u32x4){0u, 0u, 0u, 0u}; ST16_SC1(zv, rZ, z1); }
            warm(t);
            if (t & 1) fa ^= 2 * STAGE_BYTES;
            d1 = __builtin_amdgcn_mfma_f32_16x16x32_bf16(f, __builtin_bit_cast(bf16x8, ones), d1, 0, 0, 0);
            if (aw < 2) d2 = __builtin_amdgcn_mfma_f32_16x16x32_bf16(f, f, d2, 0, 0, 0);
            if (t == 5) {
#pragma unroll
                for (int i = 0; i < 4; ++i) { wsr = dot2bf(w0v[i], 0x3F803F80u, wsr); wsr = dot2bf(w1v[i], 0x3F803F80u, wsr); }
                asm volatile("" : "+v"(wsr));
            }
            if (t == 1) {
                asm volatile("" : "+v"(tag0));
                const u64 want = tag0 * 6364136223846793005ull + 1442695040888963407ull;
                wl = (unsigned)want; wh = (unsigned)(want >> 32);
                asm volatile("" : "+v"(wl), "+v"(wh));
            }
            if (t == VOTE_T && aw == 3 && idx < 8) __builtin_amdgcn_raw_buffer_store_b64((u32x2){ctl1[5], wl}, rV, blockIdx.x * 8 + lane * 512, 0, 16);
            if (t == 6) WAIT_VM0();
            asm volatile("" : "+v"(d1), "+v"(d2), "+v"(fa));
        }
        SB(); BARRIER(); SB();
        {
            const float sa = d1[0] + d1[1], sb = d1[2] + d1[3];
            float sq = sel4(n, d2[0], d2[1], d2[2], d2[3]);
            sq += dppf<0xB1>(sq);
            const u32x4 rec = {__float_as_uint((n & 2) ? sb : sa), wl, __float_as_uint(sq), wh};
            ST16_SC1(rec, rS, ro);
        }
        if (aw == 1) { STAMP(48); CSTAMP(61); }
        if (aw == 1 || aw == 2) {
            warm(8);
#pragma unroll 1
            for (int t = 9; t <= NT1 + AUX_STAY; ++t) {
                BARRIER();
                if (aw == 1 && t <= 16) { STAMP(40 + t); if (!(t & 1)) CSTAMP(t <= 12 ? 57 + t / 2 : 25 + t / 2); }
                warm(t);
            }
            FLUSH();
            return;
        }
        SB(); BARRIER(); SB();
        if (aw == 3) {
            wsr = wave_sum(wsr);
            if (lane == 0) ctl1[1] = __float_as_uint(wsr);
        } else {
            if (lane == 0) { ctl1[0] = wl | 2u; ctl1[2] = wl; ctl1[3] = wh; }
            *(LAS u32x2*)(lds + L_YB + 12288 + 256 + lane * 8) = (u32x2){0u, 0u};
        }
#pragma unroll
        for (int t = 10; t <= POLL_T; ++t) BARRIER();
        SB();
        u32x4 q0, q1, q2, q3;
        u32x2 qv;
        auto request = [&]() {
            asm volatile("" ::: "memory");
            q0 = LD16_SC1(rS, o0); q1 = LD16_SC1(rS, o0 + 65536); q2 = LD16_SC1(rS, o1); q3 = LD16_SC1(rS, o1 + 65536);
            qv = __builtin_amdgcn_raw_buffer_load_b64(rV, lane * 8, 0, 16);
            SB();
        };
        if (FIN_PRIO) __builtin_amdgcn_s_setprio(FIN_PRIO);
        request();
        int b = POLL_T + 1;
        for (int spin = 0; spin <= SPIN_CAP; ++spin) {
            if (b < NT1) { BARRIER(); ++b; }
            SB();
            unsigned d = ((q0[1] ^ wl) | (q1[1] ^ wl)) | ((q2[1] ^ wl) | (q3[1] ^ wl)) | ((q0[3] ^ wh) | (q1[3] ^ wh)) | ((q2[3] ^ wh) | (q3[3] ^ wh)) | (qv[1] ^ wl);
            asm volatile("" : "+v"(d));
            if (__all(d == 0u)) break;
            request();
        }
        {
            const unsigned a0 = q0[0], a1 = q1[0], b0 = q0[2], b1 = q1[2], c0 = q2[0], c1_ = q3[0];
            const float mean = (__uint_as_float(a0) + __uint_as_float(a1)) * (1.0f / D_LEN);
            const float var = fmaxf((__uint_as_float(b0) + __uint_as_float(b1)) * (1.0f / D_LEN) - mean * mean, 0.f);
            float rden = __builtin_amdgcn_rcpf(rbf(__builtin_amdgcn_sqrtf(rbf(rbf(var) + 1e-5f))));
            if (h != 0) rden = -rden;
            float mr = rbf(mean) * rden;
            float wv = __uint_as_float(c0) + __uint_as_float(c1_);
            const unsigned vt = qv[0];
            unsigned kv = __popcll(__ballot(vt == 2u)) > __popcll(__ballot(vt == 1u)) ? 8192u : 0u;
            asm volatile("" : "+v"(rden), "+v"(mr), "+v"(wv), "+v"(kv));
            SB();
            if (b < NT1) BARRIER();
            SB();
            LAS float* tp = (LAS float*)(size_t)tb;
            tp[0] = rden; tp[128] = mr; tp[256] = wv;
            ctl1[4] = kv;
        }
        WAIT_LGKM0();
        return;
    }
#define CTL(i) ((unsigned)__builtin_amdgcn_readfirstlane((int)ctl[i]))
    const int wp = wid & 1, wq = (wid >> 1) & 1;
#define Y_OFF ((int)WS_Y + ((rt & 7) * 8 + ct) * 65536 + (wid & 3) * 16384 + (((rt >> 3) ^ (rt >> 2)) & 1) * 8192)
#define P2_DEFS \
    char* w2 = ws + kx; \
    const int sidx = idx < 28 ? xq * 28 + idx : 112 + xq * 4 + idx - 28;     \
    volatile LAS unsigned* ctl = (volatile LAS unsigned*)(lds + (h == 0 ? L_ATAB : L_CTL)); \
    const int NT = h == 0 ? 32 + KSHIFT : 32 - KSHIFT; \
    LAS float* rstat = (LAS float*)(lds + L_ATAB + 256);     \
    const int hp_ = rt * 2 + wq;                             \
    (void)sidx; (void)ctl; (void)NT; (void)rstat; (void)hp_; (void)w2
    if (__builtin_expect(wid < 4, 1)) {
        if (wid == 3 && idx < 8) {
            const unsigned r = (((unsigned)__builtin_amdgcn_s_memrealtime() * 2654435761u + blockIdx.x * 40503u) >> 9) & 0x1fdf80u;
            const unsigned first = (idx & 1) << 13;
            const char* pb = ws + WS_PROBE;
            unsigned va, vb, left;
            asm volatile("s_waitcnt lgkmcnt(0)\n\ts_load_dword %0, %3, %4\n\ts_load_dword %1, %3, %5\n\ts_waitcnt lgkmcnt(1)\n\t"
                         "s_nop 15\n\ts_getreg_b32 %2, hwreg(HW_REG_IB_STS, 8, 4)\n\ts_waitcnt lgkmcnt(0)"
                         : "=&s"(va), "=&s"(vb), "=&s"(left) : "s"(pb), "s"(r | first), "s"(r | (first ^ 8192u)) : "memory");
            const unsigned nearbit = (left != 0u ? first : first ^ 8192u) >> 13;
            if (lane == 0) ctl1[5] = nearbit == (unsigned)(xcd >> 2) ? 1u : 2u;
        }
        f32x4 acc[4][4];
#pragma unroll
        for (int i = 0; i < 16; ++i) acc[i >> 2][i & 3] = (f32x4){0.f, 0.f, 0.f, 0.f};
        unsigned tc = (unsigned)(size_t)(tab + wp * 64 + g * 8), tw = (unsigned)(size_t)(tab + 256 + prow(wq * 64 + n));
        unsigned ta = (unsigned)(size_t)lds + wp * STAGE_BYTES + wq * 8192 + lane * 16;
        constexpr int NIL = 1 << 30;
        __amdgpu_buffer_rsrc_t rW = mk_rsrc(ws, 18u << 20);
        const int hpan = rt * 2 + wq;
        int sk = (int)WS_AT + at_panel(h * 8 + ct) + at_tile((rt * 2 + wp) * 128, (ct & 4) << 11) + wq * 16384 + lane * 16;
        int st = lane != 0 ? NIL : (int)WS_FLAG1 + (1 - wp) * 65536 + h * 32768 + (rt >> 3) * 24576 + ct * 1024 + (rt & 7) * 128 + wq * 4;
        int so = h != 0 ? Y_OFF + lane * 16 : g != (n >> 2) ? NIL
               : (int)WS_STATS + (hpan & 15) * 32768 + (ct >> 2) * 16384 + (hpan >> 4) * 8192 + ((ct & 3) * 2 + wp) * 1024 + n * 16;
        int sf = lane != 0 ? NIL : (int)WS_FLAGS + (rt * 8 + ct) * 128 + wid * 4;
        u32x4 sel[2], aw[2];
        unsigned one[2];
#pragma unroll
        for (int i = 0; i < 4; ++i) {
            one[i & 1] = g == (n >> 2) && (i & 1) == ((n >> 1) & 1) ? ((n & 1) ? 0x3F800000u : 0x00003F80u) : 0u;
            aw[0][i] = aw[1][i] = (n & 3) == 0 ? 0x3F803F80u : 0u;
        }
        if ((n & 3) == 1) {
            const bf16_t* wr = a.w_r + ct * BT + wp * 64 + g * 8;
            aw[0] = *(const u32x4*)wr; aw[1] = *(const u32x4*)(wr + 32);
        }
        unsigned ea[2];
#pragma unroll
        for (int mp = 0; mp < 2; ++mp)
            ea[mp] = (unsigned)(size_t)lds + L_YB + wq * 16384 + (h == 0 ? wp * 2048 + mp * 1024 + lane * 16 : n * 256 + (((wp * 8 + mp * 4 + g) ^ n) * 16));
        asm volatile("" : "+v"(tc), "+v"(tw), "+v"(ta), "+v"(sk), "+v"(st), "+v"(so), "+v"(sf), "+v"(one[0]), "+v"(one[1]), "+v"(ea[0]), "+v"(ea[1]));
        asm volatile("" : "+s"(rW));
        unsigned tg;
        if (wid == 0) STAMP(0);
        compute_loop<false, true>(lds, acc, NT1, wp, wq, lane, [&]() { if (wid == 0) STAMP(24); if (wid == 0) STAMP(31); });
        if (wid == 0) STAMP(25);
        __builtin_amdgcn_s_waitcnt(0x0F70);
        {
            f32x4 rd[4], mr[4];
            float wsm[4];
#pragma unroll
            for (int m = 0; m < 4; ++m) {
                rd[m] = *(const LAS f32x4*)((const LAS float*)(size_t)tc + (m >> 1) * 32 + (m & 1) * 4);
                mr[m] = *(const LAS f32x4*)((const LAS float*)(size_t)tc + 128 + (m >> 1) * 32 + (m & 1) * 4);
                wsm[m] = *((const LAS float*)(size_t)tw + (m >> 1) * 32 + (m & 1) * 4);
            }
            const unsigned kv = ctl1[4];
            tg = ctl1[0];
            SB();
            sk += (int)kv; st ^= (int)kv; so += (int)kv; sf += (int)kv;
            kx = (unsigned)__builtin_amdgcn_readfirstlane((int)kv);
#pragma unroll
            for (int nn = 0; nn < 4; ++nn)
#pragma unroll
                for (int mp = 0; mp < 2; ++mp) {
                    const f32x4 u = acc[2 * mp][nn] * rd[2 * mp] - mr[2 * mp] * wsm[nn];
                    const f32x4 v = acc[2 * mp + 1][nn] * rd[2 * mp + 1] - mr[2 * mp + 1] * wsm[nn];
                    u32x4 w;
                    w[0] = pk_bf16(u[0], u[1]); w[1] = pk_bf16(u[2], u[3]);
                    w[2] = pk_bf16(v[0], v[1]); w[3] = pk_bf16(v[2], v[3]);
                    ST16_SC1(w, rW, sk + (nn * 2 + mp) * 1024);
                    *(LAS u32x4*)((LAS unsigned char*)(size_t)ta + (nn * 2 + mp) * 1024) = w;
                }
        }
        if (wid == 0) STAMP(26);
        WAIT_LGKM0();
        BARRIER();
        SB();
        {
#pragma unroll
            for (int i = 0; i < 16; ++i) { acc[i >> 2][i & 3] = (f32x4){0.f, 0.f, 0.f, 0.f}; asm volatile("" : "+v"(acc[i >> 2][i & 3])); }
            sel[0] = (u32x4){one[0], one[1], 0u, 0u}; sel[1] = (u32x4){0u, 0u, one[0], one[1]};
            asm volatile("" : "+v"(st), "+v"(so), "+v"(sf), "+v"(tg), "+v"(sel[0]), "+v"(sel[1]));
            SB();
            WAIT_VM(0);
            __builtin_amdgcn_raw_buffer_store_b32(tg, rW, st, 0, 16);
            SB();
            if (wid == 0) STAMP(27);
        }
        compute_loop<true, false>(lds, acc, h == 0 ? 32 + KSHIFT : 32 - KSHIFT, wp, wq, lane, [&]() {
            if (wid == 0) { STAMP(h == 0 ? 1 : 19); CSTAMP(22); }
        });
        P2_DEFS;
        if (wid == 0) { STAMP(h == 0 ? 2 : 12); CSTAMP(23); }
        u32x4 hq[4][2];
        {
            bf16x8 ef[4][2];
#pragma unroll
            for (int i = 0; i < 8; ++i) ef[i >> 1][i & 1] = *(const LAS bf16x8*)((const LAS unsigned char*)(size_t)ea[i & 1] + (i >> 1) * 4096);
            SB();
#pragma unroll
            for (int i = 0; i < 16; ++i)
                acc[i & 3][i >> 2] = __builtin_amdgcn_mfma_f32_16x16x32_bf16(__builtin_bit_cast(bf16x8, sel[i & 1]), ef[i >> 2][(i >> 1) & 1], acc[i & 3][i >> 2], 0, 0, 0);
#pragma unroll
            for (int nn = 0; nn < 4; ++nn)
#pragma unroll
                for (int mp = 0; mp < 2; ++mp)
#pragma unroll
                    for (int i = 0; i < 4; ++i) {
                        const f32x4 c = acc[2 * mp + (i >> 1)][nn];
                        hq[nn][mp][i] = pk_bf16(c[(i & 1) * 2], c[(i & 1) * 2 + 1]);
                    }
        }
        if (__builtin_expect(h == 0, 1)) {
            f32x4 d1[4], d2[4];
#pragma unroll
            for (int nn = 0; nn < 4; ++nn) {
                d1[nn] = d2[nn] = (f32x4){0.f, 0.f, 0.f, 0.f};
#pragma unroll
                for (int mp = 0; mp < 2; ++mp) {
                    const bf16x8 hf = __builtin_bit_cast(bf16x8, hq[nn][mp]);
                    d1[nn] = __builtin_amdgcn_mfma_f32_16x16x32_bf16(__builtin_bit_cast(bf16x8, aw[mp]), hf, d1[nn], 0, 0, 0);
                    d2[nn] = __builtin_amdgcn_mfma_f32_16x16x32_bf16(hf, hf, d2[nn], 0, 0, 0);
                }
            }
#pragma unroll
            for (int nn = 0; nn < 4; ++nn) {
                u32x3 sv;
                sv[0] = min(__float_as_uint(d1[nn][0]), SENT - 1u);
                sv[1] = min(__float_as_uint(sel4(n, d2[nn][0], d2[nn][1], d2[nn][2], d2[nn][3])), SENT - 1u);
                sv[2] = min(__float_as_uint(d1[nn][1]), SENT - 1u);
                __builtin_amdgcn_raw_buffer_store_b96(sv, rW, so + nn * 256, 0, 16);
            }
            if (wid == 0) STAMP(3);
#pragma unroll
            for (int nn = 0; nn < 4; ++nn)
#pragma unroll
                for (int mp = 0; mp < 2; ++mp)
                    *(LAS u32x4*)(lds + L_HT + (wq * 64 + nn * 16 + n) * 256 + (((wp * 8 + mp * 4 + g) ^ n) * 16)) = hq[nn][mp];
            WAIT_LGKM0();
            BARRIER();
            BARRIER();
            if (wid == 0) STAMP(6);
            const int crow = (int)CTL(2 + wq) + wq * 64;
            const __amdgpu_buffer_rsrc_t rOut = mk_rsrc(a.out + (size_t)rt * BT * D_LEN, BT * D_LEN * 2);
#pragma unroll 2
            for (int j = 0; j < 8; ++j) {
                const int r = wq * 64 + wp * 32 + j * 4 + g;
                const float m_ = rstat[r * 2], d_ = rstat[r * 2 + 1];
                const u32x4 hh = *(const LAS u32x4*)(lds + L_HT + r * 256 + ((n ^ (r & 15)) * 16));
                u32x4 ov;
#pragma unroll
                for (int i = 0; i < 4; ++i) {
                    const unsigned u = hh[i];
                    const float h0 = bflo(u), h1 = bfhi(u);
                    const unsigned dd = pk_bf16(h0 - m_, h1 - m_);
                    const unsigned q = pk_bf16(bflo(dd) * d_, bfhi(dd) * d_);
                    ov[i] = pk_bf16(h0 + bflo(q), h1 + bfhi(q));
                }
                __builtin_amdgcn_raw_buffer_store_b128(ov, rOut, r == crow ? BT * D_LEN * 2 : (r * D_LEN + ct * BT + n * 8) * 2, 0, OUT_AUX);
            }
            FLUSH();
            return;
        }
#pragma unroll
        for (int i = 0; i < 8; ++i) __builtin_amdgcn_raw_buffer_store_b128(hq[i >> 1][i & 1], rW, so, i * 1024, 16);
        WAIT_VM0();
        __builtin_amdgcn_raw_buffer_store_b32(1u, rW, sf, 0, 16);
        if (wid == 0) STAMP(13);
    } else {
        LAS unsigned char* l0 = lds + (wid - 4) * 4096;
        P2_DEFS;
        const __amdgpu_buffer_rsrc_t rD = mk_rsrc(a.wd + (size_t)sidx * 32 * D_LEN, h == 0 && sidx < 112 && wid < 8 - BQ ? 32 * D_LEN * 2 : 0);
        auto load_wd = [&](int i) {
            dma_nt(rD, (unsigned)(size_t)lds + (i < 8 ? L_WD0 : L_WD1) + (wid & 3) * 8192 + (i & 7) * 1024, wd_off(wid & 3, i, lane), i * (4 * D_LEN));
        };
        const int lw = wid - 4;
        constexpr int NIL = 1 << 30;
        ring.rP = ring.rF = mk_rsrc(w2, (unsigned)WS_STATS);
        ring.voP[0] += (int)WS_AT + at_panel(h * 8 + ct);
        ring.voP[1] = ring.voP[2] = ring.voP[3] = ring.voP[0];
#define FLINE(k) (((k) >> 3) * (24576 - 2 * (int)kx) + ((k) & 7) * 128)
        const int ft = (g & 1) * 65536 + (g >> 1) * 4 + (int)WS_FLAG1 + (lw == 0 ? ct * 1024 : NIL);
        int vall = FLINE(n) + ft + h * 32768;
        ring.voF = (n >> 2) == xq ? vall : NIL;
        ring.fl = lds + L_YB + wq * 16384 + 12288 + wp * 2048 + 256;
        unsigned sa = (unsigned)(size_t)lds + L_YB + 12288 + 256 + lane * 4;
        const unsigned tg = (unsigned)__builtin_amdgcn_readfirstlane((int)ctl1[0]);
        float wsum = __uint_as_float(ctl1[1]);
        ring.lb = lw * 4096;
        int cq[3], cp[3];
        unsigned mk[3];
#pragma unroll
        for (int k = 0; k < 3; ++k) {
            const int nb = xq * 4 + NBR(k + 1);
            cq[k] = nb * 256; cp[k] = at_tile(cq[k], ring.par); mk[k] = 0x10000u << nb;
            asm volatile("" : "+s"(cq[k]), "+s"(cp[k]), "+s"(mk[k]));
        }
        unsigned m2 = (mk[0] | mk[1] | mk[2]) >> 16;
        asm volatile("" : "+s"(m2), "+s"(ring.rP));
        asm volatile("" : "+v"(vall), "+v"(ring.voF), "+v"(ring.voP[0]), "+v"(sa), "+v"(wsum));
        ring.rF = ring.rP;
        ring.voP[1] = ring.voP[2] = ring.voP[3] = ring.voP[0];
        WAIT_VM(8);
        BARRIER();
        SB();
        __amdgpu_buffer_rsrc_t rE = mk_rsrc(h == 0 ? (const void*)w2 : (const void*)a.x, (unsigned)WS_STATS);
        int ve[2];
        auto fetch = [&]() {
            if (wid == 4 && h == 0) STAMP(21);
            LAS unsigned char* base = lds + L_YB + wq * 16384 + wp * 2048;
            const int s2 = h == 0 ? 2048 : 16 * D_LEN * 2;
#pragma unroll
            for (int k = 0; k < 4; ++k) dma_2k(rE, (unsigned)(size_t)base + k * 4096, ve[0], ve[1], k * s2);
        };
#pragma unroll
        for (int j = 0; j < 2; ++j)
            ve[j] = h == 0 ? Y_OFF + lane * 16 : ((rt * BT + wq * 64 + wp * 8 + j * 4 + g) * D_LEN + ct * BT + ((n ^ (wp * 8 + j * 4 + g)) * 8)) * 2 - j * 1024;
        __amdgpu_buffer_rsrc_t rQ2 = mk_rsrc(a.ci + (size_t)(rt * 4 + lw) * 32 * K, 32 * K * 2);
        int vC2 = (ct * 4 + g) * (K * 2) + ((xq * 1024 + 2048 + n * 128) & 4095);
        int vT2 = h == 0 && n < (KSHIFT + 1) / 2 ? FLINE((xq * 4 - 1 - n) & 15) + ft + 32768 : NIL;
        int vY = lane == 0 ? (int)WS_FLAGS + (rt * 8 + ct) * 128 + lw * 4 : NIL;
        int n0 = C2_START + idx % C2_STAG, n1 = (h == 0 ? 32 : NT) - 11 - n0;
        int c8 = (xq * 1024 + 1024) & 4095, p8 = at_tile(c8, ring.par);
        int cK = (xq * 1024 + (32 - KSHIFT) * 128) & 4095, pK = at_tile(cK, ring.par) + at_panel(8);
        asm volatile("" : "+s"(rE), "+s"(rQ2), "+s"(n0), "+s"(n1), "+s"(c8), "+s"(p8), "+s"(cK), "+s"(pK));
        asm volatile("" : "+v"(ve[0]), "+v"(ve[1]), "+v"(vC2), "+v"(vT2), "+v"(vY));
        unsigned have = 0u;
        SB();
        if (T_DELAY) __builtin_amdgcn_s_sleep(T_DELAY);
#define GATE(m) do { if (__builtin_expect(((m) & ~have) != 0u, 0)) ring.gate(tg, sa, m, have); SB(); } while (0)
#define TILE(k, o, st) do { ring.sp = cp[k] + (o) * 256; ring.sq = cq[k] + (o); ring.template issueP<16>(l0 + (st) * STAGE_BYTES); \
                            ring.issueQ(l0 + (st) * STAGE_BYTES); ring.sample(); SB(); } while (0)
        ring.gate(tg, sa, mk[0], have);
        if (wid == 4) STAMP(28);
        ring.sp = cp[0]; ring.template issueP<16>(l0 + 2 * STAGE_BYTES); ring.sample();
        BARRIER();
        GATE(mk[1]);
        ring.sp = cp[1]; ring.template issueP<16>(l0 + 3 * STAGE_BYTES); ring.sample();
        WAIT_VM(6); BARRIER();
        if (wid == 4) STAMP(30);
        GATE(mk[2]);
        TILE(2, 0, 0);
        GATE(m2);
        ring.voF = vall;
        TILE(0, 128, 1);
        WAIT_VM(19); BARRIER();
        TILE(1, 128, 2);
        WAIT_VM(19); BARRIER();
        TILE(2, 128, 3);
        WAIT_VM(19); BARRIER();
#undef GATE
#undef TILE
        if (wid == 4) STAMP(29);
        if (have != 0xffffffffu) ring.gate(tg, sa, 0xffffffffu, have);
        ring.fl -= 256;
        ring.sq = c8; ring.sp = p8; ring.poff = 0;
        if (n0 > 0) ring.template steady<true, false>(n0);
        ring.voF = vC2;
        ring.rF = ring.rQ;
        ring.template issue<true>(lds);
        if (__builtin_expect(blockIdx.x == 255, 0) && tid == 256) {
            const u64 want = ((u64)ctl1[3] << 32) | ctl1[2];
            __hip_atomic_store((GAS u64*)(ws + WS_TAG), want, __ATOMIC_RELAXED, __HIP_MEMORY_SCOPE_AGENT);
        }
        ring.voF = vT2;
        ring.rF = ring.rP;
        ring.fl += 512;
        ring.sample();
        WAIT_VM(18); BARRIER();
        if (wid == 4) { CSTAMP(34); STAMP(36); }
        ring.template steady<true, false, 18>(2);
        ring.template steady<true, false>(n1);
        if (wid == 4) { CSTAMP(35); STAMP(37); }
        if (h == 0) {
            have = 0u;
            ring.gate(tg, sa + 256, ((1u << ((KSHIFT + 1) / 2)) - 1u) * 0x10001u, have);
            ring.voF = vY;
            ring.fl -= 512;
            ring.rQ = rQ2;
            ring.sq = cK; ring.sp = pK; ring.poff = at_panel(8);
            ring.template steady<true, true>(KSHIFT);
        }
        if (h != 0 || ring.seen()) {
            fetch();
            WAIT_VM(16); BARRIER();
            WAIT_VM(8); BARRIER();
        } else {
            ring.sample();
            WAIT_VM(10); BARRIER();
            const bool have = ring.seen();
            if (have) { fetch(); WAIT_VM(9); } else { ring.sample(); WAIT_VM(2); }
            BARRIER();
            if (!have) {
#pragma unroll 1
                for (int spin = 0; spin <= SPIN_CAP && !ring.seen(); ++spin) { ring.sample(); WAIT_VM(0); }
                fetch();
            }
        }
        if (wid == 4 && h == 0) STAMP(20);
        WAIT_VM0();
        BARRIER();
        if (h == 0) {
            const int slot = sidx;
            LAS unsigned char* abuf = lds + L_ATAB + 2048 + lw * 128;
#pragma unroll
            for (int i = 0; i < WD_EARLY; ++i) load_wd(i);
            SB();
            BARRIER();
            __builtin_amdgcn_s_setprio(TAIL_PRIO);
            if (lane < 16) *(LAS unsigned*)(abuf + 64 + lane * 4) = 0u;
            __builtin_amdgcn_s_sleep(STAT_DELAY);
            if (wp == 0) {
                const __amdgpu_buffer_rsrc_t rS = mk_rsrc(w2 + WS_STATS + (hp_ & 15) * 32768 + (hp_ >> 4) * 8192, 24576);
                __amdgpu_buffer_rsrc_t rC = mk_rsrc(w2 + WS_CAND + (size_t)hp_ * 4096, 4096);
                __amdgpu_buffer_rsrc_t rKy = mk_rsrc(w2 + WS_KEYS + hp_ * 4, ct == 0 ? 8192 + 3 * 256 + 4 : 0);
                asm volatile("" : "+s"(rC), "+s"(rKy));
                float s1, s2, s3;
#pragma unroll 1
                for (int spin = 0;; ++spin) {
                    asm volatile("" ::: "memory");
                    u32x3 sv[16];
#pragma unroll
                    for (int j = 0; j < 16; ++j) sv[j] = __builtin_amdgcn_raw_buffer_load_b96(rS, lane * 16, (j >> 3) * 16384 + (j & 7) * 1024, 16);
                    SB();
                    s1 = 0.f; s2 = 0.f; s3 = 0.f;
#pragma unroll
                    for (int j = 0; j < 16; ++j) {
                        const unsigned u0 = sv[j][0], u1 = sv[j][1], u2 = sv[j][2];
                        s1 += __uint_as_float(u0); s2 += __uint_as_float(u1); s3 += __uint_as_float(u2);
                    }
                    const float z = (s1 - s1) + (s2 - s2) + (s3 - s3);
                    if (__builtin_expect(__all(z == 0.f), 1) || spin > SPIN_CAP) break;
                    unsigned mx = 0u;
#pragma unroll
                    for (int j = 0; j < 16; ++j) mx = max(max(mx, sv[j][0]), max(sv[j][1], sv[j][2]));
                    if (__all(mx != SENT)) break;
                }
                if (wid == 4) STAMP(4);
                const float mean = s1 * (1.0f / D_LEN);
                const float var = fmaxf(s2 * (1.0f / D_LEN) - mean * mean, 0.f);
                const float mb = rbf(mean), rden = __builtin_amdgcn_rcpf(rbf(__builtin_amdgcn_sqrtf(rbf(rbf(var) + 1e-5f))));
                const unsigned kb = wave_max_u32(r2key((s3 - mb * wsum) * rden, hp_ * 64 + lane));
                __builtin_amdgcn_raw_buffer_store_b32(kb, rKy, (lane >> 2) * 8192 + (lane & 3) * 256, 0, 16);
                {
                    const int cr_ = __builtin_amdgcn_readfirstlane((int)((0xffffu - (kb & 0xffffu)) & 63u));
                    const float m_ = __int_as_float(__builtin_amdgcn_readlane(__float_as_int(mb), cr_));
                    const float d_ = __int_as_float(__builtin_amdgcn_readlane(__float_as_int(rden), cr_));
                    u32x2 c = *(const LAS u32x2*)(lds + L_HT + (wq * 64 + cr_) * 256 + (((lane & 31) * 8) ^ ((cr_ & 15) * 16)));
#pragma unroll
                    for (int i = 0; i < 2; ++i) {
                        const unsigned u = c[i];
                        const unsigned dd = pk_bf16(bflo(u) - m_, bfhi(u) - m_);
                        const unsigned q = pk_bf16(bflo(dd) * d_, bfhi(dd) * d_);
                        c[i] = min(lane < 32 ? q : u, SENT - 1u);
                    }
                    __builtin_amdgcn_raw_buffer_store_b64(c, rC, (lane < 32 ? 2048 : 0) + ct * 256 + (lane & 31) * 8, 0, 16);
                }
                rstat[(wq * 64 + lane) * 2] = mb; rstat[(wq * 64 + lane) * 2 + 1] = rden;
                if (lane == 0) ctl[2 + wq] = (0xffffu - (kb & 0xffffu)) & 63u;
                WAIT_LGKM0();
                if (wid == 4) STAMP(5);
            }
            BARRIER();
#pragma unroll
            for (int i = WD_EARLY; i < 16; ++i) load_wd(i);
            if (slot < 112 && wid < 8 - BQ) {
                WAIT_VM0();
                if (wid == 4) STAMP(7);
                stage2(w2, slot, lw, lds + L_WD0 + lw * 8192, lds + L_WD1 + lw * 8192, abuf, lane);
                if (wid == 4) STAMP(11);
            }
            FLUSH();
            return;
        }
    }

    P2_DEFS;
    LAS float* red = (LAS float*)(lds + L_RED);
    if (sidx < 112) {
        const int slot = sidx, grp = slot >> 3, kq = slot & 7;
        const __amdgpu_buffer_rsrc_t rG = mk_rsrc(a.wg, D_LEN * F_LEN * 2), rU = mk_rsrc(a.wu, D_LEN * F_LEN * 2);
        auto pair = [&](int i) {
            const int d = wid * 16 + i * 2 + (lane >> 5), ky = (d & 3) | ((d >> 1) & 4), c = lane & 31;
            const int vgu = ((kq * 128 + d) * F_LEN + grp * 256 + (((c >> 1) ^ ky) * 16) + (c & 1) * 8) * 2;
            dma_nt(rG, (unsigned)(size_t)lds + wid * 8192 + i * 1024, vgu, 0);
            dma_nt(rU, (unsigned)(size_t)lds + 65536 + wid * 8192 + i * 1024, vgu ^ 512, 0);
        };
        if (wid >= 4) {
            __builtin_amdgcn_s_sleep(W_PRE_SL);
#pragma unroll
            for (int i = 0; i < W_PRE; ++i) pair(i);
        }
        BARRIER();
        if (wid < 4) {
#pragma unroll
            for (int i = 0; i < W_PRE; ++i) pair(i);
        }
#pragma unroll
        for (int i = W_PRE; i < 8; ++i) pair(i);
        if (BQ && wid == 7) {
            const __amdgpu_buffer_rsrc_t rD = mk_rsrc(a.wd + (size_t)slot * 32 * D_LEN, 32 * D_LEN * 2);
#pragma unroll
            for (int i = 0; i < 16; ++i)
                dma_nt(rD, (unsigned)(size_t)lds + L_YB + 16384 + i * 1024, wd_off(3, i, lane), i * (4 * D_LEN));
            if (lane < 16) *(LAS unsigned*)(lds + L_YB + 4096 + 64 + lane * 4) = 0u;
        }
        const __amdgpu_buffer_rsrc_t rK = mk_rsrc(w2, wid == 0 ? (unsigned)(WS_CAND + 32 * 4096) : 0u);
        const int ok_ = lane < 40 ? (int)WS_KEYS + (xcd >> 2) * 8192 + (xcd & 3) * 256 + (lane - 32) * 16 : (int)(WS_CAND + 32 * 4096);
        const int oc = (int)WS_CAND + 2048 + kq * 256 + n * 16;
        int best = 0;
        u32x4 v = {0u, 0u, 0u, 0u};
        auto request = [&]() {
            asm volatile("" ::: "memory");
            v = LD16_SC1(rK, lane < 32 ? oc + best * 4096 : ok_);
            SB();
        };
        auto settled = [&]() {
            const unsigned mx = umax4(v), mn = min(min(v[0], v[1]), min(v[2], v[3]));
            unsigned fv = lane < 32 ? ~mx : mn | (unsigned)(lane >> 3 ^ 4);
            asm volatile("" : "+v"(fv));
            const bool fine = fv != 0u;
            unsigned km = max(mx, dppu<0xB1>(mx));
            km = max(km, dppu<0x4E>(km));
            km = max(km, dppu<0x141>(km));
            const int nb = (int)(((0xffffu - ((unsigned)__builtin_amdgcn_readlane((int)km, 32) & 0xffffu)) >> 6) & 31u);
            const bool same = nb == best;
            best = nb;
            return __all(fine) && same;
        };
        if (wid == 0) {
            request();
            WAIT_VM(0);
            settled();
            request();
        } else if (BQ && wid == 7) WAIT_VM(16);
        else WAIT_VM(0);
        BARRIER();
        if (wid == 0) STAMP(8);
        bf16x8 wfr[4][4];
        {
            const int q = (lane >> 2) & 3, ky = q | ((g & 1) << 2);
#pragma unroll
            for (int u = 0; u < 4; ++u) {
                const int tro = (u >> 1) * 65536 + (8 * g + q) * 512 + (((wid * 2 + (u & 1)) ^ ky) * 32) + (lane & 3) * 8;
#pragma unroll
                for (int ks = 0; ks < 4; ++ks) {
                    const bf16x4 w0 = __builtin_amdgcn_ds_read_tr16_b64_v4i16((LAS bf16x4*)(lds + tro + ks * 16384));
                    const bf16x4 w1 = __builtin_amdgcn_ds_read_tr16_b64_v4i16((LAS bf16x4*)(lds + tro + ks * 16384 + 2048));
                    wfr[u][ks] = __builtin_shufflevector(w0, w1, 0, 1, 2, 3, 4, 5, 6, 7);
                }
            }
            SB();
#pragma unroll
            for (int i = 0; i < 16; ++i) asm volatile("" : "+v"(wfr[i >> 2][i & 3]));
        }
        const __amdgpu_buffer_rsrc_t rPo = mk_rsrc(w2 + WS_PRE, 112 * 2048);
        int po = n < 4 ? pre_off((grp ^ (n >> 1)) * 8 + wid) + kq * 256 + n * 64 + g * 16 : 112 * 2048;
        asm volatile("" : "+v"(po));
        if (wid == 0) {
#pragma unroll 1
            for (int spin = 0; spin <= SPIN_CAP && !settled(); ++spin) {
                __builtin_amdgcn_s_sleep(1);
                request();
            }
            __builtin_amdgcn_s_waitcnt(0x0F70);
            if (lane < 16) *(LAS u32x4*)(lds + L_RED + lane * 16) = v;
            WAIT_LGKM0();
            STAMP(9);
        }
        BARRIER();
        {
            f32x4 ac[4];
            bf16x8 xf[4];
#pragma unroll
            for (int ks = 0; ks < 4; ++ks) xf[ks] = *(const LAS bf16x8*)(lds + L_RED + ks * 64 + g * 16);
            SB();
#pragma unroll
            for (int u = 0; u < 4; ++u) ac[u] = (f32x4){0.f, 0.f, 0.f, 0.f};
#pragma unroll
            for (int ks = 0; ks < 4; ++ks)
#pragma unroll
                for (int u = 0; u < 4; ++u) ac[u] = __builtin_amdgcn_mfma_f32_16x16x32_bf16(wfr[u][ks], xf[ks], ac[u], 0, 0, 0);
            u32x4 sv;
#pragma unroll
            for (int r = 0; r < 4; ++r) sv[r] = min(__float_as_uint(sel4(n, ac[0][r], ac[1][r], ac[2][r], ac[3][r])), SENT - 1u);
            ST16_SC1(sv, rPo, po);
            SB();
            asm volatile("" :: "v"(v));
        }
        if (wid == 0) STAMP(14);
        if (BQ && wid == 7) {
            WAIT_VM0();
            stage2(w2, slot, 3, lds + L_YB + 16384, lds + L_YB + 24576, lds + L_YB + 4096, lane);
        }
    } else {
        const int rj = sidx - 112;
        if (wid == 4) {
            unsigned kk;
            const unsigned k = poll_winner(w2, xcd, lane, kk);
            if (lane == 0) ctl[4] = k;
            if (lane < 32) ctl[8 + lane] = kk;
            WAIT_LGKM0();
        }
        BARRIER();
        const unsigned kw = CTL(4);
        if (wid == 0) STAMP(16);
        {
            const unsigned kc = ctl[8 + wid * 4 + g];
            const __amdgpu_buffer_rsrc_t rL = mk_rsrc(w2 + WS_CAND + (size_t)wid * 16384, 16384);
            u32x2 hv2, nv2;
            for (int spin = 0;; ++spin) {
                asm volatile("" ::: "memory");
                hv2 = __builtin_amdgcn_raw_buffer_load_b64(rL, g * 4096 + (rj * 64 + n * 4) * 2, 0, 16);
                nv2 = __builtin_amdgcn_raw_buffer_load_b64(rL, g * 4096 + 2048 + (rj * 64 + n * 4) * 2, 0, 16);
                if (__all(max(max(hv2[0], hv2[1]), max(nv2[0], nv2[1])) != SENT) || spin > SPIN_CAP) break;
                __builtin_amdgcn_s_sleep(1);
            }
            u32x2 ov;
#pragma unroll
            for (int i = 0; i < 2; ++i) {
                const unsigned u = hv2[i], q = nv2[i];
                ov[i] = pk_bf16(bflo(u) + bflo(q), bfhi(u) + bfhi(q));
            }
            const int crow = (int)(0xffffu - (kc & 0xffffu)) & (S_LEN - 1);
            if (kc != kw) *(u32x2*)(a.out + (size_t)crow * D_LEN + rj * 64 + n * 4) = ov;
        }
        const int wrow = (int)(0xffffu - (kw & 0xffffu)) & (S_LEN - 1);
        const __amdgpu_buffer_rsrc_t rC = mk_rsrc(w2 + WS_CAND + (size_t)(wrow >> 6) * 4096, 4096);
        const __amdgpu_buffer_rsrc_t rPt = mk_rsrc(w2 + WS_PART, 112 * 4096);
        const int ocol = (rj >> 2) * 256 + ((lane >> 3) & 3) * 64 + (rj & 3) * 16 + (lane & 7) * 2, opos = (lane & 7) * 8 + ((lane >> 3) & 3);
        unsigned hv, hnv;
        for (int spin = 0;; ++spin) {
            asm volatile("" ::: "memory");
            hv = __builtin_amdgcn_raw_buffer_load_b32(rC, ocol * 2, 0, 16);
            hnv = __builtin_amdgcn_raw_buffer_load_b32(rC, 2048 + ocol * 2, 0, 16);
            if (__all(max(hv, hnv) != SENT) || spin > SPIN_CAP) break;
        }
        float sgm = rbf(1.0f / (1.0f + __expf(-key2r(kw))));
        GAS unsigned* optr = (GAS unsigned*)(a.out + (size_t)wrow * D_LEN + ocol);
        asm volatile("" : "+v"(sgm), "+v"(optr));
        u32x4 pv[4];
#pragma unroll 1
        for (int spin = 0;; ++spin) {
            asm volatile("" ::: "memory");
#pragma unroll
            for (int i = 0; i < 4; ++i) pv[i] = LD16_SC1(rPt, (wid + 8 * i) * 16384 + (rj >> 3) * 8192 + g * 2048 + ((rj & 7) * 64 + n * 4) * 4);
            if (__all(max(max(umax4(pv[0]), umax4(pv[1])), max(umax4(pv[2]), umax4(pv[3]))) != SENT) || spin > SPIN_CAP) break;
            __builtin_amdgcn_s_sleep(1);
        }
        if (wid == 0) STAMP(17);
        f32x4 sm;
#pragma unroll
        for (int j = 0; j < 4; ++j) {
            const unsigned u0 = pv[0][j], u1 = pv[1][j], u2 = pv[2][j], u3 = pv[3][j];
            sm[j] = sum_rows((__uint_as_float(u0) + __uint_as_float(u1)) + (__uint_as_float(u2) + __uint_as_float(u3)));
        }
        if (lane < 16) *(LAS f32x4*)(red + wid * 64 + lane * 4) = sm;
        WAIT_LGKM0();
        BARRIER();
        if (wid == 0) {
            float t0 = 0.f, t1 = 0.f;
#pragma unroll
            for (int w = 0; w < 8; ++w) { t0 += red[w * 64 + opos]; t1 += red[w * 64 + opos + 4]; }
            const float y0 = rbf(rbf(t0) * sgm), y1 = rbf(rbf(t1) * sgm);
            const unsigned o = pk_bf16(bflo(hv) + rbf(bflo(hnv) + y0), bfhi(hv) + rbf(bfhi(hnv) + y1));
            if (lane < 32) *optr = o;
            STAMP(18);
        }
        __builtin_amdgcn_s_waitcnt(0x0F70);
    }
    FLUSH();
}

extern "C" void kernel_launch(void* const* d_in, const int* in_sizes, int n_in, void* d_out, int out_size, void* d_ws,
                              size_t ws_size, hipStream_t stream) {
    Args a;
    a.x = (const bf16_t*)d_in[0];
    a.cr = (const bf16_t*)d_in[1];
    a.ci = (const bf16_t*)d_in[2];
    a.wre = (const bf16_t*)d_in[3];
    a.wim = (const bf16_t*)d_in[4];
    a.w_r = (const bf16_t*)d_in[6];
    a.wg = (const bf16_t*)d_in[7];
    a.wu = (const bf16_t*)d_in[8];
    a.wd = (const bf16_t*)d_in[9];
    a.out = (bf16_t*)d_out;
    a.ws = (char*)d_ws + (WS_END ? ((ws_size - ((size_t)24 << 20)) & ~(size_t)255) : 0);
    (void)hipFuncSetAttribute((const void*)fused_kernel, hipFuncAttributeMaxDynamicSharedMemorySize, LDS_BYTES);
    fused_kernel<<<256, 768, LDS_BYTES, stream>>>(a);
}
```

```cpp
#include <hip/hip_runtime.h>
#include <hip/hip_bf16.h>
#include <stdint.h>

typedef unsigned short bf16_t;
typedef short bf16x8 __attribute__((ext_vector_type(8)));
typedef short bf16x4 __attribute__((ext_vector_type(4)));
typedef float f32x4 __attribute__((ext_vector_type(4)));
typedef unsigned u32x4 __attribute__((ext_vector_type(4)));
typedef unsigned u32x2 __attribute__((ext_vector_type(2)));
typedef unsigned u32x3 __attribute__((ext_vector_type(3)));
typedef unsigned long long u64;
#define LAS __attribute__((address_space(3)))
#define GAS __attribute__((address_space(1)))

constexpr int S_LEN = 2048, D_LEN = 1024, F_LEN = 3584;
constexpr unsigned SENT = 0xFFFFFFFFu;
constexpr int SPIN_CAP = 1 << 17;

constexpr size_t WS_KEYS = 0;
constexpr size_t WS_FLAGS = 16384;
constexpr size_t WS_XRD = 524288;
constexpr size_t WS_FLAG1 = 262144;
constexpr size_t WS_TAG = 131072;
constexpr size_t WS_VOTE = WS_TAG + 4096;
constexpr size_t WS_PROBE = (size_t)20 << 20;
constexpr size_t WS_AT = (size_t)4 << 20;
constexpr size_t WS_Y = (size_t)12 << 20;
constexpr size_t WS_STATS = (size_t)16 << 20;
constexpr size_t WS_PART = WS_STATS + (512u << 10);
constexpr size_t WS_CAND = WS_STATS + (1024u << 10);
constexpr size_t WS_PRE = WS_STATS + (1152u << 10);
constexpr size_t FILL_BYTES = (size_t)1408 << 10;

__device__ __forceinline__ float bflo(unsigned v) { return __uint_as_float(v << 16); }
__device__ __forceinline__ float bfhi(unsigned v) { return __uint_as_float(v & 0xffff0000u); }
__device__ __forceinline__ unsigned pk_bf16(float lo, float hi) {
    typedef __bf16 bf2 __attribute__((ext_vector_type(2)));
    typedef float f2 __attribute__((ext_vector_type(2)));
    f2 v = {lo, hi};
    bf2 r = __builtin_convertvector(v, bf2);
    return __builtin_bit_cast(unsigned, r);
}
__device__ __forceinline__ float rbf(float x) { return bflo(pk_bf16(x, 0.f) & 0xffffu); }

template <int CTRL> __device__ __forceinline__ unsigned dppu(unsigned v) {
    return (unsigned)__builtin_amdgcn_update_dpp(0, (int)v, CTRL, 0xf, 0xf, true);
}
template <int CTRL> __device__ __forceinline__ float dppf(float v) { return __uint_as_float(dppu<CTRL>(__float_as_uint(v))); }
__device__ __forceinline__ void swap16(unsigned v, unsigned& a, unsigned& b) {
    auto r = __builtin_amdgcn_permlane16_swap(v, v, false, false);
    a = r[0]; b = r[1];
}
__device__ __forceinline__ void swap32(unsigned v, unsigned& a, unsigned& b) {
    auto r = __builtin_amdgcn_permlane32_swap(v, v, false, false);
    a = r[0]; b = r[1];
}
__device__ __forceinline__ float sum_rows(float v) {
    unsigned a, b;
    swap16(__float_as_uint(v), a, b); v = __uint_as_float(a) + __uint_as_float(b);
    swap32(__float_as_uint(v), a, b); v = __uint_as_float(a) + __uint_as_float(b);
    return v;
}
__device__ __forceinline__ float wave_sum(float v) {
    v += dppf<0xB1>(v);
    v += dppf<0x4E>(v);
    v += dppf<0x141>(v);
    v += dppf<0x140>(v);
    return sum_rows(v);
}
__device__ __forceinline__ unsigned wave_max_u32(unsigned v) {
    v = max(v, dppu<0xB1>(v));
    v = max(v, dppu<0x4E>(v));
    v = max(v, dppu<0x141>(v));
    v = max(v, dppu<0x140>(v));
    unsigned a, b;
    swap16(v, a, b); v = max(a, b);
    swap32(v, a, b); v = max(a, b);
    return v;
}
__device__ __forceinline__ float sel4(int g, float a, float b, float c, float d) {
    const float t0 = (g & 1) ? b : a, t1 = (g & 1) ? d : c;
    return (g & 2) ? t1 : t0;
}
__device__ __forceinline__ unsigned umax4(u32x4 v) { return max(max(v[0], v[1]), max(v[2], v[3])); }

__device__ __forceinline__ void touch_line(const void* p, unsigned lds_dst) {
    unsigned keep;
    asm volatile("s_mov_b32 %0, m0\n\ts_mov_b32 m0, %2\n\ts_nop 0\n\tglobal_load_lds_dword %1, off\n\ts_mov_b32 m0, %0"
                 : "=&s"(keep) : "v"(p), "s"(lds_dst) : "memory");
}
__device__ __forceinline__ __amdgpu_buffer_rsrc_t mk_rsrc(const void* p, unsigned bytes) {
    return __builtin_amdgcn_make_buffer_rsrc((void*)p, 0, (int)bytes, 0x00020000);
}
#define LDS_LOAD(w, v, o, pol) "buffer_load_" w " " v ", %0, %2 offen" o pol " lds"
#define LDS_M0 "s_mov_b32 m0, %1\n\ts_nop 0\n\t"
__device__ __forceinline__ void dma_nt(__amdgpu_buffer_rsrc_t r, unsigned dst, int voff, int soff) {
    asm volatile(LDS_M0 LDS_LOAD("dwordx4", "%3", "", " nt") :: "s"(r), "s"(dst), "s"(soff), "v"(voff) : "memory", "m0");
}
template <bool SC1> __device__ __forceinline__ void dma_word(__amdgpu_buffer_rsrc_t r, unsigned dst, int voff) {
    if constexpr (SC1) asm volatile(LDS_M0 LDS_LOAD("dword", "%3", "", " sc1") :: "s"(r), "s"(dst), "n"(0), "v"(voff) : "memory", "m0");
    else asm volatile(LDS_M0 LDS_LOAD("dword", "%3", "", "") :: "s"(r), "s"(dst), "n"(0), "v"(voff) : "memory", "m0");
}
__device__ __forceinline__ void dma_2k(__amdgpu_buffer_rsrc_t r, unsigned dst, int v0, int v1, int soff) {
    asm volatile(LDS_M0 LDS_LOAD("dwordx4", "%3", "", " sc1") "\n\t" LDS_LOAD("dwordx4", "%4", " offset:1024", " sc1")
                 :: "s"(r), "s"(dst), "s"(soff), "v"(v0), "v"(v1) : "memory", "m0");
}
template <bool SC1> __device__ __forceinline__ void dma_4k(__amdgpu_buffer_rsrc_t r, unsigned dst, const int (&v)[4], int soff) {
#define FOUR(pol) asm volatile(LDS_M0 LDS_LOAD("dwordx4", "%3", "", pol) "\n\t" LDS_LOAD("dwordx4", "%4", " offset:1024", pol) "\n\t" \
        LDS_LOAD("dwordx4", "%5", " offset:2048", pol) "\n\t" LDS_LOAD("dwordx4", "%6", " offset:3072", pol) \
        :: "s"(r), "s"(dst), "s"(soff), "v"(v[0]), "v"(v[1]), "v"(v[2]), "v"(v[3]) : "memory", "m0")
    if constexpr (SC1) FOUR(" sc1"); else FOUR("");
#undef FOUR
}
#define CODE_BYTES 25856
__device__ __forceinline__ __amdgpu_buffer_rsrc_t code_rsrc() {
    unsigned long long p; unsigned d;
    asm volatile("s_getpc_b64 %0\n1:\n\ts_mov_b32 %1, 1b-_Z12fused_kernel4Args" : "=s"(p), "=s"(d));
    return __builtin_amdgcn_make_buffer_rsrc((void*)(p - d), 0, CODE_BYTES, 0x00020000);
}
#define LD16_SC1(rs, off) __builtin_amdgcn_raw_buffer_load_b128(rs, off, 0, 16)
#define ST16_SC1(v, rs, off) __builtin_amdgcn_raw_buffer_store_b128(v, rs, off, 0, 16)

constexpr int BT = 128, BK = 64, NSTAGE = 4;
constexpr int TILE_BYTES = BT * BK * 2;
constexpr int STAGE_BYTES = 2 * TILE_BYTES;
constexpr int RING_BYTES = NSTAGE * STAGE_BYTES;
constexpr int L_DUMMY = RING_BYTES;
constexpr int L_CTL = RING_BYTES + 256;
constexpr int L_RED = RING_BYTES + 512;

#define WAIT_VM(n) asm volatile("s_waitcnt vmcnt(" #n ")" ::: "memory")
#define WAIT_VM0() do { asm volatile("" ::: "memory"); __builtin_amdgcn_s_waitcnt(0x0F70); asm volatile("s_waitcnt vmcnt(0)" ::: "memory"); } while (0)
#define WAIT_LGKM0() asm volatile("s_waitcnt lgkmcnt(0)" ::: "memory")
#define BARRIER() do { asm volatile("" ::: "memory"); __builtin_amdgcn_s_barrier(); asm volatile("" ::: "memory"); } while (0)

__device__ __forceinline__ int prow(int rho) {
    const int w = rho >> 6, m = (rho >> 4) & 3, q = (rho >> 2) & 3, r = rho & 3;
    return w * 64 + (m >> 1) * 32 + q * 8 + (m & 1) * 4 + r;
}
__device__ __forceinline__ unsigned r2key(float r, int row) {
    const unsigned u = pk_bf16(r, 0.f) & 0xffffu;
    const unsigned o = (u & 0x8000u) ? (~u & 0xffffu) : (u | 0x8000u);
    return (o << 16) | (0xffffu - (unsigned)row);
}
__device__ __forceinline__ float key2r(unsigned k) {
    const unsigned o = k >> 16;
    return bflo((o & 0x8000u) ? (o & 0x7fffu) : (~o & 0xffffu));
}
#define RESET_T 2
#define RESET_U 3
#define VOTE_T 4
#define PSM 0x024600
#define FIN_PRIO 3
#define W_PRE 2
#define W_PRE_SL 20
#define BQ 1
#define POLL_T 12
#define AUX_SLEEP 6
#define WD_EARLY 4
#define TAIL_PRIO 3
#define STAT_DELAY 14
#define CODE_WG 1
#define AUX_STAY 1
#define T_DELAY 0
#define WS_END 1
#define CATCH1 4
#define CPF_MASK1 0x1080u
#define CPF_MASK2 0x80u
#define CPF_STAG 5
#define CPF_FIX 0x1000u
#define OUT_AUX 0
#define POLL_SLEEP 4
__device__ __forceinline__ float dot2bf(unsigned a, unsigned b, float c) {
    typedef __bf16 bf2 __attribute__((ext_vector_type(2)));
    return __builtin_amdgcn_fdot2_f32_bf16(__builtin_bit_cast(bf2, a), __builtin_bit_cast(bf2, b), c, false);
}

#define STAMP(i) do { } while (0)
#define CSTAMP(i) do { } while (0)
#define FLUSH() do { } while (0)
#define TSV do { } while (0)

#define KSHIFT 4

constexpr int L_HT = ((32 + KSHIFT) % NSTAGE) * STAGE_BYTES;
constexpr int L_WD0 = ((33 + KSHIFT) % NSTAGE) * STAGE_BYTES;
constexpr int L_WD1 = ((34 + KSHIFT) % NSTAGE) * STAGE_BYTES;
constexpr int L_YB = RING_BYTES;
constexpr int L_ATAB = L_YB;
constexpr int LDS_BYTES = RING_BYTES + 32768;

#define SB() __builtin_amdgcn_sched_barrier(0)
template <int N> __device__ __forceinline__ void wait_vm() { asm volatile("s_waitcnt vmcnt(%0)" :: "n"(N) : "memory"); }
__device__ __forceinline__ int at_tile(int c, int par) { return ((c << 8) & 0x78000) | (((c << 2) & 0x2000) ^ par); }
__device__ __forceinline__ int at_panel(int ep) { return ep << 19; }
__device__ __forceinline__ int pre_off(int slot) { const int s_ = slot >= 56, l = slot - 56 * s_; return (l >> 2) * 16384 + s_ * 8192 + (l & 3) * 2048; }
static_assert(KSHIFT >= 2 && KSHIFT <= 4 && KSHIFT % 2 == 0, "flag samples must have replaced older contents of their slot; even tile counts");
struct Ring {
    __amdgpu_buffer_rsrc_t rP, rQ, rF, rQ2;
    int voP[4], voQ[4], voF;
    LAS unsigned char* fl;
    unsigned lb;
    int sp, sq;
    int poff, par;
    int tp;
    int tq, ti;
    bool img;
    template <int LD, bool PIMG> __device__ __forceinline__ void setup(int lw, int lane) {
        const int r = lane >> 3, v0 = (PIMG ? r : (r & 3) + (r & 4) * 2) * (LD * 2) + (((lane & 7) ^ (lane >> 4)) << 4);
#pragma unroll
        for (int j = 0; j < 4; ++j) {
            voQ[j] = (v0 ^ ((j & 1) * 64)) + (PIMG ? j * 8 : (j & 1) * 16 + (j >> 1) * 4) * (LD * 2) - j * 1024;
            voP[j] = PIMG ? (lw >> 1) * 16384 + (lw & 1) * 4096 + lane * 16 : voQ[j];
        }
        img = PIMG;
    }
    __device__ __forceinline__ void seek(int col, int pof) {
        sq = col; poff = pof;
        sp = (img ? at_tile(sq, par) : sq) + poff;
    }
    __device__ __forceinline__ void step() {
        sq = (sq + BK * 2) & 4095;
        sp = (img ? at_tile(sq, par) : sq) + poff;
        asm volatile("" : "+s"(sq), "+s"(sp));
    }
    __device__ __forceinline__ void sample() { dma_word<true>(rF, (unsigned)(size_t)fl, voF); }
    __device__ __forceinline__ unsigned peek(unsigned addr) {
        unsigned v;
        asm volatile("ds_read_b32 %0, %1\n\ts_waitcnt lgkmcnt(0)" : "=v"(v) : "v"(addr) : "memory");
        return v;
    }
    __device__ __forceinline__ bool seen() { return __builtin_amdgcn_readfirstlane(peek((unsigned)(size_t)fl)) == 1u; }
    __device__ __forceinline__ void gate(unsigned want, unsigned sa, unsigned need, unsigned& have) {
#pragma unroll 1
        for (int spin = 0; spin <= SPIN_CAP && (need & ~have) != 0u; ++spin) {
            if (spin) { sample(); WAIT_VM(0); __builtin_amdgcn_s_sleep(1); }
            const unsigned long long m = __ballot(peek(sa) == want);
            have = (unsigned)m & (unsigned)(m >> 32);
        }
    }
    template <int AUX> __device__ __forceinline__ void issueP(LAS unsigned char* base) { dma_4k<AUX != 0>(rP, (unsigned)(size_t)base, voP, sp); }
    __device__ __forceinline__ void issueQ(LAS unsigned char* base) { dma_4k<false>(rQ, (unsigned)(size_t)base + TILE_BYTES, voQ, sq); }
    __device__ __forceinline__ void issue(LAS unsigned char* lds) {
        issueP<0>(lds + lb);
        issueQ(lds + lb);
        SB();
        lb = (lb + STAGE_BYTES) & (RING_BYTES - 1);
        asm volatile("" : "+s"(lb));
        SB();
    }
    __device__ __forceinline__ void next1() {
        ++ti;
        sp = sq = __builtin_amdgcn_readlane(tq, ti);
        asm volatile("s_nop 4" : "+s"(sq), "+s"(sp), "+s"(ti));
        SB();
    }
    template <int CAT> __device__ __forceinline__ void ramp(LAS unsigned char* lds) {
#pragma unroll 1
        for (int i = 1; i <= CAT + 1; ++i) {
            for (int z = (PSM >> (4 * i)) & 15; z > 0; --z) __builtin_amdgcn_s_sleep(1);
            issue(lds);
            next1();
            if (i == CAT + 1) continue;
            WAIT_VM(8);
            BARRIER();
        }
    }
    template <bool P2, bool SAMP, int W = 16> __device__ __forceinline__ void steady(int cnt) {
        int t, u;
#define LD(v, r, s, o) "buffer_load_dwordx4 " v ", " r ", " s " offen" o " lds\n\t"
        if constexpr (P2) {
#define PL(SMP) "s_mov_b32 m0, %0\n\ts_branch 2f\n1:\n\ts_waitcnt vmcnt(%16)\n\ts_barrier\n2:\n\t" \
            LD("%8", "%6", "%2", " sc1") "s_and_b32 %4, %1, 0xfff\n\ts_add_i32 %3, %3, 1\n\t" LD("%8", "%6", "%2", " offset:1024 sc1") "s_mov_b32 %5, %1\n\t" \
            LD("%8", "%6", "%2", " offset:2048 sc1") "v_readlane_b32 %1, %13, %3\n\t" LD("%8", "%6", "%2", " offset:3072 sc1") \
            SMP "s_add_i32 m0, %0, 0x4000\n\ts_add_i32 %0, %0, 0x8000\n\t"
#define QL(r) LD("%9", r, "%4", "") "v_readlane_b32 %2, %14, %3\n\ts_and_b32 %0, %0, 0x1ffff\n\t" LD("%10", r, "%4", " offset:1024") "s_cmp_lg_u32 %3, %15\n\t" \
            LD("%11", r, "%4", " offset:2048") LD("%12", r, "%4", " offset:3072") "s_mov_b32 m0, %0\n\ts_cbranch_scc1 1b\n\t"
#define OPS : "+s"(lb), "+s"(sq), "+s"(sp), "+s"(ti), "=&s"(t), "=&s"(u) \
            : "s"(rP), "s"(rQ), "v"(voP[0]), "v"(voQ[0]), "v"(voQ[1]), "v"(voQ[2]), "v"(voQ[3]), "v"(tq), "v"(tp), "s"(cnt), "n"(W), \
              "s"(rF), "v"(voF), "s"((unsigned)(size_t)fl), "s"(rQ2) : "memory", "scc", "m0"
            if constexpr (SAMP) asm volatile(PL("s_mov_b32 m0, %19\n\ts_nop 0\n\tbuffer_load_dword %18, %17, 0 offen sc1 lds\n\t")
                "s_bitcmp1_b32 %5, 12\n\ts_cbranch_scc0 3f\n\t" QL("%20") "s_branch 4f\n3:\n\t" QL("%7") "4:\n\ts_waitcnt vmcnt(%16)\n\ts_barrier" OPS);
            else asm volatile(PL("") QL("%7") "s_waitcnt vmcnt(%16)\n\ts_barrier" OPS);
#undef PL
#undef QL
#undef OPS
        } else {
            asm volatile("s_mov_b32 m0, %0\n\ts_branch 2f\n1:\n\ts_waitcnt vmcnt(16)\n\ts_barrier\n2:\n\t"
                LD("%6", "%4", "%1", "") LD("%7", "%4", "%1", " offset:1024") LD("%8", "%4", "%1", " offset:2048") LD("%9", "%4", "%1", " offset:3072")
                "s_add_i32 m0, %0, 0x4000\n\ts_mov_b32 %3, %1\n\t"
                LD("%6", "%5", "%3", "") "s_add_i32 %2, %2, 1\n\ts_add_i32 %0, %0, 0x8000\n\t"
                LD("%7", "%5", "%3", " offset:1024") "s_and_b32 %0, %0, 0x1ffff\n\tv_readlane_b32 %1, %10, %2\n\t"
                LD("%8", "%5", "%3", " offset:2048") "s_cmp_lg_u32 %2, %11\n\t"
                LD("%9", "%5", "%3", " offset:3072") "s_mov_b32 m0, %0\n\ts_cbranch_scc1 1b\n\ts_waitcnt vmcnt(16)\n\ts_barrier"
                : "+s"(lb), "+s"(sq), "+s"(ti), "=&s"(t)
                : "s"(rP), "s"(rQ), "v"(voQ[0]), "v"(voQ[1]), "v"(voQ[2]), "v"(voQ[3]), "v"(tq), "n"(D_LEN / BK)
                : "memory", "scc", "m0");
            (void)cnt;
            (void)u;
        }
#undef LD
    }
};

template <bool PLIN, bool FIRST, class Pre>
__device__ __forceinline__ void compute_loop(const LAS unsigned char* lds, f32x4 (&acc)[4][4], int NT, int wp, int wq, int lane, Pre pre) {
    const int fr = lane & 15, fq = lane >> 4, key = (fr >> 1) & 7;
    const unsigned l0 = (unsigned)(size_t)lds;
    const unsigned oP0 = l0 + (PLIN ? wp * 8192 + lane * 16 : (wp * 64 + fr) * 128 + ((fq ^ key) * 16));
    const unsigned oP1 = l0 + (PLIN ? wp * 8192 + 1024 + lane * 16 : (wp * 64 + fr) * 128 + (((4 + fq) ^ key) * 16));
    const unsigned oQ0 = l0 + TILE_BYTES + (wq * 64 + fr) * 128 + ((fq ^ key) * 16);
    const unsigned oQ1 = l0 + TILE_BYTES + (wq * 64 + fr) * 128 + (((4 + fq) ^ key) * 16);
    unsigned aP0 = oP0, aQ0 = oQ0, aP1 = oP1, aQ1 = oQ1;
    bf16x8 p0[4], p1[4], r0[4], r1[4], q0[4], q1[4];
#pragma unroll
    for (int i = 0; i < 16; ++i) asm volatile("" : "+v"(acc[i >> 2][i & 3]));
    asm volatile("" : "+v"(aP0), "+v"(aQ0), "+v"(aP1), "+v"(aQ1));
#define MM(pp, qq, m, n) do { acc[m][n] = __builtin_amdgcn_mfma_f32_16x16x32_bf16(pp[m], qq[n], acc[m][n], 0, 0, 0); SB(); } while (0)
#define RD(dst, i, a, st) do { dst[i] = *(const LAS bf16x8*)((const LAS unsigned char*)(size_t)(a) + (st) * STAGE_BYTES + (i) * 2048); SB(); } while (0)
#define ADR(a) do { a ^= 2 * STAGE_BYTES; asm volatile("" : "+v"(a)); SB(); } while (0)
    __builtin_amdgcn_s_waitcnt(0xC07F);
    SB();
    if (FIRST) BARRIER();
    SB();
    RD(p0, 0, aP0, 0); RD(q0, 0, aQ0, 0); RD(p1, 0, aP1, 0); RD(q1, 0, aQ1, 0); RD(p1, 1, aP1, 0); RD(p0, 1, aP0, 0);
    RD(p0, 2, aP0, 0); RD(p1, 2, aP1, 0); RD(p1, 3, aP1, 0); RD(p0, 3, aP0, 0);
    pre();
    SB();
    int t = NT;
#pragma unroll 1
    do {
        MM(p0, q0, 0, 0); MM(p1, q1, 0, 0); RD(q0, 1, aQ0, 0);
        MM(p1, q1, 1, 0); MM(p0, q0, 1, 0); RD(q1, 1, aQ1, 0);
        MM(p0, q0, 2, 0); MM(p1, q1, 2, 0); RD(q0, 2, aQ0, 0);
        MM(p1, q1, 3, 0); MM(p0, q0, 3, 0); RD(q1, 2, aQ1, 0);
        MM(p0, q0, 3, 1); MM(p1, q1, 3, 1); RD(q0, 3, aQ0, 0);
        MM(p1, q1, 2, 1); MM(p0, q0, 2, 1); RD(q1, 3, aQ1, 0);
        MM(p0, q0, 1, 1); MM(p1, q1, 1, 1);
        MM(p1, q1, 0, 1); MM(p0, q0, 0, 1);
        MM(p0, q0, 0, 2); MM(p1, q1, 0, 2); __builtin_amdgcn_s_waitcnt(0xC07F); SB(); BARRIER(); SB();
        MM(p1, q1, 1, 2); RD(r0, 0, aP0, 1); MM(p0, q0, 1, 2); RD(q0, 0, aQ0, 1);
        MM(p0, q0, 2, 2); RD(r1, 0, aP1, 1); MM(p1, q1, 2, 2); RD(q1, 0, aQ1, 1);
        MM(p1, q1, 3, 2); MM(p0, q0, 3, 2); RD(r1, 1, aP1, 1);
        MM(p0, q0, 3, 3); RD(r0, 1, aP0, 1); MM(p1, q1, 3, 3);
        MM(p1, q1, 2, 3); RD(r0, 2, aP0, 1); MM(p0, q0, 2, 3); RD(r1, 2, aP1, 1);
        MM(p0, q0, 1, 3); MM(p1, q1, 1, 3); RD(r1, 3, aP1, 1);
        MM(p1, q1, 0, 3); RD(r0, 3, aP0, 1); MM(p0, q0, 0, 3);

        MM(r0, q0, 0, 0); MM(r1, q1, 0, 0); RD(q0, 1, aQ0, 1);
        MM(r1, q1, 1, 0); MM(r0, q0, 1, 0); RD(q1, 1, aQ1, 1);
        MM(r0, q0, 2, 0); MM(r1, q1, 2, 0); RD(q0, 2, aQ0, 1);
        MM(r1, q1, 3, 0); MM(r0, q0, 3, 0); RD(q1, 2, aQ1, 1);
        MM(r0, q0, 3, 1); MM(r1, q1, 3, 1); RD(q0, 3, aQ0, 1);
        MM(r1, q1, 2, 1); MM(r0, q0, 2, 1); RD(q1, 3, aQ1, 1);
        MM(r0, q0, 1, 1); t -= 2; asm volatile("" : "+s"(t)); SB(); MM(r1, q1, 1, 1); ADR(aP0);
        MM(r1, q1, 0, 1); ADR(aP1); MM(r0, q0, 0, 1); ADR(aQ0);
        MM(r0, q0, 0, 2); ADR(aQ1); MM(r1, q1, 0, 2); __builtin_amdgcn_s_waitcnt(0xC07F); SB(); BARRIER(); SB();
        MM(r1, q1, 1, 2); RD(p0, 0, aP0, 0); MM(r0, q0, 1, 2); RD(q0, 0, aQ0, 0);
        MM(r0, q0, 2, 2); RD(p1, 0, aP1, 0); MM(r1, q1, 2, 2); RD(q1, 0, aQ1, 0);
        MM(r1, q1, 3, 2); MM(r0, q0, 3, 2); RD(p1, 1, aP1, 0);
        MM(r0, q0, 3, 3); RD(p0, 1, aP0, 0); MM(r1, q1, 3, 3);
        MM(r1, q1, 2, 3); RD(p0, 2, aP0, 0); MM(r0, q0, 2, 3); RD(p1, 2, aP1, 0);
        MM(r0, q0, 1, 3); MM(r1, q1, 1, 3); RD(p1, 3, aP1, 0);
        MM(r1, q1, 0, 3); RD(p0, 3, aP0, 0); MM(r0, q0, 0, 3);
    } while (t != 0);
    SB();
#undef MM
#undef RD
#undef ADR
}

struct Args {
    const bf16_t* x; const bf16_t* wre; const bf16_t* wim; char* ws; const bf16_t* cr; const bf16_t* ci;
    const bf16_t* w_r; const bf16_t* wg; const bf16_t* wu; const bf16_t* wd; bf16_t* out;
};

__device__ __forceinline__ unsigned poll_winner(const char* ws, int rep, int lane, unsigned& mine) {
    GAS unsigned* kp = (GAS unsigned*)(ws + WS_KEYS + (size_t)(rep >> 2) * 8192 + (rep & 3) * 256) + (lane & 31);
    unsigned kw;
    for (int spin = 0;; ++spin) {
            asm volatile("" ::: "memory");
        kw = __hip_atomic_load(kp, __ATOMIC_RELAXED, __HIP_MEMORY_SCOPE_AGENT);
        if (__all(kw != 0u) || spin > SPIN_CAP) break;
        __builtin_amdgcn_s_sleep(POLL_SLEEP);
    }
    mine = kw;
    return __builtin_amdgcn_readfirstlane(wave_max_u32(kw));
}

__device__ __forceinline__ void stage2(char* w2, int slot, int lw, LAS unsigned char* img0, LAS unsigned char* img1, LAS unsigned char* abuf, int lane) {
    const int g = lane >> 4, n = lane & 15;
    const __amdgpu_buffer_rsrc_t rPi = mk_rsrc(w2 + WS_PRE + pre_off(slot), 2048);
    unsigned pp[8];
    auto request = [&]() {
        asm volatile("" ::: "memory");
#pragma unroll
        for (int k = 0; k < 8; ++k) pp[k] = __builtin_amdgcn_raw_buffer_load_b32(rPi, k * 256 + lane * 4, 0, 16);
        SB();
    };
    request();
    bf16x8 wf[16];
    {
        const int q = (lane >> 2) & 3, ky = q | ((g & 1) << 2);
#pragma unroll
        for (int j = 0; j < 16; ++j) {
            LAS unsigned char* t = (g < 2 ? img0 : img1) + (8 * (g & 1) + q) * 512 + (((j & 7) ^ ky) + (j & 8)) * 32 + (lane & 3) * 8;
            const bf16x4 w0 = __builtin_amdgcn_ds_read_tr16_b64_v4i16((LAS bf16x4*)t);
            const bf16x4 w1 = __builtin_amdgcn_ds_read_tr16_b64_v4i16((LAS bf16x4*)(t + 2048));
            wf[j] = __builtin_shufflevector(w0, w1, 0, 1, 2, 3, 4, 5, 6, 7);
        }
        SB();
#pragma unroll
        for (int j = 0; j < 16; ++j) asm volatile("" : "+v"(wf[j]));
    }
    unsigned aa[5], pto = lane * 16;
#pragma unroll
    for (int m = 0; m < 4; ++m) aa[m] = (unsigned)(size_t)abuf + ((n >> 2) == m ? 0 : 64) + g * 16;
    aa[4] = (unsigned)(size_t)abuf + (lane & 31) * 2;
    __amdgpu_buffer_rsrc_t rPt = mk_rsrc(w2 + WS_PART + __builtin_amdgcn_readfirstlane((slot >> 2) * 16384 + (lw >> 1) * 8192 + (slot & 3) * 2048 + (lw & 1) * 1024), 1024);
    asm volatile("" : "+v"(aa[0]), "+v"(aa[1]), "+v"(aa[2]), "+v"(aa[3]), "+v"(aa[4]), "+v"(pto));
    asm volatile("" : "+s"(rPt));
    SB();
#pragma unroll 1
    for (int spin = 0; spin <= SPIN_CAP && !__all(max(max(max(pp[0], pp[1]), max(pp[2], pp[3])), max(max(pp[4], pp[5]), max(pp[6], pp[7]))) != SENT); ++spin) request();
    const float sv = ((__uint_as_float(pp[0]) + __uint_as_float(pp[1])) + (__uint_as_float(pp[2]) + __uint_as_float(pp[3]))) +
                     ((__uint_as_float(pp[4]) + __uint_as_float(pp[5])) + (__uint_as_float(pp[6]) + __uint_as_float(pp[7])));
    unsigned ug, uu;
    swap32(__float_as_uint(sv), ug, uu);
    const float gg = rbf(__uint_as_float(ug)), up = rbf(__uint_as_float(uu));
    const unsigned act = pk_bf16(rbf(gg * __builtin_amdgcn_rcpf(1.0f + __expf(-gg))) * up, 0.f);
    *(volatile LAS unsigned short*)(size_t)aa[4] = (unsigned short)act;
    bf16x8 af[4];
#pragma unroll
    for (int m = 0; m < 4; ++m) af[m] = *(volatile LAS bf16x8*)(size_t)aa[m];
    u32x4 pv;
#pragma unroll
    for (int q = 0; q < 4; ++q) {
        f32x4 d = {0.f, 0.f, 0.f, 0.f};
#pragma unroll
        for (int m = 0; m < 4; ++m) d = __builtin_amdgcn_mfma_f32_16x16x32_bf16(af[m], wf[4 * q + m], d, 0, 0, 0);
        const float e = d[0];
        pv[q] = min(__float_as_uint(e), SENT - 1u);
    }
    ST16_SC1(pv, rPt, pto);
}
__device__ __forceinline__ int wd_off(int lw, int i, int lane) {
    const int r = 2 * (i & 5) + (lane >> 5), ky = (r & 3) | ((r >> 1) & 4), c = lane & 31;
    return (lane >> 5) * (D_LEN * 2) + lw * 512 + (((c >> 1) ^ ky) * 2 + (c & 1)) * 16;
}

__global__ __launch_bounds__(768, 3) void fused_kernel(const Args a) {
    extern __shared__ __attribute__((aligned(16))) unsigned char smem[];
    constexpr int K = S_LEN, NT1 = D_LEN / BK;
    const int tid = threadIdx.x, wid = __builtin_amdgcn_readfirstlane(tid >> 6), lane = tid & 63;
    const int xcd = blockIdx.x & 7, idx = blockIdx.x >> 3;
    TSV;
    LAS unsigned char* lds = (LAS unsigned char*)smem;
    if (idx < CODE_WG && wid < 4) dma_word<false>(code_rsrc(), (unsigned)(size_t)lds + L_DUMMY, tid * 128);
    SB();
    const int h = xcd & 1, xq = xcd >> 1, rt = xq * 4 + (idx & 3), ct = idx >> 2;
    volatile LAS unsigned* ctl1 = (volatile LAS unsigned*)(lds + L_CTL);
#define NBR(k) ((rt + (k)) & 3)
    unsigned kx;
    Ring ring;
    if (__builtin_expect((wid & 12) == 4, 1)) {
        __builtin_amdgcn_s_setprio(3);
        ring.template setup<D_LEN, false>(wid - 4, lane);
        ring.lb = (wid - 4) * 4096;
        auto ord1 = [&](int i) {
            const int q = (i - idx - (idx >> 2)) & 3;
            return ((xq * 4 + h * 8 + i + 4 - (q == 0 ? 0 : q == 2 ? 4 : 2)) & 15) * 128;
        };
        ring.poff = 0;
        ring.sp = ring.sq = ord1(0);
        unsigned ox = (unsigned)(rt * 4 + wid - 4) * (32 * D_LEN * 2), ow = (unsigned)(ct * 4 + wid - 4) * (32 * D_LEN * 2);
        asm volatile("" : "+s"(ring.sq), "+s"(ring.lb), "+s"(ox), "+s"(ow), "+v"(ring.voQ[0]), "+v"(ring.voQ[1]), "+v"(ring.voQ[2]), "+v"(ring.voQ[3]));
        SB();
        ring.sp = ring.sq;
#pragma unroll
        for (int j = 0; j < 4; ++j) ring.voP[j] = ring.voQ[j];
        ring.rP = mk_rsrc((const char*)a.x + ox, 32 * D_LEN * 2);
        ring.rQ = mk_rsrc((const char*)(h == 0 ? a.wre : a.wim) + ow, 32 * D_LEN * 2);
        ring.issue(lds);
        __builtin_amdgcn_s_setprio(0);
        ring.tq = ord1(lane);
        ring.ti = 0;
        ring.next1();
        ring.par = (ct & 4) << 11;
        ring.template ramp<CATCH1>(lds);
        ring.template steady<false, false>(NT1 - 2 - CATCH1);
        ring.template setup<K, true>(wid - 4, lane);
        ring.rQ = mk_rsrc((h == 0 ? a.cr : a.ci) + (size_t)(rt * 4 + wid - 4) * 32 * K, 32 * K * 2);
        ring.seek(rt * 256, 0);
        LAS unsigned char* l0 = lds + (wid - 4) * 4096;
        ring.issueQ(l0); ring.step();
        WAIT_VM(12); BARRIER();
        ring.issueQ(l0 + STAGE_BYTES);
        ring.seek((xq * 4 + NBR(1)) * 256, 0);
        WAIT_VM(8); BARRIER();
        ring.issueQ(l0 + 2 * STAGE_BYTES);
        ring.seek((xq * 4 + NBR(2)) * 256, 0);
        BARRIER();
        ring.issueQ(l0 + 3 * STAGE_BYTES);
        kx = (unsigned)__builtin_amdgcn_readfirstlane((int)ctl1[4]);
        SB();
    }
    char* ws = a.ws + ((0ull - (unsigned long long)a.ws) & 0x1fffffull);
    const int g = lane >> 4, n = lane & 15;
    LAS float* tab = (LAS float*)(lds + L_RED);
    if (wid >= 8) {
        const int aw = wid - 8;
        GAS u64* tagp = (GAS u64*)(ws + WS_TAG);
        if (AUX_SLEEP) __builtin_amdgcn_s_sleep(AUX_SLEEP);
        u64 tag0 = __hip_atomic_load(tagp, __ATOMIC_RELAXED, __HIP_MEMORY_SCOPE_AGENT);
        SB();
        const int rho = (aw < 2 ? ct : (xq & 1) * 4 + (idx & 3)) * 16 + (aw & 1) * 8 + (n >> 1);
        unsigned fa = (unsigned)(size_t)lds + (aw >> 1) * TILE_BYTES + rho * 128 + ((((n & 1) * 4 + g) ^ ((rho >> 1) & 7)) * 16);
        constexpr int NIL = 1 << 30;
        const int at = tid - 512;
        __amdgpu_buffer_rsrc_t rZ = mk_rsrc(ws, 18u << 20);
        int z0 = (int)WS_STATS + blockIdx.x * (int)(FILL_BYTES / 256) + at * 16;
        int z1 = aw < 2 ? (at < (int)(FILL_BYTES / 4096) - 256 ? z0 + 4096 : NIL) : aw == 2 ? (lane < 10 ? blockIdx.x * 160 + lane * 16 : NIL)
               : lane < 32 ? (int)WS_FLAG1 + (lane >> 4) * 65536 + h * 32768 + (rt >> 3) * 16384 + ((lane >> 3) & 1) * 8192 + ct * 1024 + (rt & 7) * 128 + (lane & 7) * 16
               : NIL;
        u32x4 zv = {SENT, SENT, SENT, SENT};
        asm volatile("" : "+v"(fa), "+v"(z0), "+v"(z1), "+v"(zv), "+s"(rZ));
        f32x4 d1 = {0.f, 0.f, 0.f, 0.f}, d2 = {0.f, 0.f, 0.f, 0.f};
        u32x4 ones = {0x3F803F80u, 0x3F803F80u, 0x3F803F80u, 0x3F803F80u};
        asm volatile("" : "+v"(ones), "+v"(d1), "+v"(d2));
        if (aw == 0) {
            const int j = lane < 4 ? ct * 4 + lane : (idx & 3) * 8 + lane - 4;
            dma_word<true>(rZ, (unsigned)(size_t)lds + L_DUMMY, lane >= 12 ? NIL : (int)WS_XRD + (j >> 4) * 65536 + (lane < 4 ? rt : 16 + h * 8 + ct) * 2048 + (j & 15) * 128);
        }
        const char* cb;
        __amdgpu_buffer_rsrc_t rR, rS, rV;
        unsigned mask, tb, wl, wh;
        int ti, ro, o0, o1;
        auto warm = [&](int t) { if (t >= 4 && ((mask >> t) & 1)) { touch_line(cb + (ti & 7) * 512, L_DUMMY); ti += 2; } };
        u32x4 w0v, w1v;
        float wsr = 0.f;
#pragma unroll
        for (int t = 0; t < 8; ++t) {
            SB();
            BARRIER(); SB();
            const bf16x8 f = *(const LAS bf16x8*)((const LAS unsigned char*)(size_t)fa + (t & 1) * STAGE_BYTES);
            SB();
            if (aw == 1) { STAMP(40 + t); if (!(t & 1)) CSTAMP(57 + t / 2); }
            if (t == 0) {
                rR = mk_rsrc(a.w_r, aw == 3 && h == 0 ? D_LEN * 2 : 0);
                rS = mk_rsrc(ws + WS_XRD, 131072);
                rV = mk_rsrc(ws + WS_VOTE, 512);
                asm volatile("" : "+s"(rR), "+s"(rS), "+s"(rV));
            }
            if (t == 1) {
                cb = (const char*)(h ? a.ci : a.cr) + (size_t)rt * (512u << 10) + ct * 65536 + (lane >> 2) * 4096 + (lane & 3) * 128;
                mask = aw == 1 ? CPF_MASK1 : aw == 2 ? CPF_MASK2 : 0u;
                mask = (mask & CPF_FIX) | ((mask & ~CPF_FIX) << (idx % CPF_STAG));
                ti = 2 * xq + aw - 1;
                asm volatile("" : "+v"(cb), "+s"(mask), "+s"(ti));
            }
            if (t == 2) {
                w0v = __builtin_amdgcn_raw_buffer_load_b128(rR, lane * 16, 0, 0); w1v = __builtin_amdgcn_raw_buffer_load_b128(rR, lane * 16, 1024, 0);
                ro = (n | 2) != g * 4 + 2 ? 131072 : (aw < 2 ? h * 65536 + rt * 2048 : (xq >> 1) * 65536 + (16 + h * 8 + ct) * 2048) + rho * 16;
                asm volatile("" : "+v"(ro));
            }
            if (t == 3) {
                const int hi = aw == 3 ? 64 : 0;
                o0 = rt * 2048 + (hi + lane) * 16;
                o1 = o0 + (16 + h * 8 + ct - rt) * 2048;
                tb = (unsigned)(size_t)(tab + prow(hi + lane));
                asm volatile("" : "+v"(o0), "+v"(o1), "+v"(tb));
            }
            if (t == RESET_T) ST16_SC1(zv, rZ, z0);
            if (t == RESET_U) { if (aw >= 2) zv = (u32x4){0u, 0u, 0u, 0u}; ST16_SC1(zv, rZ, z1); }
            warm(t);
            if (t & 1) fa ^= 2 * STAGE_BYTES;
            d1 = __builtin_amdgcn_mfma_f32_16x16x32_bf16(f, __builtin_bit_cast(bf16x8, ones), d1, 0, 0, 0);
            if (aw < 2) d2 = __builtin_amdgcn_mfma_f32_16x16x32_bf16(f, f, d2, 0, 0, 0);
            if (t == 5) {
#pragma unroll
                for (int i = 0; i < 4; ++i) { wsr = dot2bf(w0v[i], 0x3F803F80u, wsr); wsr = dot2bf(w1v[i], 0x3F803F80u, wsr); }
                asm volatile("" : "+v"(wsr));
            }
            if (t == 1) {
                asm volatile("" : "+v"(tag0));
                const u64 want = tag0 * 6364136223846793005ull + 1442695040888963407ull;
                wl = (unsigned)want; wh = (unsigned)(want >> 32);
                asm volatile("" : "+v"(wl), "+v"(wh));
            }
            if (t == VOTE_T && aw == 3 && idx < 8) __builtin_amdgcn_raw_buffer_store_b64((u32x2){ctl1[5], wl}, rV, blockIdx.x * 8 + lane * 512, 0, 16);
            if (t == 6) WAIT_VM0();
            asm volatile("" : "+v"(d1), "+v"(d2), "+v"(fa));
        }
        SB(); BARRIER(); SB();
        {
            const float sa = d1[0] + d1[1], sb = d1[2] + d1[3];
            float sq = sel4(n, d2[0], d2[1], d2[2], d2[3]);
            sq += dppf<0xB1>(sq);
            const u32x4 rec = {__float_as_uint((n & 2) ? sb : sa), wl, __float_as_uint(sq), wh};
            ST16_SC1(rec, rS, ro);
        }
        if (aw == 1) { STAMP(48); CSTAMP(61); }
        if (aw == 1 || aw == 2) {
            warm(8);
#pragma unroll 1
            for (int t = 9; t <= NT1 + AUX_STAY; ++t) {
                BARRIER();
                if (aw == 1 && t <= 16) { STAMP(40 + t); if (!(t & 1)) CSTAMP(t <= 12 ? 57 + t / 2 : 25 + t / 2); }
                warm(t);
            }
            FLUSH();
            return;
        }
        SB(); BARRIER(); SB();
        if (aw == 3) {
            wsr = wave_sum(wsr);
            if (lane == 0) ctl1[1] = __float_as_uint(wsr);
        } else {
            if (lane == 0) { ctl1[0] = wl | 2u; ctl1[2] = wl; ctl1[3] = wh; }
            *(LAS u32x2*)(lds + L_YB + 12288 + 256 + lane * 8) = (u32x2){0u, 0u};
        }
#pragma unroll
        for (int t = 10; t <= POLL_T; ++t) BARRIER();
        SB();
        u32x4 q0, q1, q2, q3;
        u32x2 qv;
        auto request = [&]() {
            asm volatile("" ::: "memory");
            q0 = LD16_SC1(rS, o0); q1 = LD16_SC1(rS, o0 + 65536); q2 = LD16_SC1(rS, o1); q3 = LD16_SC1(rS, o1 + 65536);
            qv = __builtin_amdgcn_raw_buffer_load_b64(rV, lane * 8, 0, 16);
            SB();
        };
        if (FIN_PRIO) __builtin_amdgcn_s_setprio(FIN_PRIO);
        request();
        int b = POLL_T + 1;
        for (int spin = 0; spin <= SPIN_CAP; ++spin) {
            if (b < NT1) { BARRIER(); ++b; }
            SB();
            unsigned d = ((q0[1] ^ wl) | (q1[1] ^ wl)) | ((q2[1] ^ wl) | (q3[1] ^ wl)) | ((q0[3] ^ wh) | (q1[3] ^ wh)) | ((q2[3] ^ wh) | (q3[3] ^ wh)) | (qv[1] ^ wl);
            asm volatile("" : "+v"(d));
            if (__all(d == 0u)) break;
            request();
        }
        {
            const unsigned a0 = q0[0], a1 = q1[0], b0 = q0[2], b1 = q1[2], c0 = q2[0], c1_ = q3[0];
            const float mean = (__uint_as_float(a0) + __uint_as_float(a1)) * (1.0f / D_LEN);
            const float var = fmaxf((__uint_as_float(b0) + __uint_as_float(b1)) * (1.0f / D_LEN) - mean * mean, 0.f);
            float rden = __builtin_amdgcn_rcpf(rbf(__builtin_amdgcn_sqrtf(rbf(rbf(var) + 1e-5f))));
            if (h != 0) rden = -rden;
            float mr = rbf(mean) * rden;
            float wv = __uint_as_float(c0) + __uint_as_float(c1_);
            const unsigned vt = qv[0];
            unsigned kv = __popcll(__ballot(vt == 2u)) > __popcll(__ballot(vt == 1u)) ? 8192u : 0u;
            asm volatile("" : "+v"(rden), "+v"(mr), "+v"(wv), "+v"(kv));
            SB();
            if (b < NT1) BARRIER();
            SB();
            LAS float* tp = (LAS float*)(size_t)tb;
            tp[0] = rden; tp[128] = mr; tp[256] = wv;
            ctl1[4] = kv;
        }
        WAIT_LGKM0();
        return;
    }
#define CTL(i) ((unsigned)__builtin_amdgcn_readfirstlane((int)ctl[i]))
    const int wp = wid & 1, wq = (wid >> 1) & 1;
#define Y_OFF ((int)WS_Y + ((rt & 7) * 8 + ct) * 65536 + (wid & 3) * 16384 + (((rt >> 3) ^ (rt >> 2)) & 1) * 8192)
#define P2_DEFS \
    char* w2 = ws + kx; \
    const int sidx = idx < 28 ? xq * 28 + idx : 112 + xq * 4 + idx - 28;     \
    volatile LAS unsigned* ctl = (volatile LAS unsigned*)(lds + (h == 0 ? L_ATAB : L_CTL)); \
    const int NT = h == 0 ? 32 + KSHIFT : 32 - KSHIFT; \
    LAS float* rstat = (LAS float*)(lds + L_ATAB + 256);     \
    const int hp_ = rt * 2 + wq;                             \
    (void)sidx; (void)ctl; (void)NT; (void)rstat; (void)hp_; (void)w2
    if (__builtin_expect(wid < 4, 1)) {
        if (wid == 3 && idx < 8) {
            const unsigned r = (((unsigned)__builtin_amdgcn_s_memrealtime() * 2654435761u + blockIdx.x * 40503u) >> 9) & 0x1fdf80u;
            const unsigned first = (idx & 1) << 13;
            const char* pb = ws + WS_PROBE;
            unsigned va, vb, left;
            asm volatile("s_waitcnt lgkmcnt(0)\n\ts_load_dword %0, %3, %4\n\ts_load_dword %1, %3, %5\n\ts_waitcnt lgkmcnt(1)\n\t"
                         "s_nop 15\n\ts_getreg_b32 %2, hwreg(HW_REG_IB_STS, 8, 4)\n\ts_waitcnt lgkmcnt(0)"
                         : "=&s"(va), "=&s"(vb), "=&s"(left) : "s"(pb), "s"(r | first), "s"(r | (first ^ 8192u)) : "memory");
            const unsigned nearbit = (left != 0u ? first : first ^ 8192u) >> 13;
            if (lane == 0) ctl1[5] = nearbit == (unsigned)(xcd >> 2) ? 1u : 2u;
        }
        f32x4 acc[4][4];
#pragma unroll
        for (int i = 0; i < 16; ++i) acc[i >> 2][i & 3] = (f32x4){0.f, 0.f, 0.f, 0.f};
        unsigned tc = (unsigned)(size_t)(tab + wp * 64 + g * 8), tw = (unsigned)(size_t)(tab + 256 + prow(wq * 64 + n));
        unsigned ta = (unsigned)(size_t)lds + wp * STAGE_BYTES + wq * 8192 + lane * 16;
        constexpr int NIL = 1 << 30;
        __amdgpu_buffer_rsrc_t rW = mk_rsrc(ws, 18u << 20);
        const int hpan = rt * 2 + wq;
        int sk = (int)WS_AT + at_panel(h * 8 + ct) + at_tile((rt * 2 + wp) * 128, (ct & 4) << 11) + wq * 16384 + lane * 16;
        int st = lane != 0 ? NIL : (int)WS_FLAG1 + (1 - wp) * 65536 + h * 32768 + (rt >> 3) * 24576 + ct * 1024 + (rt & 7) * 128 + wq * 4;
        int so = h != 0 ? Y_OFF + lane * 16 : g != (n >> 2) ? NIL
               : (int)WS_STATS + (hpan & 15) * 32768 + (ct >> 2) * 16384 + (hpan >> 4) * 8192 + ((ct & 3) * 2 + wp) * 1024 + n * 16;
        int sf = lane != 0 ? NIL : (int)WS_FLAGS + (rt * 8 + ct) * 128 + wid * 4;
        u32x4 sel[2], aw[2];
        unsigned one[2];
#pragma unroll
        for (int i = 0; i < 4; ++i) {
            one[i & 1] = g == (n >> 2) && (i & 1) == ((n >> 1) & 1) ? ((n & 1) ? 0x3F800000u : 0x00003F80u) : 0u;
            aw[0][i] = aw[1][i] = (n & 3) == 0 ? 0x3F803F80u : 0u;
        }
        if ((n & 3) == 1) {
            const bf16_t* wr = a.w_r + ct * BT + wp * 64 + g * 8;
            aw[0] = *(const u32x4*)wr; aw[1] = *(const u32x4*)(wr + 32);
        }
        unsigned ea[2];
#pragma unroll
        for (int mp = 0; mp < 2; ++mp)
            ea[mp] = (unsigned)(size_t)lds + L_YB + wq * 16384 + (h == 0 ? wp * 2048 + mp * 1024 + lane * 16 : n * 256 + (((wp * 8 + mp * 4 + g) ^ n) * 16));
        asm volatile("" : "+v"(tc), "+v"(tw), "+v"(ta), "+v"(sk), "+v"(st), "+v"(so), "+v"(sf), "+v"(one[0]), "+v"(one[1]), "+v"(ea[0]), "+v"(ea[1]));
        asm volatile("" : "+s"(rW));
        unsigned tg;
        if (wid == 0) STAMP(0);
        compute_loop<false, true>(lds, acc, NT1, wp, wq, lane, [&]() { if (wid == 0) STAMP(24); if (wid == 0) STAMP(31); });
        if (wid == 0) STAMP(25);
        __builtin_amdgcn_s_waitcnt(0x0F70);
        {
            f32x4 rd[4], mr[4];
            float wsm[4];
#pragma unroll
            for (int m = 0; m < 4; ++m) {
                rd[m] = *(const LAS f32x4*)((const LAS float*)(size_t)tc + (m >> 1) * 32 + (m & 1) * 4);
                mr[m] = *(const LAS f32x4*)((const LAS float*)(size_t)tc + 128 + (m >> 1) * 32 + (m & 1) * 4);
                wsm[m] = *((const LAS float*)(size_t)tw + (m >> 1) * 32 + (m & 1) * 4);
            }
            const unsigned kv = ctl1[4];
            tg = ctl1[0];
            SB();
            sk += (int)kv; st ^= (int)kv; so += (int)kv; sf += (int)kv;
            kx = (unsigned)__builtin_amdgcn_readfirstlane((int)kv);
#pragma unroll
            for (int nn = 0; nn < 4; ++nn)
#pragma unroll
                for (int mp = 0; mp < 2; ++mp) {
                    const f32x4 u = acc[2 * mp][nn] * rd[2 * mp] - mr[2 * mp] * wsm[nn];
                    const f32x4 v = acc[2 * mp + 1][nn] * rd[2 * mp + 1] - mr[2 * mp + 1] * wsm[nn];
                    u32x4 w;
                    w[0] = pk_bf16(u[0], u[1]); w[1] = pk_bf16(u[2], u[3]);
                    w[2] = pk_bf16(v[0], v[1]); w[3] = pk_bf16(v[2], v[3]);
                    ST16_SC1(w, rW, sk + (nn * 2 + mp) * 1024);
                    *(LAS u32x4*)((LAS unsigned char*)(size_t)ta + (nn * 2 + mp) * 1024) = w;
                }
        }
        if (wid == 0) STAMP(26);
        WAIT_LGKM0();
        BARRIER();
        SB();
        {
#pragma unroll
            for (int i = 0; i < 16; ++i) { acc[i >> 2][i & 3] = (f32x4){0.f, 0.f, 0.f, 0.f}; asm volatile("" : "+v"(acc[i >> 2][i & 3])); }
            sel[0] = (u32x4){one[0], one[1], 0u, 0u}; sel[1] = (u32x4){0u, 0u, one[0], one[1]};
            asm volatile("" : "+v"(st), "+v"(so), "+v"(sf), "+v"(tg), "+v"(sel[0]), "+v"(sel[1]));
            SB();
            WAIT_VM(0);
            __builtin_amdgcn_raw_buffer_store_b32(tg, rW, st, 0, 16);
            SB();
            if (wid == 0) STAMP(27);
        }
        compute_loop<true, false>(lds, acc, h == 0 ? 32 + KSHIFT : 32 - KSHIFT, wp, wq, lane, [&]() {
            if (wid == 0) { STAMP(h == 0 ? 1 : 19); CSTAMP(22); }
        });
        P2_DEFS;
        if (wid == 0) { STAMP(h == 0 ? 2 : 12); CSTAMP(23); }
        u32x4 hq[4][2];
        {
            bf16x8 ef[4][2];
#pragma unroll
            for (int i = 0; i < 8; ++i) ef[i >> 1][i & 1] = *(const LAS bf16x8*)((const LAS unsigned char*)(size_t)ea[i & 1] + (i >> 1) * 4096);
            SB();
#pragma unroll
            for (int i = 0; i < 16; ++i)
                acc[i & 3][i >> 2] = __builtin_amdgcn_mfma_f32_16x16x32_bf16(__builtin_bit_cast(bf16x8, sel[i & 1]), ef[i >> 2][(i >> 1) & 1], acc[i & 3][i >> 2], 0, 0, 0);
#pragma unroll
            for (int nn = 0; nn < 4; ++nn)
#pragma unroll
                for (int mp = 0; mp < 2; ++mp)
#pragma unroll
                    for (int i = 0; i < 4; ++i) {
                        const f32x4 c = acc[2 * mp + (i >> 1)][nn];
                        hq[nn][mp][i] = pk_bf16(c[(i & 1) * 2], c[(i & 1) * 2 + 1]);
                    }
        }
        if (__builtin_expect(h == 0, 1)) {
            f32x4 d1[4], d2[4];
#pragma unroll
            for (int nn = 0; nn < 4; ++nn) {
                d1[nn] = d2[nn] = (f32x4){0.f, 0.f, 0.f, 0.f};
#pragma unroll
                for (int mp = 0; mp < 2; ++mp) {
                    const bf16x8 hf = __builtin_bit_cast(bf16x8, hq[nn][mp]);
                    d1[nn] = __builtin_amdgcn_mfma_f32_16x16x32_bf16(__builtin_bit_cast(bf16x8, aw[mp]), hf, d1[nn], 0, 0, 0);
                    d2[nn] = __builtin_amdgcn_mfma_f32_16x16x32_bf16(hf, hf, d2[nn], 0, 0, 0);
                }
            }
#pragma unroll
            for (int nn = 0; nn < 4; ++nn) {
                u32x3 sv;
                sv[0] = min(__float_as_uint(d1[nn][0]), SENT - 1u);
                sv[1] = min(__float_as_uint(sel4(n, d2[nn][0], d2[nn][1], d2[nn][2], d2[nn][3])), SENT - 1u);
                sv[2] = min(__float_as_uint(d1[nn][1]), SENT - 1u);
                __builtin_amdgcn_raw_buffer_store_b96(sv, rW, so + nn * 256, 0, 16);
            }
            if (wid == 0) STAMP(3);
#pragma unroll
            for (int nn = 0; nn < 4; ++nn)
#pragma unroll
                for (int mp = 0; mp < 2; ++mp)
                    *(LAS u32x4*)(lds + L_HT + (wq * 64 + nn * 16 + n) * 256 + (((wp * 8 + mp * 4 + g) ^ n) * 16)) = hq[nn][mp];
            WAIT_LGKM0();
            BARRIER();
            BARRIER();
            if (wid == 0) STAMP(6);
            const int crow = (int)CTL(2 + wq) + wq * 64;
            const __amdgpu_buffer_rsrc_t rOut = mk_rsrc(a.out + (size_t)rt * BT * D_LEN, BT * D_LEN * 2);
#pragma unroll 2
            for (int j = 0; j < 8; ++j) {
                const int r = wq * 64 + wp * 32 + j * 4 + g;
                const float m_ = rstat[r * 2], d_ = rstat[r * 2 + 1];
                const u32x4 hh = *(const LAS u32x4*)(lds + L_HT + r * 256 + ((n ^ (r & 15)) * 16));
                u32x4 ov;
#pragma unroll
                for (int i = 0; i < 4; ++i) {
                    const unsigned u = hh[i];
                    const float h0 = bflo(u), h1 = bfhi(u);
                    const unsigned dd = pk_bf16(h0 - m_, h1 - m_);
                    const unsigned q = pk_bf16(bflo(dd) * d_, bfhi(dd) * d_);
                    ov[i] = pk_bf16(h0 + bflo(q), h1 + bfhi(q));
                }
                __builtin_amdgcn_raw_buffer_store_b128(ov, rOut, r == crow ? BT * D_LEN * 2 : (r * D_LEN + ct * BT + n * 8) * 2, 0, OUT_AUX);
            }
            FLUSH();
            return;
        }
#pragma unroll
        for (int i = 0; i < 8; ++i) __builtin_amdgcn_raw_buffer_store_b128(hq[i >> 1][i & 1], rW, so, i * 1024, 16);
        WAIT_VM0();
        __builtin_amdgcn_raw_buffer_store_b32(1u, rW, sf, 0, 16);
        if (wid == 0) STAMP(13);
    } else {
        LAS unsigned char* l0 = lds + (wid - 4) * 4096;
        P2_DEFS;
        const __amdgpu_buffer_rsrc_t rD = mk_rsrc(a.wd + (size_t)sidx * 32 * D_LEN, h == 0 && sidx < 112 && wid < 8 - BQ ? 32 * D_LEN * 2 : 0);
        auto load_wd = [&](int i) {
            dma_nt(rD, (unsigned)(size_t)lds + (i < 8 ? L_WD0 : L_WD1) + (wid & 3) * 8192 + (i & 7) * 1024, wd_off(wid & 3, i, lane), i * (4 * D_LEN));
        };
        const int lw = wid - 4;
        constexpr int NIL = 1 << 30;
        ring.rP = ring.rF = mk_rsrc(w2, (unsigned)WS_STATS);
        ring.voP[0] += (int)WS_AT + at_panel(h * 8 + ct);
        ring.voP[1] = ring.voP[2] = ring.voP[3] = ring.voP[0];
#define FLINE(k) (((k) >> 3) * (24576 - 2 * (int)kx) + ((k) & 7) * 128)
        const int ft = (g & 1) * 65536 + (g >> 1) * 4 + (int)WS_FLAG1 + (lw == 0 ? ct * 1024 : NIL);
        int vall = FLINE(n) + ft + h * 32768;
        ring.voF = (n >> 2) == xq ? vall : NIL;
        ring.fl = lds + L_YB + wq * 16384 + 12288 + wp * 2048 + 256;
        unsigned sa = (unsigned)(size_t)lds + L_YB + 12288 + 256 + lane * 4;
        const unsigned tg = (unsigned)__builtin_amdgcn_readfirstlane((int)ctl1[0]);
        float wsum = __uint_as_float(ctl1[1]);
        ring.lb = lw * 4096;
        int cq[3], cp[3];
        unsigned mk[3];
#pragma unroll
        for (int k = 0; k < 3; ++k) {
            const int nb = xq * 4 + NBR(k + 1);
            cq[k] = nb * 256; cp[k] = at_tile(cq[k], ring.par); mk[k] = 0x10000u << nb;
            asm volatile("" : "+s"(cq[k]), "+s"(cp[k]), "+s"(mk[k]));
        }
        unsigned m2 = (mk[0] | mk[1] | mk[2]) >> 16;
        asm volatile("" : "+s"(m2), "+s"(ring.rP));
        asm volatile("" : "+v"(vall), "+v"(ring.voF), "+v"(ring.voP[0]), "+v"(sa), "+v"(wsum));
        ring.rF = ring.rP;
        ring.voP[1] = ring.voP[2] = ring.voP[3] = ring.voP[0];
        WAIT_VM(8);
        BARRIER();
        SB();
        __amdgpu_buffer_rsrc_t rE = mk_rsrc(h == 0 ? (const void*)w2 : (const void*)a.x, (unsigned)WS_STATS);
        int ve[2];
        auto fetch = [&]() {
            if (wid == 4 && h == 0) STAMP(21);
            LAS unsigned char* base = lds + L_YB + wq * 16384 + wp * 2048;
            const int s2 = h == 0 ? 2048 : 16 * D_LEN * 2;
#pragma unroll
            for (int k = 0; k < 4; ++k) dma_2k(rE, (unsigned)(size_t)base + k * 4096, ve[0], ve[1], k * s2);
        };
#pragma unroll
        for (int j = 0; j < 2; ++j)
            ve[j] = h == 0 ? Y_OFF + lane * 16 : ((rt * BT + wq * 64 + wp * 8 + j * 4 + g) * D_LEN + ct * BT + ((n ^ (wp * 8 + j * 4 + g)) * 8)) * 2 - j * 1024;
        ring.rQ2 = mk_rsrc(a.ci + (size_t)(rt * 4 + lw) * 32 * K, 32 * K * 2);
        int vT2 = h == 0 && n < (KSHIFT + 1) / 2 ? FLINE((xq * 4 - 1 - n) & 15) + ft + 32768 : NIL;
        int vY = lane == 0 ? (int)WS_FLAGS + (rt * 8 + ct) * 128 + lw * 4 : NIL;
        int n1 = h == 0 ? 22 : NT - 8;
        {
            const int N = NT - 8, c = (idx + (idx >> 2)) & 3, r = (lane - 2 - c) & 3;
            int j = r == 0 ? lane + 2 : r == 3 ? lane : lane - 1;
            if (lane < 4 && !(lane >= 2 && r == 0)) j = (lane & 1) * 2 + ((c >> 1) ^ (lane >> 1)) - (lane == 3 && c == 0 ? 2 : 0);
            if (j >= N) j = lane; else if (lane == N - 1 && r == 1) j = lane;
            if (ct >= 4 && j >= 4 && j < 20) j = 4 + ((j - 4) ^ 4);
            const int im = j >= 24;
            const int col = (xq * 1024 + 1024 + (j - im * KSHIFT) * 128) & 4095;
            ring.tq = col | (im << 12);
            ring.tp = at_tile(col, ring.par) + im * at_panel(8);
        }
        int c8 = __builtin_amdgcn_readlane(ring.tq, 0), p8 = __builtin_amdgcn_readlane(ring.tp, 0);
        asm volatile("" : "+s"(rE), "+s"(ring.rQ2), "+s"(n1), "+s"(c8), "+s"(p8));
        asm volatile("" : "+v"(ring.tq), "+v"(ring.tp));
        asm volatile("" : "+v"(ve[0]), "+v"(ve[1]), "+v"(vT2), "+v"(vY));
        unsigned have = 0u;
        SB();
        if (T_DELAY) __builtin_amdgcn_s_sleep(T_DELAY);
#define GATE(m) do { if (__builtin_expect(((m) & ~have) != 0u, 0)) ring.gate(tg, sa, m, have); SB(); } while (0)
#define TILE(k, o, st) do { ring.sp = cp[k] + (o) * 256; ring.sq = cq[k] + (o); ring.template issueP<16>(l0 + (st) * STAGE_BYTES); \
                            ring.issueQ(l0 + (st) * STAGE_BYTES); ring.sample(); SB(); } while (0)
        ring.gate(tg, sa, mk[0], have);
        if (wid == 4) STAMP(28);
        ring.sp = cp[0]; ring.template issueP<16>(l0 + 2 * STAGE_BYTES); ring.sample();
        BARRIER();
        GATE(mk[1]);
        ring.sp = cp[1]; ring.template issueP<16>(l0 + 3 * STAGE_BYTES); ring.sample();
        WAIT_VM(6); BARRIER();
        if (wid == 4) STAMP(30);
        GATE(mk[2]);
        TILE(2, 0, 0);
        GATE(m2);
        ring.voF = vall;
        TILE(0, 128, 1);
        WAIT_VM(19); BARRIER();
        TILE(1, 128, 2);
        WAIT_VM(19); BARRIER();
        TILE(2, 128, 3);
        WAIT_VM(19); BARRIER();
#undef GATE
#undef TILE
        if (wid == 4) STAMP(29);
        if (have != 0xffffffffu) ring.gate(tg, sa, 0xffffffffu, have);
        ring.sq = c8; ring.sp = p8; ring.ti = 0;
        ring.voF = vT2;
        ring.fl += 256;
        ring.sample();
        ring.template steady<true, false, 17>(2);
        if (__builtin_expect(blockIdx.x == 255, 0) && tid == 256) {
            const u64 want = ((u64)ctl1[3] << 32) | ctl1[2];
            __hip_atomic_store((GAS u64*)(ws + WS_TAG), want, __ATOMIC_RELAXED, __HIP_MEMORY_SCOPE_AGENT);
        }
        if (wid == 4) { CSTAMP(34); STAMP(36); }
        ring.template steady<true, false>(n1);
        if (wid == 4) { CSTAMP(35); STAMP(37); }
        if (h == 0) {
            have = 0u;
            ring.gate(tg, sa + 256, ((1u << ((KSHIFT + 1) / 2)) - 1u) * 0x10001u, have);
            ring.voF = vY;
            ring.fl -= 512;
            ring.template steady<true, true>(24 + KSHIFT);
        }
        if (h != 0 || ring.seen()) {
            fetch();
            WAIT_VM(16); BARRIER();
            WAIT_VM(8); BARRIER();
        } else {
            ring.sample();
            WAIT_VM(10); BARRIER();
            const bool have = ring.seen();
            if (have) { fetch(); WAIT_VM(9); } else { ring.sample(); WAIT_VM(2); }
            BARRIER();
            if (!have) {
#pragma unroll 1
                for (int spin = 0; spin <= SPIN_CAP && !ring.seen(); ++spin) { ring.sample(); WAIT_VM(0); }
                fetch();
            }
        }
        if (wid == 4 && h == 0) STAMP(20);
        WAIT_VM0();
        BARRIER();
        if (h == 0) {
            const int slot = sidx;
            LAS unsigned char* abuf = lds + L_ATAB + 2048 + lw * 128;
#pragma unroll
            for (int i = 0; i < WD_EARLY; ++i) load_wd(i);
            SB();
            BARRIER();
            __builtin_amdgcn_s_setprio(TAIL_PRIO);
            if (lane < 16) *(LAS unsigned*)(abuf + 64 + lane * 4) = 0u;
            __builtin_amdgcn_s_sleep(STAT_DELAY);
            if (wp == 0) {
                const __amdgpu_buffer_rsrc_t rS = mk_rsrc(w2 + WS_STATS + (hp_ & 15) * 32768 + (hp_ >> 4) * 8192, 24576);
                __amdgpu_buffer_rsrc_t rC = mk_rsrc(w2 + WS_CAND + (size_t)hp_ * 4096, 4096);
                __amdgpu_buffer_rsrc_t rKy = mk_rsrc(w2 + WS_KEYS + hp_ * 4, ct == 0 ? 8192 + 3 * 256 + 4 : 0);
                asm volatile("" : "+s"(rC), "+s"(rKy));
                float s1, s2, s3;
#pragma unroll 1
                for (int spin = 0;; ++spin) {
                    asm volatile("" ::: "memory");
                    u32x3 sv[16];
#pragma unroll
                    for (int j = 0; j < 16; ++j) sv[j] = __builtin_amdgcn_raw_buffer_load_b96(rS, lane * 16, (j >> 3) * 16384 + (j & 7) * 1024, 16);
                    SB();
                    s1 = 0.f; s2 = 0.f; s3 = 0.f;
#pragma unroll
                    for (int j = 0; j < 16; ++j) {
                        const unsigned u0 = sv[j][0], u1 = sv[j][1], u2 = sv[j][2];
                        s1 += __uint_as_float(u0); s2 += __uint_as_float(u1); s3 += __uint_as_float(u2);
                    }
                    const float z = (s1 - s1) + (s2 - s2) + (s3 - s3);
                    if (__builtin_expect(__all(z == 0.f), 1) || spin > SPIN_CAP) break;
                    unsigned mx = 0u;
#pragma unroll
                    for (int j = 0; j < 16; ++j) mx = max(max(mx, sv[j][0]), max(sv[j][1], sv[j][2]));
                    if (__all(mx != SENT)) break;
                }
                if (wid == 4) STAMP(4);
                const float mean = s1 * (1.0f / D_LEN);
                const float var = fmaxf(s2 * (1.0f / D_LEN) - mean * mean, 0.f);
                const float mb = rbf(mean), rden = __builtin_amdgcn_rcpf(rbf(__builtin_amdgcn_sqrtf(rbf(rbf(var) + 1e-5f))));
                const unsigned kb = wave_max_u32(r2key((s3 - mb * wsum) * rden, hp_ * 64 + lane));
                __builtin_amdgcn_raw_buffer_store_b32(kb, rKy, (lane >> 2) * 8192 + (lane & 3) * 256, 0, 16);
                {
                    const int cr_ = __builtin_amdgcn_readfirstlane((int)((0xffffu - (kb & 0xffffu)) & 63u));
                    const float m_ = __int_as_float(__builtin_amdgcn_readlane(__float_as_int(mb), cr_));
                    const float d_ = __int_as_float(__builtin_amdgcn_readlane(__float_as_int(rden), cr_));
                    u32x2 c = *(const LAS u32x2*)(lds + L_HT + (wq * 64 + cr_) * 256 + (((lane & 31) * 8) ^ ((cr_ & 15) * 16)));
#pragma unroll
                    for (int i = 0; i < 2; ++i) {
                        const unsigned u = c[i];
                        const unsigned dd = pk_bf16(bflo(u) - m_, bfhi(u) - m_);
                        const unsigned q = pk_bf16(bflo(dd) * d_, bfhi(dd) * d_);
                        c[i] = min(lane < 32 ? q : u, SENT - 1u);
                    }
                    __builtin_amdgcn_raw_buffer_store_b64(c, rC, (lane < 32 ? 2048 : 0) + ct * 256 + (lane & 31) * 8, 0, 16);
                }
                rstat[(wq * 64 + lane) * 2] = mb; rstat[(wq * 64 + lane) * 2 + 1] = rden;
                if (lane == 0) ctl[2 + wq] = (0xffffu - (kb & 0xffffu)) & 63u;
                WAIT_LGKM0();
                if (wid == 4) STAMP(5);
            }
            BARRIER();
#pragma unroll
            for (int i = WD_EARLY; i < 16; ++i) load_wd(i);
            if (slot < 112 && wid < 8 - BQ) {
                WAIT_VM0();
                if (wid == 4) STAMP(7);
                stage2(w2, slot, lw, lds + L_WD0 + lw * 8192, lds + L_WD1 + lw * 8192, abuf, lane);
                if (wid == 4) STAMP(11);
            }
            FLUSH();
            return;
        }
    }

    P2_DEFS;
    LAS float* red = (LAS float*)(lds + L_RED);
    if (sidx < 112) {
        const int slot = sidx, grp = slot >> 3, kq = slot & 7;
        const __amdgpu_buffer_rsrc_t rG = mk_rsrc(a.wg, D_LEN * F_LEN * 2), rU = mk_rsrc(a.wu, D_LEN * F_LEN * 2);
        auto pair = [&](int i) {
            const int d = wid * 16 + i * 2 + (lane >> 5), ky = (d & 3) | ((d >> 1) & 4), c = lane & 31;
            const int vgu = ((kq * 128 + d) * F_LEN + grp * 256 + (((c >> 1) ^ ky) * 16) + (c & 1) * 8) * 2;
            dma_nt(rG, (unsigned)(size_t)lds + wid * 8192 + i * 1024, vgu, 0);
            dma_nt(rU, (unsigned)(size_t)lds + 65536 + wid * 8192 + i * 1024, vgu ^ 512, 0);
        };
        if (wid >= 4) {
            __builtin_amdgcn_s_sleep(W_PRE_SL);
#pragma unroll
            for (int i = 0; i < W_PRE; ++i) pair(i);
        }
        BARRIER();
        if (wid < 4) {
#pragma unroll
            for (int i = 0; i < W_PRE; ++i) pair(i);
        }
#pragma unroll
        for (int i = W_PRE; i < 8; ++i) pair(i);
        if (BQ && wid == 7) {
            const __amdgpu_buffer_rsrc_t rD = mk_rsrc(a.wd + (size_t)slot * 32 * D_LEN, 32 * D_LEN * 2);
#pragma unroll
            for (int i = 0; i < 16; ++i)
                dma_nt(rD, (unsigned)(size_t)lds + L_YB + 16384 + i * 1024, wd_off(3, i, lane), i * (4 * D_LEN));
            if (lane < 16) *(LAS unsigned*)(lds + L_YB + 4096 + 64 + lane * 4) = 0u;
        }
        const __amdgpu_buffer_rsrc_t rK = mk_rsrc(w2, wid == 0 ? (unsigned)(WS_CAND + 32 * 4096) : 0u);
        const int ok_ = lane < 40 ? (int)WS_KEYS + (xcd >> 2) * 8192 + (xcd & 3) * 256 + (lane - 32) * 16 : (int)(WS_CAND + 32 * 4096);
        const int oc = (int)WS_CAND + 2048 + kq * 256 + n * 16;
        int best = 0;
        u32x4 v = {0u, 0u, 0u, 0u};
        auto request = [&]() {
            asm volatile("" ::: "memory");
            v = LD16_SC1(rK, lane < 32 ? oc + best * 4096 : ok_);
            SB();
        };
        auto settled = [&]() {
            const unsigned mx = umax4(v), mn = min(min(v[0], v[1]), min(v[2], v[3]));
            unsigned fv = lane < 32 ? ~mx : mn | (unsigned)(lane >> 3 ^ 4);
            asm volatile("" : "+v"(fv));
            const bool fine = fv != 0u;
            unsigned km = max(mx, dppu<0xB1>(mx));
            km = max(km, dppu<0x4E>(km));
            km = max(km, dppu<0x141>(km));
            const int nb = (int)(((0xffffu - ((unsigned)__builtin_amdgcn_readlane((int)km, 32) & 0xffffu)) >> 6) & 31u);
            const bool same = nb == best;
            best = nb;
            return __all(fine) && same;
        };
        if (wid == 0) {
            request();
            WAIT_VM(0);
            settled();
            request();
        } else if (BQ && wid == 7) WAIT_VM(16);
        else WAIT_VM(0);
        BARRIER();
        if (wid == 0) STAMP(8);
        bf16x8 wfr[4][4];
        {
            const int q = (lane >> 2) & 3, ky = q | ((g & 1) << 2);
#pragma unroll
            for (int u = 0; u < 4; ++u) {
                const int tro = (u >> 1) * 65536 + (8 * g + q) * 512 + (((wid * 2 + (u & 1)) ^ ky) * 32) + (lane & 3) * 8;
#pragma unroll
                for (int ks = 0; ks < 4; ++ks) {
                    const bf16x4 w0 = __builtin_amdgcn_ds_read_tr16_b64_v4i16((LAS bf16x4*)(lds + tro + ks * 16384));
                    const bf16x4 w1 = __builtin_amdgcn_ds_read_tr16_b64_v4i16((LAS bf16x4*)(lds + tro + ks * 16384 + 2048));
                    wfr[u][ks] = __builtin_shufflevector(w0, w1, 0, 1, 2, 3, 4, 5, 6, 7);
                }
            }
            SB();
#pragma unroll
            for (int i = 0; i < 16; ++i) asm volatile("" : "+v"(wfr[i >> 2][i & 3]));
        }
        const __amdgpu_buffer_rsrc_t rPo = mk_rsrc(w2 + WS_PRE, 112 * 2048);
        int po = n < 4 ? pre_off((grp ^ (n >> 1)) * 8 + wid) + kq * 256 + n * 64 + g * 16 : 112 * 2048;
        asm volatile("" : "+v"(po));
        if (wid == 0) {
#pragma unroll 1
            for (int spin = 0; spin <= SPIN_CAP && !settled(); ++spin) {
                __builtin_amdgcn_s_sleep(1);
                request();
            }
            __builtin_amdgcn_s_waitcnt(0x0F70);
            if (lane < 16) *(LAS u32x4*)(lds + L_RED + lane * 16) = v;
            WAIT_LGKM0();
            STAMP(9);
        }
        BARRIER();
        {
            f32x4 ac[4];
            bf16x8 xf[4];
#pragma unroll
            for (int ks = 0; ks < 4; ++ks) xf[ks] = *(const LAS bf16x8*)(lds + L_RED + ks * 64 + g * 16);
            SB();
#pragma unroll
            for (int u = 0; u < 4; ++u) ac[u] = (f32x4){0.f, 0.f, 0.f, 0.f};
#pragma unroll
            for (int ks = 0; ks < 4; ++ks)
#pragma unroll
                for (int u = 0; u < 4; ++u) ac[u] = __builtin_amdgcn_mfma_f32_16x16x32_bf16(wfr[u][ks], xf[ks], ac[u], 0, 0, 0);
            u32x4 sv;
#pragma unroll
            for (int r = 0; r < 4; ++r) sv[r] = min(__float_as_uint(sel4(n, ac[0][r], ac[1][r], ac[2][r], ac[3][r])), SENT - 1u);
            ST16_SC1(sv, rPo, po);
            SB();
            asm volatile("" :: "v"(v));
        }
        if (wid == 0) STAMP(14);
        if (BQ && wid == 7) {
            WAIT_VM0();
            stage2(w2, slot, 3, lds + L_YB + 16384, lds + L_YB + 24576, lds + L_YB + 4096, lane);
        }
    } else {
        const int rj = sidx - 112;
        if (wid == 4) {
            unsigned kk;
            const unsigned k = poll_winner(w2, xcd, lane, kk);
            if (lane == 0) ctl[4] = k;
            if (lane < 32) ctl[8 + lane] = kk;
            WAIT_LGKM0();
        }
        BARRIER();
        const unsigned kw = CTL(4);
        if (wid == 0) STAMP(16);
        {
            const unsigned kc = ctl[8 + wid * 4 + g];
            const __amdgpu_buffer_rsrc_t rL = mk_rsrc(w2 + WS_CAND + (size_t)wid * 16384, 16384);
            u32x2 hv2, nv2;
            for (int spin = 0;; ++spin) {
                asm volatile("" ::: "memory");
                hv2 = __builtin_amdgcn_raw_buffer_load_b64(rL, g * 4096 + (rj * 64 + n * 4) * 2, 0, 16);
                nv2 = __builtin_amdgcn_raw_buffer_load_b64(rL, g * 4096 + 2048 + (rj * 64 + n * 4) * 2, 0, 16);
                if (__all(max(max(hv2[0], hv2[1]), max(nv2[0], nv2[1])) != SENT) || spin > SPIN_CAP) break;
                __builtin_amdgcn_s_sleep(1);
            }
            u32x2 ov;
#pragma unroll
            for (int i = 0; i < 2; ++i) {
                const unsigned u = hv2[i], q = nv2[i];
                ov[i] = pk_bf16(bflo(u) + bflo(q), bfhi(u) + bfhi(q));
            }
            const int crow = (int)(0xffffu - (kc & 0xffffu)) & (S_LEN - 1);
            if (kc != kw) *(u32x2*)(a.out + (size_t)crow * D_LEN + rj * 64 + n * 4) = ov;
        }
        const int wrow = (int)(0xffffu - (kw & 0xffffu)) & (S_LEN - 1);
        const __amdgpu_buffer_rsrc_t rC = mk_rsrc(w2 + WS_CAND + (size_t)(wrow >> 6) * 4096, 4096);
        const __amdgpu_buffer_rsrc_t rPt = mk_rsrc(w2 + WS_PART, 112 * 4096);
        const int ocol = (rj >> 2) * 256 + ((lane >> 3) & 3) * 64 + (rj & 3) * 16 + (lane & 7) * 2, opos = (lane & 7) * 8 + ((lane >> 3) & 3);
        unsigned hv, hnv;
        for (int spin = 0;; ++spin) {
            asm volatile("" ::: "memory");
            hv = __builtin_amdgcn_raw_buffer_load_b32(rC, ocol * 2, 0, 16);
            hnv = __builtin_amdgcn_raw_buffer_load_b32(rC, 2048 + ocol * 2, 0, 16);
            if (__all(max(hv, hnv) != SENT) || spin > SPIN_CAP) break;
        }
        float sgm = rbf(1.0f / (1.0f + __expf(-key2r(kw))));
        GAS unsigned* optr = (GAS unsigned*)(a.out + (size_t)wrow * D_LEN + ocol);
        asm volatile("" : "+v"(sgm), "+v"(optr));
        u32x4 pv[4];
#pragma unroll 1
        for (int spin = 0;; ++spin) {
            asm volatile("" ::: "memory");
#pragma unroll
            for (int i = 0; i < 4; ++i) pv[i] = LD16_SC1(rPt, (wid + 8 * i) * 16384 + (rj >> 3) * 8192 + g * 2048 + ((rj & 7) * 64 + n * 4) * 4);
            if (__all(max(max(umax4(pv[0]), umax4(pv[1])), max(umax4(pv[2]), umax4(pv[3]))) != SENT) || spin > SPIN_CAP) break;
            __builtin_amdgcn_s_sleep(1);
        }
        if (wid == 0) STAMP(17);
        f32x4 sm;
#pragma unroll
        for (int j = 0; j < 4; ++j) {
            const unsigned u0 = pv[0][j], u1 = pv[1][j], u2 = pv[2][j], u3 = pv[3][j];
            sm[j] = sum_rows((__uint_as_float(u0) + __uint_as_float(u1)) + (__uint_as_float(u2) + __uint_as_float(u3)));
        }
        if (lane < 16) *(LAS f32x4*)(red + wid * 64 + lane * 4) = sm;
        WAIT_LGKM0();
        BARRIER();
        if (wid == 0) {
            float t0 = 0.f, t1 = 0.f;
#pragma unroll
            for (int w = 0; w < 8; ++w) { t0 += red[w * 64 + opos]; t1 += red[w * 64 + opos + 4]; }
            const float y0 = rbf(rbf(t0) * sgm), y1 = rbf(rbf(t1) * sgm);
            const unsigned o = pk_bf16(bflo(hv) + rbf(bflo(hnv) + y0), bfhi(hv) + rbf(bfhi(hnv) + y1));
            if (lane < 32) *optr = o;
            STAMP(18);
        }
        __builtin_amdgcn_s_waitcnt(0x0F70);
    }
    FLUSH();
}

extern "C" void kernel_launch(void* const* d_in, const int* in_sizes, int n_in, void* d_out, int out_size, void* d_ws,
                              size_t ws_size, hipStream_t stream) {
    Args a;
    a.x = (const bf16_t*)d_in[0];
    a.cr = (const bf16_t*)d_in[1];
    a.ci = (const bf16_t*)d_in[2];
    a.wre = (const bf16_t*)d_in[3];
    a.wim = (const bf16_t*)d_in[4];
    a.w_r = (const bf16_t*)d_in[6];
    a.wg = (const bf16_t*)d_in[7];
    a.wu = (const bf16_t*)d_in[8];
    a.wd = (const bf16_t*)d_in[9];
    a.out = (bf16_t*)d_out;
    a.ws = (char*)d_ws + (WS_END ? ((ws_size - ((size_t)24 << 20)) & ~(size_t)255) : 0);
    (void)hipFuncSetAttribute((const void*)fused_kernel, hipFuncAttributeMaxDynamicSharedMemorySize, LDS_BYTES);
    fused_kernel<<<256, 768, LDS_BYTES, stream>>>(a);
}
```

```cpp
#include <hip/hip_runtime.h>
#include <hip/hip_bf16.h>
#include <stdint.h>

typedef unsigned short bf16_t;
typedef short bf16x8 __attribute__((ext_vector_type(8)));
typedef short bf16x4 __attribute__((ext_vector_type(4)));
typedef float f32x4 __attribute__((ext_vector_type(4)));
typedef unsigned u32x4 __attribute__((ext_vector_type(4)));
typedef unsigned u32x2 __attribute__((ext_vector_type(2)));
typedef unsigned u32x3 __attribute__((ext_vector_type(3)));
typedef unsigned long long u64;
#define LAS __attribute__((address_space(3)))
#define GAS __attribute__((address_space(1)))

constexpr int S_LEN = 2048, D_LEN = 1024, F_LEN = 3584;
constexpr unsigned SENT = 0xFFFFFFFFu;
constexpr int SPIN_CAP = 1 << 17;

constexpr size_t WS_KEYS = 0;
constexpr size_t WS_FLAGS = 16384;
constexpr size_t WS_XRD = 524288;
constexpr size_t WS_FLAG1 = 262144;
constexpr size_t WS_TAG = 131072;
constexpr size_t WS_VOTE = WS_TAG + 4096;
constexpr size_t WS_PROBE = (size_t)20 << 20;
constexpr size_t WS_AT = (size_t)4 << 20;
constexpr size_t WS_Y = (size_t)12 << 20;
constexpr size_t WS_STATS = (size_t)16 << 20;
constexpr size_t WS_PART = WS_STATS + (512u << 10);
constexpr size_t WS_CAND = WS_STATS + (1024u << 10);
constexpr size_t WS_PRE = WS_STATS + (1152u << 10);
constexpr size_t FILL_BYTES = (size_t)1408 << 10;

__device__ __forceinline__ float bflo(unsigned v) { return __uint_as_float(v << 16); }
__device__ __forceinline__ float bfhi(unsigned v) { return __uint_as_float(v & 0xffff0000u); }
__device__ __forceinline__ unsigned pk_bf16(float lo, float hi) {
    typedef __bf16 bf2 __attribute__((ext_vector_type(2)));
    typedef float f2 __attribute__((ext_vector_type(2)));
    f2 v = {lo, hi};
    bf2 r = __builtin_convertvector(v, bf2);
    return __builtin_bit_cast(unsigned, r);
}
__device__ __forceinline__ float rbf(float x) { return bflo(pk_bf16(x, 0.f) & 0xffffu); }

template <int CTRL> __device__ __forceinline__ unsigned dppu(unsigned v) {
    return (unsigned)__builtin_amdgcn_update_dpp(0, (int)v, CTRL, 0xf, 0xf, true);
}
template <int CTRL> __device__ __forceinline__ float dppf(float v) { return __uint_as_float(dppu<CTRL>(__float_as_uint(v))); }
__device__ __forceinline__ void swap16(unsigned v, unsigned& a, unsigned& b) {
    auto r = __builtin_amdgcn_permlane16_swap(v, v, false, false);
    a = r[0]; b = r[1];
}
__device__ __forceinline__ void swap32(unsigned v, unsigned& a, unsigned& b) {
    auto r = __builtin_amdgcn_permlane32_swap(v, v, false, false);
    a = r[0]; b = r[1];
}
__device__ __forceinline__ float sum_rows(float v) {
    unsigned a, b;
    swap16(__float_as_uint(v), a, b); v = __uint_as_float(a) + __uint_as_float(b);
    swap32(__float_as_uint(v), a, b); v = __uint_as_float(a) + __uint_as_float(b);
    return v;
}
__device__ __forceinline__ float wave_sum(float v) {
    v += dppf<0xB1>(v);
    v += dppf<0x4E>(v);
    v += dppf<0x141>(v);
    v += dppf<0x140>(v);
    return sum_rows(v);
}
__device__ __forceinline__ unsigned wave_max_u32(unsigned v) {
    v = max(v, dppu<0xB1>(v));
    v = max(v, dppu<0x4E>(v));
    v = max(v, dppu<0x141>(v));
    v = max(v, dppu<0x140>(v));
    unsigned a, b;
    swap16(v, a, b); v = max(a, b);
    swap32(v, a, b); v = max(a, b);
    return v;
}
__device__ __forceinline__ float sel4(int g, float a, float b, float c, float d) {
    const float t0 = (g & 1) ? b : a, t1 = (g & 1) ? d : c;
    return (g & 2) ? t1 : t0;
}
__device__ __forceinline__ unsigned umax4(u32x4 v) { return max(max(v[0], v[1]), max(v[2], v[3])); }

__device__ __forceinline__ void touch_line(const void* p, unsigned lds_dst) {
    unsigned keep;
    asm volatile("s_mov_b32 %0, m0\n\ts_mov_b32 m0, %2\n\ts_nop 0\n\tglobal_load_lds_dword %1, off\n\ts_mov_b32 m0, %0"
                 : "=&s"(keep) : "v"(p), "s"(lds_dst) : "memory");
}
__device__ __forceinline__ __amdgpu_buffer_rsrc_t mk_rsrc(const void* p, unsigned bytes) {
    return __builtin_amdgcn_make_buffer_rsrc((void*)p, 0, (int)bytes, 0x00020000);
}
#define LDS_LOAD(w, v, o, pol) "buffer_load_" w " " v ", %0, %2 offen" o pol " lds"
#define LDS_M0 "s_mov_b32 m0, %1\n\ts_nop 0\n\t"
__device__ __forceinline__ void dma_nt(__amdgpu_buffer_rsrc_t r, unsigned dst, int voff, int soff) {
    asm volatile(LDS_M0 LDS_LOAD("dwordx4", "%3", "", " nt") :: "s"(r), "s"(dst), "s"(soff), "v"(voff) : "memory", "m0");
}
template <bool SC1> __device__ __forceinline__ void dma_word(__amdgpu_buffer_rsrc_t r, unsigned dst, int voff) {
    if constexpr (SC1) asm volatile(LDS_M0 LDS_LOAD("dword", "%3", "", " sc1") :: "s"(r), "s"(dst), "n"(0), "v"(voff) : "memory", "m0");
    else asm volatile(LDS_M0 LDS_LOAD("dword", "%3", "", "") :: "s"(r), "s"(dst), "n"(0), "v"(voff) : "memory", "m0");
}
__device__ __forceinline__ void dma_2k(__amdgpu_buffer_rsrc_t r, unsigned dst, int v0, int v1, int soff) {
    asm volatile(LDS_M0 LDS_LOAD("dwordx4", "%3", "", " sc1") "\n\t" LDS_LOAD("dwordx4", "%4", " offset:1024", " sc1")
                 :: "s"(r), "s"(dst), "s"(soff), "v"(v0), "v"(v1) : "memory", "m0");
}
template <bool SC1> __device__ __forceinline__ void dma_4k(__amdgpu_buffer_rsrc_t r, unsigned dst, const int (&v)[4], int soff) {
#define FOUR(pol) asm volatile(LDS_M0 LDS_LOAD("dwordx4", "%3", "", pol) "\n\t" LDS_LOAD("dwordx4", "%4", " offset:1024", pol) "\n\t" \
        LDS_LOAD("dwordx4", "%5", " offset:2048", pol) "\n\t" LDS_LOAD("dwordx4", "%6", " offset:3072", pol) \
        :: "s"(r), "s"(dst), "s"(soff), "v"(v[0]), "v"(v[1]), "v"(v[2]), "v"(v[3]) : "memory", "m0")
    if constexpr (SC1) FOUR(" sc1"); else FOUR("");
#undef FOUR
}
#define CODE_BYTES 26752
__device__ __forceinline__ __amdgpu_buffer_rsrc_t code_rsrc() {
    unsigned long long p; unsigned d;
    asm volatile("s_getpc_b64 %0\n1:\n\ts_mov_b32 %1, 1b-_Z12fused_kernel4Args" : "=s"(p), "=s"(d));
    return __builtin_amdgcn_make_buffer_rsrc((void*)(p - d), 0, CODE_BYTES, 0x00020000);
}
#define LD16_SC1(rs, off) __builtin_amdgcn_raw_buffer_load_b128(rs, off, 0, 16)
#define ST16_SC1(v, rs, off) __builtin_amdgcn_raw_buffer_store_b128(v, rs, off, 0, 16)

constexpr int BT = 128, BK = 64, NSTAGE = 4;
constexpr int TILE_BYTES = BT * BK * 2;
constexpr int STAGE_BYTES = 2 * TILE_BYTES;
constexpr int RING_BYTES = NSTAGE * STAGE_BYTES;
constexpr int L_DUMMY = RING_BYTES;
constexpr int L_CTL = RING_BYTES + 256;
constexpr int L_RED = RING_BYTES + 512;

#define WAIT_VM(n) asm volatile("s_waitcnt vmcnt(" #n ")" ::: "memory")
#define WAIT_VM0() do { asm volatile("" ::: "memory"); __builtin_amdgcn_s_waitcnt(0x0F70); asm volatile("s_waitcnt vmcnt(0)" ::: "memory"); } while (0)
#define WAIT_LGKM0() asm volatile("s_waitcnt lgkmcnt(0)" ::: "memory")
#define BARRIER() do { asm volatile("" ::: "memory"); __builtin_amdgcn_s_barrier(); asm volatile("" ::: "memory"); } while (0)

__device__ __forceinline__ int prow(int rho) {
    const int w = rho >> 6, m = (rho >> 4) & 3, q = (rho >> 2) & 3, r = rho & 3;
    return w * 64 + (m >> 1) * 32 + q * 8 + (m & 1) * 4 + r;
}
__device__ __forceinline__ unsigned r2key(float r, int row) {
    const unsigned u = pk_bf16(r, 0.f) & 0xffffu;
    const unsigned o = (u & 0x8000u) ? (~u & 0xffffu) : (u | 0x8000u);
    return (o << 16) | (0xffffu - (unsigned)row);
}
__device__ __forceinline__ float key2r(unsigned k) {
    const unsigned o = k >> 16;
    return bflo((o & 0x8000u) ? (o & 0x7fffu) : (~o & 0xffffu));
}
#define RESET_T 2
#define RESET_U 3
#define VOTE_T 4
#define NSAMP 4
#define PSM 0x008800
#define FIN_PRIO 3
#define W_PRE 2
#define W_PRE_SL 20
#define AQ 10
constexpr int BQ = 4 - __builtin_popcount(AQ);
constexpr int bquarter(int k) {
    for (int q = 3; q >= 0; --q) if (!((AQ >> q) & 1) && k-- == 0) return q;
    return 0;
}
#define POLL_T 12
#define AUX_SLEEP 6
#define WD_EARLY 4
#define TAIL_PRIO 3
#define STAT_DELAY 6
#define CODE_WG 1
#define AUX_STAY 1
#define T_DELAY 0
#define WS_END 1
#define CATCH1 4
#define CPF_MASK1 0x1080u
#define CPF_MASK2 0x80u
#define CPF_STAG 5
#define CPF_FIX 0x1000u
#define OUT_AUX 0
#define POLL_SLEEP 4
__device__ __forceinline__ float dot2bf(unsigned a, unsigned b, float c) {
    typedef __bf16 bf2 __attribute__((ext_vector_type(2)));
    return __builtin_amdgcn_fdot2_f32_bf16(__builtin_bit_cast(bf2, a), __builtin_bit_cast(bf2, b), c, false);
}

#define STAMP(i) do { } while (0)
#define CSTAMP(i) do { } while (0)
#define FLUSH() do { } while (0)
#define TSV do { } while (0)

#define KSHIFT 6

constexpr int L_HT = ((32 + KSHIFT) % NSTAGE) * STAGE_BYTES;
constexpr int L_WD0 = ((33 + KSHIFT) % NSTAGE) * STAGE_BYTES;
constexpr int L_WD1 = ((34 + KSHIFT) % NSTAGE) * STAGE_BYTES;
constexpr int L_YB = RING_BYTES;
constexpr int L_ATAB = L_YB;
constexpr int LDS_BYTES = RING_BYTES + 32768;

#define SB() __builtin_amdgcn_sched_barrier(0)
template <int N> __device__ __forceinline__ void wait_vm() { asm volatile("s_waitcnt vmcnt(%0)" :: "n"(N) : "memory"); }
__device__ __forceinline__ int at_tile(int c, int par) { return ((c << 8) & 0x78000) | (((c << 2) & 0x2000) ^ par); }
__device__ __forceinline__ int at_panel(int ep) { return ep << 19; }
__device__ __forceinline__ int pre_off(int slot) { const int s_ = slot >= 56, l = slot - 56 * s_; return (l >> 2) * 16384 + s_ * 8192 + (l & 3) * 2048; }
static_assert(KSHIFT >= 2 && KSHIFT <= 8 && KSHIFT % 2 == 0, "flag samples must have replaced older contents of their slot; even tile counts");
struct Ring {
    __amdgpu_buffer_rsrc_t rP, rQ, rF;
    u64 bq[2];
    int voP[4], voQ[4], voF;
    LAS unsigned char* fl;
    unsigned lb;
    int sp, sq;
    int poff, par;
    int tp;
    int tq, ti;
    bool img;
    template <int LD, bool PIMG> __device__ __forceinline__ void setup(int lw, int lane) {
        const int r = lane >> 3, v0 = (PIMG ? r : (r & 3) + (r & 4) * 2) * (LD * 2) + (((lane & 7) ^ (lane >> 4)) << 4);
#pragma unroll
        for (int j = 0; j < 4; ++j) {
            voQ[j] = (v0 ^ ((j & 1) * 64)) + (PIMG ? j * 8 : (j & 1) * 16 + (j >> 1) * 4) * (LD * 2) - j * 1024;
            voP[j] = PIMG ? (lw >> 1) * 16384 + (lw & 1) * 4096 + lane * 16 : voQ[j];
        }
        img = PIMG;
    }
    __device__ __forceinline__ void seek(int col, int pof) {
        sq = col; poff = pof;
        sp = (img ? at_tile(sq, par) : sq) + poff;
    }
    __device__ __forceinline__ void step() {
        sq = (sq + BK * 2) & 4095;
        sp = (img ? at_tile(sq, par) : sq) + poff;
        asm volatile("" : "+s"(sq), "+s"(sp));
    }
    __device__ __forceinline__ void sample() { dma_word<true>(rF, (unsigned)(size_t)fl, voF); }
    __device__ __forceinline__ unsigned peek(unsigned addr) {
        unsigned v;
        asm volatile("ds_read_b32 %0, %1\n\ts_waitcnt lgkmcnt(0)" : "=v"(v) : "v"(addr) : "memory");
        return v;
    }
    __device__ __forceinline__ bool seen() { return __builtin_amdgcn_readfirstlane(peek((unsigned)(size_t)fl)) == 1u; }
    __device__ __forceinline__ void gate(unsigned want, unsigned sa, unsigned need, unsigned& have) {
#pragma unroll 1
        for (int spin = 0; spin <= SPIN_CAP && (need & ~have) != 0u; ++spin) {
            if (spin) { sample(); WAIT_VM(0); __builtin_amdgcn_s_sleep(1); }
            const unsigned long long m = __ballot(peek(sa) == want);
            have = (unsigned)m & (unsigned)(m >> 32);
        }
    }
    template <int AUX> __device__ __forceinline__ void issueP(LAS unsigned char* base) { dma_4k<AUX != 0>(rP, (unsigned)(size_t)base, voP, sp); }
    __device__ __forceinline__ void issueQ(LAS unsigned char* base) { dma_4k<false>(rQ, (unsigned)(size_t)base + TILE_BYTES, voQ, sq); }
    __device__ __forceinline__ void issue(LAS unsigned char* lds) {
        issueP<0>(lds + lb);
        issueQ(lds + lb);
        SB();
        lb = (lb + STAGE_BYTES) & (RING_BYTES - 1);
        asm volatile("" : "+s"(lb));
        SB();
    }
    __device__ __forceinline__ void next1() {
        ++ti;
        sp = sq = __builtin_amdgcn_readlane(tq, ti);
        asm volatile("s_nop 4" : "+s"(sq), "+s"(sp), "+s"(ti));
        SB();
    }
    template <int CAT> __device__ __forceinline__ void ramp(LAS unsigned char* lds) {
#pragma unroll 1
        for (int i = 1; i <= CAT + 1; ++i) {
            for (int z = (PSM >> (4 * i)) & 15; z > 0; --z) __builtin_amdgcn_s_sleep(1);
            issue(lds);
            next1();
            if (i == CAT + 1) continue;
            WAIT_VM(8);
            BARRIER();
        }
    }
    template <bool P2, bool SAMP, int W = 16> __device__ __forceinline__ void steady(int cnt) {
        int t, u;
#define LD(v, r, s, o) "buffer_load_dwordx4 " v ", " r ", " s " offen" o " lds\n\t"
        if constexpr (P2) {
#define BODY(SMP) asm volatile("s_mov_b32 m0, %0\n\ts_branch 2f\n1:\n\ts_waitcnt vmcnt(%15)\n\ts_barrier\n2:\n\t" \
            LD("%7", "%6", "%2", " sc1") "s_bitcmp1_b32 %1, 12\n\ts_cselect_b64 s[96:97], %20, %19\n\t" \
            LD("%7", "%6", "%2", " offset:1024 sc1") "s_and_b32 %4, %1, 0xfff\n\ts_add_i32 %3, %3, 1\n\t" \
            LD("%7", "%6", "%2", " offset:2048 sc1") "v_readlane_b32 %1, %12, %3\n\t" LD("%7", "%6", "%2", " offset:3072 sc1") \
            SMP "s_add_i32 m0, %0, 0x4000\n\ts_add_i32 %0, %0, 0x8000\n\t" \
            LD("%8", "%5", "%4", "") "v_readlane_b32 %2, %13, %3\n\ts_and_b32 %0, %0, 0x1ffff\n\t" LD("%9", "%5", "%4", " offset:1024") "s_cmp_lg_u32 %3, %14\n\t" \
            LD("%10", "%5", "%4", " offset:2048") LD("%11", "%5", "%4", " offset:3072") "s_mov_b32 m0, %0\n\ts_cbranch_scc1 1b\n\ts_waitcnt vmcnt(%15)\n\ts_barrier" \
            : "+s"(lb), "+s"(sq), "+s"(sp), "+s"(ti), "=&s"(t), "+{s[96:99]}"(rQ) \
            : "s"(rP), "v"(voP[0]), "v"(voQ[0]), "v"(voQ[1]), "v"(voQ[2]), "v"(voQ[3]), "v"(tq), "v"(tp), "s"(cnt), "n"(W), \
              "s"(rF), "v"(voF), "s"((unsigned)(size_t)fl), "s"(bq[0]), "s"(bq[1]) : "memory", "scc", "m0")
            if constexpr (SAMP) BODY("s_mov_b32 m0, %18\n\ts_nop 0\n\tbuffer_load_dword %17, %16, 0 offen sc1 lds\n\t");
            else BODY("");
#undef BODY
            (void)u;
        } else {
            asm volatile("s_mov_b32 m0, %0\n\ts_branch 2f\n1:\n\ts_waitcnt vmcnt(16)\n\ts_barrier\n2:\n\t"
                LD("%6", "%4", "%1", "") LD("%7", "%4", "%1", " offset:1024") LD("%8", "%4", "%1", " offset:2048") LD("%9", "%4", "%1", " offset:3072")
                "s_add_i32 m0, %0, 0x4000\n\ts_mov_b32 %3, %1\n\t"
                LD("%6", "%5", "%3", "") "s_add_i32 %2, %2, 1\n\ts_add_i32 %0, %0, 0x8000\n\t"
                LD("%7", "%5", "%3", " offset:1024") "s_and_b32 %0, %0, 0x1ffff\n\tv_readlane_b32 %1, %10, %2\n\t"
                LD("%8", "%5", "%3", " offset:2048") "s_cmp_lg_u32 %2, %11\n\t"
                LD("%9", "%5", "%3", " offset:3072") "s_mov_b32 m0, %0\n\ts_cbranch_scc1 1b\n\ts_waitcnt vmcnt(16)\n\ts_barrier"
                : "+s"(lb), "+s"(sq), "+s"(ti), "=&s"(t)
                : "s"(rP), "s"(rQ), "v"(voQ[0]), "v"(voQ[1]), "v"(voQ[2]), "v"(voQ[3]), "v"(tq), "n"(D_LEN / BK)
                : "memory", "scc", "m0");
            (void)cnt;
            (void)u;
        }
#undef LD
    }
};

template <bool PLIN, bool FIRST, class Pre>
__device__ __forceinline__ void compute_loop(const LAS unsigned char* lds, f32x4 (&acc)[4][4], bf16x8 (&p0)[4], bf16x8 (&p1)[4], int NT, const unsigned (&ea)[2],
                                             int wp, int wq, int lane, Pre pre) {
    const int fr = lane & 15, fq = lane >> 4, key = (fr >> 1) & 7;
    const unsigned l0 = (unsigned)(size_t)lds;
    const unsigned oP0 = l0 + (PLIN ? wp * 8192 + lane * 16 : (wp * 64 + fr) * 128 + ((fq ^ key) * 16));
    const unsigned oP1 = l0 + (PLIN ? wp * 8192 + 1024 + lane * 16 : (wp * 64 + fr) * 128 + (((4 + fq) ^ key) * 16));
    const unsigned oQ0 = l0 + TILE_BYTES + (wq * 64 + fr) * 128 + ((fq ^ key) * 16);
    const unsigned oQ1 = l0 + TILE_BYTES + (wq * 64 + fr) * 128 + (((4 + fq) ^ key) * 16);
    unsigned aP0 = oP0, aQ0 = oQ0, aP1 = oP1, aQ1 = oQ1;
    bf16x8 r0[4], r1[4], q0[4], q1[4];

#pragma unroll
    for (int i = 0; i < 16; ++i) asm volatile("" : "+v"(acc[i >> 2][i & 3]));
    asm volatile("" : "+v"(aP0), "+v"(aQ0), "+v"(aP1), "+v"(aQ1));
#define MM(pp, qq, m, n) do { acc[m][n] = __builtin_amdgcn_mfma_f32_16x16x32_bf16(pp[m], qq[n], acc[m][n], 0, 0, 0); SB(); } while (0)
#define RD(dst, i, a, st) do { dst[i] = *(const LAS bf16x8*)((const LAS unsigned char*)(size_t)(a) + (st) * STAGE_BYTES + (i) * 2048); SB(); } while (0)
#define ADR(a) do { a ^= 2 * STAGE_BYTES; asm volatile("" : "+v"(a)); SB(); } while (0)
#define ADP(a, e) do { if (PLIN) { a = t == 0 ? e : a; asm volatile("" : "+v"(a)); } SB(); } while (0)
    __builtin_amdgcn_s_waitcnt(0xC07F);
    SB();
    if (FIRST) BARRIER();
    SB();
    RD(p0, 0, aP0, 0); RD(q0, 0, aQ0, 0); RD(p1, 0, aP1, 0); RD(q1, 0, aQ1, 0); RD(p1, 1, aP1, 0); RD(p0, 1, aP0, 0);
    RD(p0, 2, aP0, 0); RD(p1, 2, aP1, 0); RD(p1, 3, aP1, 0); RD(p0, 3, aP0, 0);
    pre();
    SB();
    int t = NT;
#pragma unroll 1
    do {
        MM(p0, q0, 0, 0); MM(p1, q1, 0, 0); RD(q0, 1, aQ0, 0);
        MM(p1, q1, 1, 0); MM(p0, q0, 1, 0); RD(q1, 1, aQ1, 0);
        MM(p0, q0, 2, 0); MM(p1, q1, 2, 0); RD(q0, 2, aQ0, 0);
        MM(p1, q1, 3, 0); MM(p0, q0, 3, 0); RD(q1, 2, aQ1, 0);
        MM(p0, q0, 3, 1); MM(p1, q1, 3, 1); RD(q0, 3, aQ0, 0);
        MM(p1, q1, 2, 1); MM(p0, q0, 2, 1); RD(q1, 3, aQ1, 0);
        MM(p0, q0, 1, 1); MM(p1, q1, 1, 1);
        MM(p1, q1, 0, 1); MM(p0, q0, 0, 1);
        MM(p0, q0, 0, 2); MM(p1, q1, 0, 2); __builtin_amdgcn_s_waitcnt(0xC07F); SB(); BARRIER(); SB();
        MM(p1, q1, 1, 2); RD(r0, 0, aP0, 1); MM(p0, q0, 1, 2); RD(q0, 0, aQ0, 1);
        MM(p0, q0, 2, 2); RD(r1, 0, aP1, 1); MM(p1, q1, 2, 2); RD(q1, 0, aQ1, 1);
        MM(p1, q1, 3, 2); MM(p0, q0, 3, 2); RD(r1, 1, aP1, 1);
        MM(p0, q0, 3, 3); RD(r0, 1, aP0, 1); MM(p1, q1, 3, 3);
        MM(p1, q1, 2, 3); RD(r0, 2, aP0, 1); MM(p0, q0, 2, 3); RD(r1, 2, aP1, 1);
        MM(p0, q0, 1, 3); MM(p1, q1, 1, 3); RD(r1, 3, aP1, 1);
        MM(p1, q1, 0, 3); RD(r0, 3, aP0, 1); MM(p0, q0, 0, 3);

        MM(r0, q0, 0, 0); MM(r1, q1, 0, 0); RD(q0, 1, aQ0, 1);
        MM(r1, q1, 1, 0); MM(r0, q0, 1, 0); RD(q1, 1, aQ1, 1);
        MM(r0, q0, 2, 0); MM(r1, q1, 2, 0); RD(q0, 2, aQ0, 1);
        MM(r1, q1, 3, 0); MM(r0, q0, 3, 0); RD(q1, 2, aQ1, 1);
        MM(r0, q0, 3, 1); MM(r1, q1, 3, 1); RD(q0, 3, aQ0, 1);
        MM(r1, q1, 2, 1); t -= 2; asm volatile("" : "+s"(t)); SB(); MM(r0, q0, 2, 1); RD(q1, 3, aQ1, 1);
        MM(r0, q0, 1, 1); ADR(aP0); MM(r1, q1, 1, 1); ADR(aP1);
        MM(r1, q1, 0, 1); ADP(aP0, ea[0]); ADR(aQ0); MM(r0, q0, 0, 1); ADP(aP1, ea[1]);
        MM(r0, q0, 0, 2); ADR(aQ1); MM(r1, q1, 0, 2); __builtin_amdgcn_s_waitcnt(0xC07F); SB(); BARRIER(); SB();
        MM(r1, q1, 1, 2); RD(p0, 0, aP0, 0); MM(r0, q0, 1, 2); RD(q0, 0, aQ0, 0);
        MM(r0, q0, 2, 2); RD(p1, 0, aP1, 0); MM(r1, q1, 2, 2); RD(q1, 0, aQ1, 0);
        MM(r1, q1, 3, 2); MM(r0, q0, 3, 2); RD(p1, 1, aP1, 0);
        MM(r0, q0, 3, 3); RD(p0, 1, aP0, 0); MM(r1, q1, 3, 3);
        MM(r1, q1, 2, 3); RD(p0, 2, aP0, 0); MM(r0, q0, 2, 3); RD(p1, 2, aP1, 0);
        MM(r0, q0, 1, 3); MM(r1, q1, 1, 3); RD(p1, 3, aP1, 0);
        MM(r1, q1, 0, 3); RD(p0, 3, aP0, 0); MM(r0, q0, 0, 3);
    } while (t != 0);
    SB();
#undef MM
#undef RD
#undef ADR
#undef ADP
}

struct Args {
    const bf16_t* x; const bf16_t* wre; const bf16_t* wim; char* ws; const bf16_t* cr; const bf16_t* ci;
    const bf16_t* w_r; const bf16_t* wg; const bf16_t* wu; const bf16_t* wd; bf16_t* out;
};

__device__ __forceinline__ unsigned poll_winner(const char* ws, int rep, int lane, unsigned& mine) {
    GAS unsigned* kp = (GAS unsigned*)(ws + WS_KEYS + (size_t)(rep >> 2) * 8192 + (rep & 3) * 256) + (lane & 31);
    unsigned kw;
    for (int spin = 0;; ++spin) {
            asm volatile("" ::: "memory");
        kw = __hip_atomic_load(kp, __ATOMIC_RELAXED, __HIP_MEMORY_SCOPE_AGENT);
        if (__all(kw != 0u) || spin > SPIN_CAP) break;
        __builtin_amdgcn_s_sleep(POLL_SLEEP);
    }
    mine = kw;
    return __builtin_amdgcn_readfirstlane(wave_max_u32(kw));
}

__device__ __forceinline__ void stage2(char* w2, int slot, int lw, LAS unsigned char* img0, LAS unsigned char* img1, LAS unsigned char* abuf, int lane) {
    const int g = lane >> 4, n = lane & 15;
    const __amdgpu_buffer_rsrc_t rPi = mk_rsrc(w2 + WS_PRE + pre_off(slot), 2048);
    unsigned pp[8];
    auto request = [&]() {
        asm volatile("" ::: "memory");
#pragma unroll
        for (int k = 0; k < 8; ++k) pp[k] = __builtin_amdgcn_raw_buffer_load_b32(rPi, k * 256 + lane * 4, 0, 16);
        SB();
    };
    request();
    bf16x8 wf[16];
    {
        const int q = (lane >> 2) & 3, ky = q | ((g & 1) << 2);
#pragma unroll
        for (int j = 0; j < 16; ++j) {
            LAS unsigned char* t = (g < 2 ? img0 : img1) + (8 * (g & 1) + q) * 512 + (((j & 7) ^ ky) + (j & 8)) * 32 + (lane & 3) * 8;
            const bf16x4 w0 = __builtin_amdgcn_ds_read_tr16_b64_v4i16((LAS bf16x4*)t);
            const bf16x4 w1 = __builtin_amdgcn_ds_read_tr16_b64_v4i16((LAS bf16x4*)(t + 2048));
            wf[j] = __builtin_shufflevector(w0, w1, 0, 1, 2, 3, 4, 5, 6, 7);
        }
        SB();
#pragma unroll
        for (int j = 0; j < 16; ++j) asm volatile("" : "+v"(wf[j]));
    }
    unsigned aa[5], pto = lane * 16;
#pragma unroll
    for (int m = 0; m < 4; ++m) aa[m] = (unsigned)(size_t)abuf + ((n >> 2) == m ? 0 : 64) + g * 16;
    aa[4] = (unsigned)(size_t)abuf + (lane & 31) * 2;
    __amdgpu_buffer_rsrc_t rPt = mk_rsrc(w2 + WS_PART + __builtin_amdgcn_readfirstlane((slot >> 2) * 16384 + (lw >> 1) * 8192 + (slot & 3) * 2048 + (lw & 1) * 1024), 1024);
    asm volatile("" : "+v"(aa[0]), "+v"(aa[1]), "+v"(aa[2]), "+v"(aa[3]), "+v"(aa[4]), "+v"(pto));
    asm volatile("" : "+s"(rPt));
    SB();
#pragma unroll 1
    for (int spin = 0; spin <= SPIN_CAP && !__all(max(max(max(pp[0], pp[1]), max(pp[2], pp[3])), max(max(pp[4], pp[5]), max(pp[6], pp[7]))) != SENT); ++spin) request();
    const float sv = ((__uint_as_float(pp[0]) + __uint_as_float(pp[1])) + (__uint_as_float(pp[2]) + __uint_as_float(pp[3]))) +
                     ((__uint_as_float(pp[4]) + __uint_as_float(pp[5])) + (__uint_as_float(pp[6]) + __uint_as_float(pp[7])));
    unsigned ug, uu;
    swap32(__float_as_uint(sv), ug, uu);
    const float gg = rbf(__uint_as_float(ug)), up = rbf(__uint_as_float(uu));
    const unsigned act = pk_bf16(rbf(gg * __builtin_amdgcn_rcpf(1.0f + __expf(-gg))) * up, 0.f);
    *(volatile LAS unsigned short*)(size_t)aa[4] = (unsigned short)act;
    bf16x8 af[4];
#pragma unroll
    for (int m = 0; m < 4; ++m) af[m] = *(volatile LAS bf16x8*)(size_t)aa[m];
    u32x4 pv;
#pragma unroll
    for (int q = 0; q < 4; ++q) {
        f32x4 d = {0.f, 0.f, 0.f, 0.f};
#pragma unroll
        for (int m = 0; m < 4; ++m) d = __builtin_amdgcn_mfma_f32_16x16x32_bf16(af[m], wf[4 * q + m], d, 0, 0, 0);
        const float e = d[0];
        pv[q] = min(__float_as_uint(e), SENT - 1u);
    }
    ST16_SC1(pv, rPt, pto);
}
__device__ __forceinline__ int wd_off(int lw, int i, int lane) {
    const int r = 2 * (i & 5) + (lane >> 5), ky = (r & 3) | ((r >> 1) & 4), c = lane & 31;
    return (lane >> 5) * (D_LEN * 2) + lw * 512 + (((c >> 1) ^ ky) * 2 + (c & 1)) * 16;
}

__global__ __launch_bounds__(768, 3) void fused_kernel(const Args a) {
    extern __shared__ __attribute__((aligned(16))) unsigned char smem[];
    constexpr int K = S_LEN, NT1 = D_LEN / BK;
    const int tid = threadIdx.x, wid = __builtin_amdgcn_readfirstlane(tid >> 6), lane = tid & 63;
    const int xcd = blockIdx.x & 7, idx = blockIdx.x >> 3;
    TSV;
    LAS unsigned char* lds = (LAS unsigned char*)smem;
    if (idx < CODE_WG && wid < 4) dma_word<false>(code_rsrc(), (unsigned)(size_t)lds + L_DUMMY, tid * 128);
    SB();
    const int h = xcd & 1, xq = xcd >> 1, rt = xq * 4 + (idx & 3), ct = idx >> 2;
    volatile LAS unsigned* ctl1 = (volatile LAS unsigned*)(lds + L_CTL);
#define NBR(k) ((rt + (k)) & 3)
    unsigned kx;
    Ring ring;
    if (__builtin_expect((wid & 12) == 4, 1)) {
        __builtin_amdgcn_s_setprio(3);
        ring.template setup<D_LEN, false>(wid - 4, lane);
        ring.lb = (wid - 4) * 4096;
        auto ord1 = [&](int i) {
            const int q = (i - idx - (idx >> 2)) & 3;
            return ((xq * 4 + h * 8 + i + 4 - (q == 0 ? 0 : q == 2 ? 4 : 2)) & 15) * 128;
        };
        ring.poff = 0;
        ring.sp = ring.sq = ord1(0);
        unsigned ox = (unsigned)(rt * 4 + wid - 4) * (32 * D_LEN * 2), ow = (unsigned)(ct * 4 + wid - 4) * (32 * D_LEN * 2);
        asm volatile("" : "+s"(ring.sq), "+s"(ring.lb), "+s"(ox), "+s"(ow), "+v"(ring.voQ[0]), "+v"(ring.voQ[1]), "+v"(ring.voQ[2]), "+v"(ring.voQ[3]));
        SB();
        ring.sp = ring.sq;
#pragma unroll
        for (int j = 0; j < 4; ++j) ring.voP[j] = ring.voQ[j];
        ring.rP = mk_rsrc((const char*)a.x + ox, 32 * D_LEN * 2);
        ring.rQ = mk_rsrc((const char*)(h == 0 ? a.wre : a.wim) + ow, 32 * D_LEN * 2);
        ring.issue(lds);
        __builtin_amdgcn_s_setprio(0);
        ring.tq = ord1(lane);
        ring.ti = 0;
        ring.next1();
        ring.par = (ct & 4) << 11;
        ring.template ramp<CATCH1>(lds);
        ring.template steady<false, false>(NT1 - 2 - CATCH1);
        ring.template setup<K, true>(wid - 4, lane);
        ring.rQ = mk_rsrc((h == 0 ? a.cr : a.ci) + (size_t)(rt * 4 + wid - 4) * 32 * K, 32 * K * 2);
        ring.seek(rt * 256, 0);
        LAS unsigned char* l0 = lds + (wid - 4) * 4096;
        ring.issueQ(l0); ring.step();
        WAIT_VM(12); BARRIER();
        ring.issueQ(l0 + STAGE_BYTES);
        ring.seek((xq * 4 + NBR(1)) * 256, 0);
        WAIT_VM(8); BARRIER();
        ring.issueQ(l0 + 2 * STAGE_BYTES);
        ring.seek((xq * 4 + NBR(2)) * 256, 0);
        BARRIER();
        ring.issueQ(l0 + 3 * STAGE_BYTES);
        kx = (unsigned)__builtin_amdgcn_readfirstlane((int)ctl1[4]);
        SB();
    }
    char* ws = a.ws + ((0ull - (unsigned long long)a.ws) & 0x1fffffull);
    const int g = lane >> 4, n = lane & 15;
    LAS float* tab = (LAS float*)(lds + L_RED);
    if (wid >= 8) {
        const int aw = wid - 8;
        GAS u64* tagp = (GAS u64*)(ws + WS_TAG);
        if (AUX_SLEEP) __builtin_amdgcn_s_sleep(AUX_SLEEP);
        u64 tag0 = __hip_atomic_load(tagp, __ATOMIC_RELAXED, __HIP_MEMORY_SCOPE_AGENT);
        SB();
        const int rho = (aw < 2 ? ct : (xq & 1) * 4 + (idx & 3)) * 16 + (aw & 1) * 8 + (n >> 1);
        unsigned fa = (unsigned)(size_t)lds + (aw >> 1) * TILE_BYTES + rho * 128 + ((((n & 1) * 4 + g) ^ ((rho >> 1) & 7)) * 16);
        constexpr int NIL = 1 << 30;
        const int at = tid - 512;
        __amdgpu_buffer_rsrc_t rZ = mk_rsrc(ws, 18u << 20);
        int z0 = (int)WS_STATS + blockIdx.x * (int)(FILL_BYTES / 256) + at * 16;
        int z1 = aw < 2 ? (at < (int)(FILL_BYTES / 4096) - 256 ? z0 + 4096 : NIL) : aw == 2 ? (lane < 10 ? blockIdx.x * 160 + lane * 16 : NIL)
               : lane < 32 ? (int)WS_FLAG1 + (lane >> 4) * 65536 + h * 32768 + (rt >> 3) * 16384 + ((lane >> 3) & 1) * 8192 + ct * 1024 + (rt & 7) * 128 + (lane & 7) * 16
               : NIL;
        u32x4 zv = {SENT, SENT, SENT, SENT};
        asm volatile("" : "+v"(fa), "+v"(z0), "+v"(z1), "+v"(zv), "+s"(rZ));
        f32x4 d1 = {0.f, 0.f, 0.f, 0.f}, d2 = {0.f, 0.f, 0.f, 0.f};
        u32x4 ones = {0x3F803F80u, 0x3F803F80u, 0x3F803F80u, 0x3F803F80u};
        asm volatile("" : "+v"(ones), "+v"(d1), "+v"(d2));
        if (aw == 0) {
            const int j = lane < 4 ? ct * 4 + lane : (idx & 3) * 8 + lane - 4;
            dma_word<true>(rZ, (unsigned)(size_t)lds + L_DUMMY, lane >= 12 ? NIL : (int)WS_XRD + (j >> 4) * 65536 + (lane < 4 ? rt : 16 + h * 8 + ct) * 2048 + (j & 15) * 128);
        }
        const char* cb;
        __amdgpu_buffer_rsrc_t rR, rS, rV;
        unsigned mask, tb, wl, wh;
        int ti, ro, o0, o1;
        auto warm = [&](int t) { if (t >= 4 && ((mask >> t) & 1)) { touch_line(cb + (ti & 7) * 512, L_DUMMY); ti += 2; } };
        u32x4 w0v, w1v;
        float wsr = 0.f;
#pragma unroll
        for (int t = 0; t < 8; ++t) {
            SB();
            BARRIER(); SB();
            const bf16x8 f = *(const LAS bf16x8*)((const LAS unsigned char*)(size_t)fa + (t & 1) * STAGE_BYTES);
            SB();
            if (aw == 1) { STAMP(40 + t); if (!(t & 1)) CSTAMP(57 + t / 2); }
            if (t == 0) {
                rR = mk_rsrc(a.w_r, aw == 3 && h == 0 ? D_LEN * 2 : 0);
                rS = mk_rsrc(ws + WS_XRD, 131072);
                rV = mk_rsrc(ws + WS_VOTE, 512);
                asm volatile("" : "+s"(rR), "+s"(rS), "+s"(rV));
            }
            if (t == 1) {
                cb = (const char*)(h ? a.ci : a.cr) + (size_t)rt * (512u << 10) + ct * 65536 + (lane >> 2) * 4096 + (lane & 3) * 128;
                mask = aw == 1 ? CPF_MASK1 : aw == 2 ? CPF_MASK2 : 0u;
                mask = (mask & CPF_FIX) | ((mask & ~CPF_FIX) << (idx % CPF_STAG));
                ti = 2 * xq + aw - 1;
                asm volatile("" : "+v"(cb), "+s"(mask), "+s"(ti));
            }
            if (t == 2) {
                w0v = __builtin_amdgcn_raw_buffer_load_b128(rR, lane * 16, 0, 0); w1v = __builtin_amdgcn_raw_buffer_load_b128(rR, lane * 16, 1024, 0);
                ro = (n | 2) != g * 4 + 2 ? 131072 : (aw < 2 ? h * 65536 + rt * 2048 : (xq >> 1) * 65536 + (16 + h * 8 + ct) * 2048) + rho * 16;
                asm volatile("" : "+v"(ro));
            }
            if (t == 3) {
                const int hi = aw == 3 ? 64 : 0;
                o0 = rt * 2048 + (hi + lane) * 16;
                o1 = o0 + (16 + h * 8 + ct - rt) * 2048;
                tb = (unsigned)(size_t)(tab + prow(hi + lane));
                asm volatile("" : "+v"(o0), "+v"(o1), "+v"(tb));
            }
            if (t == RESET_T) ST16_SC1(zv, rZ, z0);
            if (t == RESET_U) { if (aw >= 2) zv = (u32x4){0u, 0u, 0u, 0u}; ST16_SC1(zv, rZ, z1); }
            warm(t);
            if (t & 1) fa ^= 2 * STAGE_BYTES;
            d1 = __builtin_amdgcn_mfma_f32_16x16x32_bf16(f, __builtin_bit_cast(bf16x8, ones), d1, 0, 0, 0);
            if (aw < 2) d2 = __builtin_amdgcn_mfma_f32_16x16x32_bf16(f, f, d2, 0, 0, 0);
            if (t == 5) {
#pragma unroll
                for (int i = 0; i < 4; ++i) { wsr = dot2bf(w0v[i], 0x3F803F80u, wsr); wsr = dot2bf(w1v[i], 0x3F803F80u, wsr); }
                asm volatile("" : "+v"(wsr));
            }
            if (t == 1) {
                asm volatile("" : "+v"(tag0));
                const u64 want = tag0 * 6364136223846793005ull + 1442695040888963407ull;
                wl = (unsigned)want; wh = (unsigned)(want >> 32);
                asm volatile("" : "+v"(wl), "+v"(wh));
            }
            if (t == VOTE_T && aw == 3 && idx < 8) __builtin_amdgcn_raw_buffer_store_b64((u32x2){ctl1[5], wl}, rV, blockIdx.x * 8 + lane * 512, 0, 16);
            if (t == 6) WAIT_VM0();
            asm volatile("" : "+v"(d1), "+v"(d2), "+v"(fa));
        }
        SB(); BARRIER(); SB();
        {
            const float sa = d1[0] + d1[1], sb = d1[2] + d1[3];
            float sq = sel4(n, d2[0], d2[1], d2[2], d2[3]);
            sq += dppf<0xB1>(sq);
            const u32x4 rec = {__float_as_uint((n & 2) ? sb : sa), wl, __float_as_uint(sq), wh};
            ST16_SC1(rec, rS, ro);
        }
        if (aw == 1) { STAMP(48); CSTAMP(61); }
        if (aw == 1 || aw == 2) {
            warm(8);
#pragma unroll 1
            for (int t = 9; t <= NT1 + AUX_STAY; ++t) {
                BARRIER();
                if (aw == 1 && t <= 16) { STAMP(40 + t); if (!(t & 1)) CSTAMP(t <= 12 ? 57 + t / 2 : 25 + t / 2); }
                warm(t);
            }
            FLUSH();
            return;
        }
        SB(); BARRIER(); SB();
        if (aw == 3) {
            wsr = wave_sum(wsr);
            if (lane == 0) ctl1[1] = __float_as_uint(wsr);
        } else {
            if (lane == 0) { ctl1[0] = wl | 2u; ctl1[2] = wl; ctl1[3] = wh; }
            *(LAS u32x2*)(lds + L_YB + 6144 + 256 + lane * 8) = (u32x2){0u, 0u};
        }
#pragma unroll
        for (int t = 10; t <= POLL_T; ++t) BARRIER();
        SB();
        u32x4 q0, q1, q2, q3;
        u32x2 qv;
        auto request = [&]() {
            asm volatile("" ::: "memory");
            q0 = LD16_SC1(rS, o0); q1 = LD16_SC1(rS, o0 + 65536); q2 = LD16_SC1(rS, o1); q3 = LD16_SC1(rS, o1 + 65536);
            qv = __builtin_amdgcn_raw_buffer_load_b64(rV, lane * 8, 0, 16);
            SB();
        };
        if (FIN_PRIO) __builtin_amdgcn_s_setprio(FIN_PRIO);
        request();
        int b = POLL_T + 1;
        for (int spin = 0; spin <= SPIN_CAP; ++spin) {
            if (b < NT1) { BARRIER(); ++b; }
            SB();
            unsigned d = ((q0[1] ^ wl) | (q1[1] ^ wl)) | ((q2[1] ^ wl) | (q3[1] ^ wl)) | ((q0[3] ^ wh) | (q1[3] ^ wh)) | ((q2[3] ^ wh) | (q3[3] ^ wh)) | (qv[1] ^ wl);
            asm volatile("" : "+v"(d));
            if (__all(d == 0u)) break;
            request();
        }
        {
            const unsigned a0 = q0[0], a1 = q1[0], b0 = q0[2], b1 = q1[2], c0 = q2[0], c1_ = q3[0];
            const float mean = (__uint_as_float(a0) + __uint_as_float(a1)) * (1.0f / D_LEN);
            const float var = fmaxf((__uint_as_float(b0) + __uint_as_float(b1)) * (1.0f / D_LEN) - mean * mean, 0.f);
            float rden = __builtin_amdgcn_rcpf(rbf(__builtin_amdgcn_sqrtf(rbf(rbf(var) + 1e-5f))));
            if (h != 0) rden = -rden;
            float mr = rbf(mean) * rden;
            float wv = __uint_as_float(c0) + __uint_as_float(c1_);
            const unsigned vt = qv[0];
            unsigned kv = __popcll(__ballot(vt == 2u)) > __popcll(__ballot(vt == 1u)) ? 8192u : 0u;
            asm volatile("" : "+v"(rden), "+v"(mr), "+v"(wv), "+v"(kv));
            SB();
            if (b < NT1) BARRIER();
            SB();
            LAS float* tp = (LAS float*)(size_t)tb;
            tp[0] = rden; tp[128] = mr; tp[256] = wv;
            ctl1[4] = kv;
        }
        WAIT_LGKM0();
        return;
    }
#define CTL(i) ((unsigned)__builtin_amdgcn_readfirstlane((int)ctl[i]))
    const int wp = wid & 1, wq = (wid >> 1) & 1;
#define Y_OFF ((int)WS_Y + ((rt & 7) * 8 + ct) * 65536 + (wid & 3) * 16384 + (((rt >> 3) ^ (rt >> 2)) & 1) * 8192)
#define P2_DEFS \
    char* w2 = ws + kx; \
    const int sidx = idx < 28 ? xq * 28 + idx : 112 + xq * 4 + idx - 28;     \
    volatile LAS unsigned* ctl = (volatile LAS unsigned*)(lds + (h == 0 ? L_ATAB : L_CTL)); \
    const int NT = h == 0 ? 32 + KSHIFT : 32 - KSHIFT; \
    LAS float* rstat = (LAS float*)(lds + L_ATAB + 256);     \
    const int hp_ = rt * 2 + wq;                             \
    (void)sidx; (void)ctl; (void)NT; (void)rstat; (void)hp_; (void)w2
    if (__builtin_expect(wid < 4, 1)) {
        if (wid == 3 && idx < 8) {
            const unsigned r = (((unsigned)__builtin_amdgcn_s_memrealtime() * 2654435761u + blockIdx.x * 40503u) >> 9) & 0x1fdf80u;
            const unsigned first = (idx & 1) << 13;
            const char* pb = ws + WS_PROBE;
            unsigned va, vb, left;
            asm volatile("s_waitcnt lgkmcnt(0)\n\ts_load_dword %0, %3, %4\n\ts_load_dword %1, %3, %5\n\ts_waitcnt lgkmcnt(1)\n\t"
                         "s_nop 15\n\ts_getreg_b32 %2, hwreg(HW_REG_IB_STS, 8, 4)\n\ts_waitcnt lgkmcnt(0)"
                         : "=&s"(va), "=&s"(vb), "=&s"(left) : "s"(pb), "s"(r | first), "s"(r | (first ^ 8192u)) : "memory");
            const unsigned nearbit = (left != 0u ? first : first ^ 8192u) >> 13;
            if (lane == 0) ctl1[5] = nearbit == (unsigned)(xcd >> 2) ? 1u : 2u;
        }
        f32x4 acc[4][4];
#pragma unroll
        for (int i = 0; i < 16; ++i) acc[i >> 2][i & 3] = (f32x4){0.f, 0.f, 0.f, 0.f};
        unsigned tc = (unsigned)(size_t)(tab + wp * 64 + g * 8), tw = (unsigned)(size_t)(tab + 256 + prow(wq * 64 + n));
        unsigned ta = (unsigned)(size_t)lds + wp * STAGE_BYTES + wq * 8192 + lane * 16;
        constexpr int NIL = 1 << 30;
        __amdgpu_buffer_rsrc_t rW = mk_rsrc(ws, 18u << 20);
        const int hpan = rt * 2 + wq;
        int sk = (int)WS_AT + at_panel(h * 8 + ct) + at_tile((rt * 2 + wp) * 128, (ct & 4) << 11) + wq * 16384 + lane * 16;
        int st = lane != 0 ? NIL : (int)WS_FLAG1 + (1 - wp) * 65536 + h * 32768 + (rt >> 3) * 24576 + ct * 1024 + (rt & 7) * 128 + wq * 4;
        int so = h != 0 ? Y_OFF + lane * 16 : g != (n >> 2) ? NIL
               : (int)WS_STATS + (hpan & 15) * 32768 + (ct >> 2) * 16384 + (hpan >> 4) * 8192 + ((ct & 3) * 2 + wp) * 1024 + n * 16;
        int sf = lane != 0 ? NIL : (int)WS_FLAGS + (rt * 8 + ct) * 128 + wid * 4;
        u32x4 sel[2], aw[2];
        unsigned one[2];
#pragma unroll
        for (int i = 0; i < 4; ++i) {
            one[i & 1] = g == (n >> 2) && (i & 1) == ((n >> 1) & 1) ? ((n & 1) ? 0x3F800000u : 0x00003F80u) : 0u;
            aw[0][i] = aw[1][i] = (n & 3) == 0 ? 0x3F803F80u : 0u;
        }
        if ((n & 3) == 1) {
            const bf16_t* wr = a.w_r + ct * BT + wp * 64 + g * 8;
            aw[0] = *(const u32x4*)wr; aw[1] = *(const u32x4*)(wr + 32);
        }
        bf16x8 e0[4], e1[4];
        unsigned ea[2];
#pragma unroll
        for (int mp = 0; mp < 2; ++mp)
            ea[mp] = (unsigned)(size_t)lds + L_YB + wq * 16384 + wp * 8192 + (h == 0 ? mp * 1024 + lane * 16 : n * 128 + (((mp * 4 + g) ^ (n >> 1)) * 16));
        asm volatile("" : "+v"(ea[0]), "+v"(ea[1]));
        asm volatile("" : "+v"(tc), "+v"(tw), "+v"(ta), "+v"(sk), "+v"(st), "+v"(so), "+v"(sf), "+v"(one[0]), "+v"(one[1]));
        asm volatile("" : "+s"(rW));
        unsigned tg;
        if (wid == 0) STAMP(0);
        compute_loop<false, true>(lds, acc, e0, e1, NT1, ea, wp, wq, lane, [&]() { if (wid == 0) STAMP(24); if (wid == 0) STAMP(31); });
        if (wid == 0) STAMP(25);
        __builtin_amdgcn_s_waitcnt(0x0F70);
        {
            f32x4 rd[4], mr[4];
            float wsm[4];
#pragma unroll
            for (int m = 0; m < 4; ++m) {
                rd[m] = *(const LAS f32x4*)((const LAS float*)(size_t)tc + (m >> 1) * 32 + (m & 1) * 4);
                mr[m] = *(const LAS f32x4*)((const LAS float*)(size_t)tc + 128 + (m >> 1) * 32 + (m & 1) * 4);
                wsm[m] = *((const LAS float*)(size_t)tw + (m >> 1) * 32 + (m & 1) * 4);
            }
            const unsigned kv = ctl1[4];
            tg = ctl1[0];
            SB();
            sk += (int)kv; st ^= (int)kv; so += (int)kv; sf += (int)kv;
            kx = (unsigned)__builtin_amdgcn_readfirstlane((int)kv);
#pragma unroll
            for (int nn = 0; nn < 4; ++nn)
#pragma unroll
                for (int mp = 0; mp < 2; ++mp) {
                    const f32x4 u = acc[2 * mp][nn] * rd[2 * mp] - mr[2 * mp] * wsm[nn];
                    const f32x4 v = acc[2 * mp + 1][nn] * rd[2 * mp + 1] - mr[2 * mp + 1] * wsm[nn];
                    u32x4 w;
                    w[0] = pk_bf16(u[0], u[1]); w[1] = pk_bf16(u[2], u[3]);
                    w[2] = pk_bf16(v[0], v[1]); w[3] = pk_bf16(v[2], v[3]);
                    ST16_SC1(w, rW, sk + (nn * 2 + mp) * 1024);
                    *(LAS u32x4*)((LAS unsigned char*)(size_t)ta + (nn * 2 + mp) * 1024) = w;
                }
        }
        if (wid == 0) STAMP(26);
        WAIT_LGKM0();
        BARRIER();
        SB();
        {
#pragma unroll
            for (int i = 0; i < 16; ++i) { acc[i >> 2][i & 3] = (f32x4){0.f, 0.f, 0.f, 0.f}; asm volatile("" : "+v"(acc[i >> 2][i & 3])); }
            sel[0] = (u32x4){one[0], one[1], 0u, 0u}; sel[1] = (u32x4){0u, 0u, one[0], one[1]};
            asm volatile("" : "+v"(st), "+v"(so), "+v"(sf), "+v"(tg), "+v"(sel[0]), "+v"(sel[1]));
            SB();
            WAIT_VM(0);
            __builtin_amdgcn_raw_buffer_store_b32(tg, rW, st, 0, 16);
            SB();
            if (wid == 0) STAMP(27);
        }
        compute_loop<true, false>(lds, acc, e0, e1, h == 0 ? 32 + KSHIFT : 32 - KSHIFT, ea, wp, wq, lane, [&]() {
            if (wid == 0) { STAMP(h == 0 ? 1 : 19); CSTAMP(22); }
        });
        P2_DEFS;
        if (wid == 0) { STAMP(h == 0 ? 2 : 12); CSTAMP(23); }
        u32x4 hq[4][2];
#define ADD(nn, m) do { acc[m][nn] = __builtin_amdgcn_mfma_f32_16x16x32_bf16(__builtin_bit_cast(bf16x8, sel[(m) & 1]), (m) < 2 ? e0[nn] : e1[nn], acc[m][nn], 0, 0, 0); SB(); } while (0)
#define PK(nn, m) do { const f32x4 c_ = acc[m][nn]; hq[nn][(m) >> 1][((m) & 1) * 2] = pk_bf16(c_[0], c_[1]); hq[nn][(m) >> 1][((m) & 1) * 2 + 1] = pk_bf16(c_[2], c_[3]); SB(); } while (0)
        if (__builtin_expect(h == 0, 1)) {
            f32x4 d1[4], d2[4];
            const f32x4 z4 = {0.f, 0.f, 0.f, 0.f};
#define S1(nn, mp) do { d1[nn] = __builtin_amdgcn_mfma_f32_16x16x32_bf16(__builtin_bit_cast(bf16x8, aw[mp]), __builtin_bit_cast(bf16x8, hq[nn][mp]), (mp) ? d1[nn] : z4, 0, 0, 0); SB(); } while (0)
#define S2(nn, mp) do { const bf16x8 f_ = __builtin_bit_cast(bf16x8, hq[nn][mp]); d2[nn] = __builtin_amdgcn_mfma_f32_16x16x32_bf16(f_, f_, (mp) ? d2[nn] : z4, 0, 0, 0); SB(); } while (0)
#define OUT(nn) do { u32x3 sv; sv[0] = min(__float_as_uint(d1[nn][0]), SENT - 1u); \
                sv[1] = min(__float_as_uint(sel4(n, d2[nn][0], d2[nn][1], d2[nn][2], d2[nn][3])), SENT - 1u); \
                sv[2] = min(__float_as_uint(d1[nn][1]), SENT - 1u); \
                __builtin_amdgcn_raw_buffer_store_b96(sv, rW, so + (nn) * 256, 0, 16); SB(); } while (0)
            ADD(0, 0); ADD(0, 1); ADD(0, 2); ADD(0, 3);
            ADD(1, 0); PK(0, 0); ADD(1, 1); PK(0, 1); ADD(1, 2); PK(0, 2); ADD(1, 3); PK(0, 3);
            ADD(2, 0); PK(1, 0); ADD(2, 1); PK(1, 1); ADD(2, 2); PK(1, 2); ADD(2, 3); PK(1, 3);
            ADD(3, 0); PK(2, 0); ADD(3, 1); PK(2, 1); ADD(3, 2); PK(2, 2); ADD(3, 3); PK(2, 3);
            S1(0, 0); PK(3, 0); S2(0, 0); PK(3, 1); S1(0, 1); PK(3, 2); S2(0, 1); PK(3, 3);
            S1(1, 0); S2(1, 0); S1(1, 1); S2(1, 1); S1(2, 0); OUT(0); S2(2, 0); S1(2, 1); S2(2, 1);
            S1(3, 0); OUT(1); S2(3, 0); S1(3, 1); S2(3, 1); OUT(2); OUT(3);
#undef S1
#undef S2
#undef OUT
            if (wid == 0) STAMP(3);
#pragma unroll
            for (int nn = 0; nn < 4; ++nn)
#pragma unroll
                for (int mp = 0; mp < 2; ++mp)
                    *(LAS u32x4*)(lds + L_HT + (wq * 64 + nn * 16 + n) * 256 + (((wp * 8 + mp * 4 + g) ^ n) * 16)) = hq[nn][mp];
            WAIT_LGKM0();
            BARRIER();
            BARRIER();
            if (wid == 0) STAMP(6);
            const int crow = (int)CTL(2 + wq) + wq * 64;
            const __amdgpu_buffer_rsrc_t rOut = mk_rsrc(a.out + (size_t)rt * BT * D_LEN, BT * D_LEN * 2);
#pragma unroll 2
            for (int j = 0; j < 8; ++j) {
                const int r = wq * 64 + wp * 32 + j * 4 + g;
                const float m_ = rstat[r * 2], d_ = rstat[r * 2 + 1];
                const u32x4 hh = *(const LAS u32x4*)(lds + L_HT + r * 256 + ((n ^ (r & 15)) * 16));
                u32x4 ov;
#pragma unroll
                for (int i = 0; i < 4; ++i) {
                    const unsigned u = hh[i];
                    const float h0 = bflo(u), h1 = bfhi(u);
                    const unsigned dd = pk_bf16(h0 - m_, h1 - m_);
                    const unsigned q = pk_bf16(bflo(dd) * d_, bfhi(dd) * d_);
                    ov[i] = pk_bf16(h0 + bflo(q), h1 + bfhi(q));
                }
                __builtin_amdgcn_raw_buffer_store_b128(ov, rOut, r == crow ? BT * D_LEN * 2 : (r * D_LEN + ct * BT + n * 8) * 2, 0, OUT_AUX);
            }
            FLUSH();
            return;
        }
#define YS(i) do { __builtin_amdgcn_raw_buffer_store_b128(hq[(i) >> 1][(i) & 1], rW, so, (i) * 1024, 16); SB(); } while (0)
        ADD(0, 0); ADD(0, 1); ADD(0, 2); ADD(0, 3);
        ADD(1, 0); PK(0, 0); ADD(1, 1); PK(0, 1); ADD(1, 2); PK(0, 2); YS(0); ADD(1, 3); PK(0, 3); YS(1);
        ADD(2, 0); PK(1, 0); ADD(2, 1); PK(1, 1); ADD(2, 2); PK(1, 2); YS(2); ADD(2, 3); PK(1, 3); YS(3);
        ADD(3, 0); PK(2, 0); ADD(3, 1); PK(2, 1); ADD(3, 2); PK(2, 2); YS(4); ADD(3, 3); PK(2, 3); YS(5);
        PK(3, 0); PK(3, 1); YS(6); PK(3, 2); PK(3, 3); YS(7);
#undef YS
#undef ADD
#undef PK
        WAIT_VM0();
        __builtin_amdgcn_raw_buffer_store_b32(1u, rW, sf, 0, 16);
        if (wid == 0) STAMP(13);
    } else {
        LAS unsigned char* l0 = lds + (wid - 4) * 4096;
        P2_DEFS;
        const __amdgpu_buffer_rsrc_t rD = mk_rsrc(a.wd + (size_t)sidx * 32 * D_LEN, h == 0 && sidx < 112 && ((AQ << 4 >> wid) & 1) ? 32 * D_LEN * 2 : 0);
        auto load_wd = [&](int i) {
            dma_nt(rD, (unsigned)(size_t)lds + (i < 8 ? L_WD0 : L_WD1) + (wid & 3) * 8192 + (i & 7) * 1024, wd_off(wid & 3, i, lane), i * (4 * D_LEN));
        };
        const int lw = wid - 4;
        constexpr int NIL = 1 << 30;
        ring.rP = ring.rF = mk_rsrc(w2, (unsigned)WS_STATS);
        ring.voP[0] += (int)WS_AT + at_panel(h * 8 + ct);
        ring.voP[1] = ring.voP[2] = ring.voP[3] = ring.voP[0];
#define FLINE(k) (((k) >> 3) * (24576 - 2 * (int)kx) + ((k) & 7) * 128)
        const int ft = (g & 1) * 65536 + (g >> 1) * 4 + (int)WS_FLAG1 + (lw == 0 ? ct * 1024 : NIL);
        int vall = FLINE(n) + ft + h * 32768;
        ring.voF = (n >> 2) == xq ? vall : NIL;
        ring.fl = lds + L_YB + wq * 16384 + wp * 8192 + 6144 + 256;
        unsigned sa = (unsigned)(size_t)lds + L_YB + 6144 + 256 + lane * 4;
        const unsigned tg = (unsigned)__builtin_amdgcn_readfirstlane((int)ctl1[0]);
        float wsum = __uint_as_float(ctl1[1]);
        ring.lb = lw * 4096;
        int cq[3], cp[3];
        unsigned mk[3];
#pragma unroll
        for (int k = 0; k < 3; ++k) {
            const int nb = xq * 4 + NBR(k + 1);
            cq[k] = nb * 256; cp[k] = at_tile(cq[k], ring.par); mk[k] = 0x10000u << nb;
            asm volatile("" : "+s"(cq[k]), "+s"(cp[k]), "+s"(mk[k]));
        }
        unsigned m2 = (mk[0] | mk[1] | mk[2]) >> 16;
        asm volatile("" : "+s"(m2), "+s"(ring.rP));
        asm volatile("" : "+v"(vall), "+v"(ring.voF), "+v"(ring.voP[0]), "+v"(sa), "+v"(wsum));
        ring.rF = ring.rP;
        ring.voP[1] = ring.voP[2] = ring.voP[3] = ring.voP[0];
        WAIT_VM(8);
        BARRIER();
        SB();
        __amdgpu_buffer_rsrc_t rE = mk_rsrc(h == 0 ? (const void*)w2 : (const void*)a.x, (unsigned)WS_STATS);
        int ve[2];
        auto fetch = [&]() {
            if (wid == 4 && h == 0) STAMP(21);
            LAS unsigned char* base = lds + L_YB + wq * 16384 + wp * 8192;
            const int s2 = h == 0 ? 2048 : 16 * D_LEN * 2;
#pragma unroll
            for (int k = 0; k < 4; ++k) dma_2k(rE, (unsigned)(size_t)base + k * 2048, ve[0], ve[1], k * s2);
        };
#pragma unroll
        for (int j = 0; j < 2; ++j)
            ve[j] = h == 0 ? Y_OFF + lane * 16 : ((rt * BT + wq * 64 + j * 8 + (lane >> 3)) * D_LEN + ct * BT + wp * 64 + ((lane & 7) ^ (j * 4 + g)) * 8) * 2 - j * 1024;
        ring.bq[0] = (u64)((h == 0 ? a.cr : a.ci) + (size_t)(rt * 4 + lw) * 32 * K) & 0xffffffffffffull;
        ring.bq[1] = (u64)(a.ci + (size_t)(rt * 4 + lw) * 32 * K) & 0xffffffffffffull;
        int vT2 = h == 0 && n < (KSHIFT + 1) / 2 ? FLINE((xq * 4 - 1 - n) & 15) + ft + 32768 : NIL;
        int vY = lane == 0 ? (int)WS_FLAGS + (rt * 8 + ct) * 128 + lw * 4 : NIL;
        int n1 = h == 0 ? NT - 8 - NSAMP : NT - 8;
        {
            const int N = NT - 8, c = (idx + (idx >> 2)) & 3, r = (lane - 2 - c) & 3;
            int j = r == 0 ? lane + 2 : r == 3 ? lane : lane - 1;
            if (lane < 4 && !(lane >= 2 && r == 0)) j = (lane & 1) * 2 + ((c >> 1) ^ (lane >> 1)) - (lane == 3 && c == 0 ? 2 : 0);
            if (j >= N) j = lane; else if (lane == N - 1 && r == 1) j = lane;
            if (ct >= 4 && j >= 4) j = N + 3 - j;
            const int im = j >= 24;
            const int col = (xq * 1024 + 1024 + (j - im * KSHIFT) * 128) & 4095;
            ring.tq = col | (im << 12);
            ring.tp = at_tile(col, ring.par) + im * at_panel(8);
        }
        int c8 = __builtin_amdgcn_readlane(ring.tq, 0), p8 = __builtin_amdgcn_readlane(ring.tp, 0);
        asm volatile("" : "+s"(rE), "+s"(ring.bq[0]), "+s"(ring.bq[1]), "+s"(n1), "+s"(c8), "+s"(p8));
        asm volatile("" : "+v"(ring.tq), "+v"(ring.tp));
        asm volatile("" : "+v"(ve[0]), "+v"(ve[1]), "+v"(vT2), "+v"(vY));
        unsigned have = 0u;
        SB();
        if (T_DELAY) __builtin_amdgcn_s_sleep(T_DELAY);
#define GATE(m) do { if (__builtin_expect(((m) & ~have) != 0u, 0)) ring.gate(tg, sa, m, have); SB(); } while (0)
#define TILE(k, o, st) do { ring.sp = cp[k] + (o) * 256; ring.sq = cq[k] + (o); ring.template issueP<16>(l0 + (st) * STAGE_BYTES); \
                            ring.issueQ(l0 + (st) * STAGE_BYTES); ring.sample(); SB(); } while (0)
        ring.gate(tg, sa, mk[0], have);
        if (wid == 4) STAMP(28);
        ring.sp = cp[0]; ring.template issueP<16>(l0 + 2 * STAGE_BYTES); ring.sample();
        BARRIER();
        GATE(mk[1]);
        ring.sp = cp[1]; ring.template issueP<16>(l0 + 3 * STAGE_BYTES); ring.sample();
        WAIT_VM(6); BARRIER();
        if (wid == 4) STAMP(30);
        GATE(mk[2]);
        TILE(2, 0, 0);
        GATE(m2);
        ring.voF = vall;
        TILE(0, 128, 1);
        WAIT_VM(19); BARRIER();
        TILE(1, 128, 2);
        WAIT_VM(19); BARRIER();
        TILE(2, 128, 3);
        WAIT_VM(19); BARRIER();
#undef GATE
#undef TILE
        if (wid == 4) STAMP(29);
        if (have != 0xffffffffu) ring.gate(tg, sa, 0xffffffffu, have);
        ring.sq = c8; ring.sp = p8; ring.ti = 0;
        ring.voF = vT2;
        ring.fl += 256;
        ring.sample();
        ring.template steady<true, false, 17>(2);
        if (__builtin_expect(blockIdx.x == 255, 0) && tid == 256) {
            const u64 want = ((u64)ctl1[3] << 32) | ctl1[2];
            __hip_atomic_store((GAS u64*)(ws + WS_TAG), want, __ATOMIC_RELAXED, __HIP_MEMORY_SCOPE_AGENT);
        }
        if (h == 0) {
            have = 0u;
            ring.gate(tg, sa + 256, ((1u << ((KSHIFT + 1) / 2)) - 1u) * 0x10001u, have);
        }
        if (wid == 4) { CSTAMP(34); STAMP(36); }
        ring.template steady<true, false>(n1);
        if (wid == 4) { CSTAMP(35); STAMP(37); }
        if (h == 0) {
            ring.voF = vY;
            ring.fl -= 512;
            ring.template steady<true, true>(24 + KSHIFT);
        }
        if (h != 0 || ring.seen()) {
            fetch();
            WAIT_VM(16); BARRIER();
            WAIT_VM(8); BARRIER();
        } else {
            ring.sample();
            WAIT_VM(10); BARRIER();
            const bool have = ring.seen();
            if (have) { fetch(); WAIT_VM(9); } else { ring.sample(); WAIT_VM(2); }
            BARRIER();
            if (!have) {
#pragma unroll 1
                for (int spin = 0; spin <= SPIN_CAP && !ring.seen(); ++spin) { ring.sample(); WAIT_VM(0); }
                fetch();
            }
        }
        if (wid == 4 && h == 0) STAMP(20);
        WAIT_VM0();
        BARRIER();
        if (h == 0) {
            const int slot = sidx;
            LAS unsigned char* abuf = lds + L_ATAB + 2048 + lw * 128;
#pragma unroll
            for (int i = 0; i < WD_EARLY; ++i) load_wd(i);
            SB();
            BARRIER();
            __builtin_amdgcn_s_setprio(TAIL_PRIO);
            if (lane < 16) *(LAS unsigned*)(abuf + 64 + lane * 4) = 0u;
            __builtin_amdgcn_s_sleep(STAT_DELAY);
            if (wp == 0) {
                const __amdgpu_buffer_rsrc_t rS = mk_rsrc(w2 + WS_STATS + (hp_ & 15) * 32768 + (hp_ >> 4) * 8192, 24576);
                __amdgpu_buffer_rsrc_t rC = mk_rsrc(w2 + WS_CAND + (size_t)hp_ * 4096, 4096);
                __amdgpu_buffer_rsrc_t rKy = mk_rsrc(w2 + WS_KEYS + hp_ * 4, ct == 0 ? 8192 + 3 * 256 + 4 : 0);
                asm volatile("" : "+s"(rC), "+s"(rKy));
                float s1, s2, s3;
#pragma unroll 1
                for (int spin = 0;; ++spin) {
                    asm volatile("" ::: "memory");
                    u32x3 sv[16];
#pragma unroll
                    for (int j = 0; j < 16; ++j) sv[j] = __builtin_amdgcn_raw_buffer_load_b96(rS, lane * 16, (j >> 3) * 16384 + (j & 7) * 1024, 16);
                    SB();
                    s1 = 0.f; s2 = 0.f; s3 = 0.f;
#pragma unroll
                    for (int j = 0; j < 16; ++j) {
                        const unsigned u0 = sv[j][0], u1 = sv[j][1], u2 = sv[j][2];
                        s1 += __uint_as_float(u0); s2 += __uint_as_float(u1); s3 += __uint_as_float(u2);
                    }
                    const float z = (s1 - s1) + (s2 - s2) + (s3 - s3);
                    if (__builtin_expect(__all(z == 0.f), 1) || spin > SPIN_CAP) break;
                    unsigned mx = 0u;
#pragma unroll
                    for (int j = 0; j < 16; ++j) mx = max(max(mx, sv[j][0]), max(sv[j][1], sv[j][2]));
                    if (__all(mx != SENT)) break;
                }
                if (wid == 4) STAMP(4);
                const float mean = s1 * (1.0f / D_LEN);
                const float var = fmaxf(s2 * (1.0f / D_LEN) - mean * mean, 0.f);
                const float mb = rbf(mean), rden = __builtin_amdgcn_rcpf(rbf(__builtin_amdgcn_sqrtf(rbf(rbf(var) + 1e-5f))));
                const unsigned kb = wave_max_u32(r2key((s3 - mb * wsum) * rden, hp_ * 64 + lane));
                __builtin_amdgcn_raw_buffer_store_b32(kb, rKy, (lane >> 2) * 8192 + (lane & 3) * 256, 0, 16);
                {
                    const int cr_ = __builtin_amdgcn_readfirstlane((int)((0xffffu - (kb & 0xffffu)) & 63u));
                    const float m_ = __int_as_float(__builtin_amdgcn_readlane(__float_as_int(mb), cr_));
                    const float d_ = __int_as_float(__builtin_amdgcn_readlane(__float_as_int(rden), cr_));
                    u32x2 c = *(const LAS u32x2*)(lds + L_HT + (wq * 64 + cr_) * 256 + (((lane & 31) * 8) ^ ((cr_ & 15) * 16)));
#pragma unroll
                    for (int i = 0; i < 2; ++i) {
                        const unsigned u = c[i];
                        const unsigned dd = pk_bf16(bflo(u) - m_, bfhi(u) - m_);
                        const unsigned q = pk_bf16(bflo(dd) * d_, bfhi(dd) * d_);
                        c[i] = min(lane < 32 ? q : u, SENT - 1u);
                    }
                    __builtin_amdgcn_raw_buffer_store_b64(c, rC, (lane < 32 ? 2048 : 0) + ct * 256 + (lane & 31) * 8, 0, 16);
                }
                rstat[(wq * 64 + lane) * 2] = mb; rstat[(wq * 64 + lane) * 2 + 1] = rden;
                if (lane == 0) ctl[2 + wq] = (0xffffu - (kb & 0xffffu)) & 63u;
                WAIT_LGKM0();
                if (wid == 4) STAMP(5);
            }
            BARRIER();
#pragma unroll
            for (int i = WD_EARLY; i < 16; ++i) load_wd(i);
            if (slot < 112 && ((AQ << 4 >> wid) & 1)) {
                WAIT_VM0();
                if (wid == 5) STAMP(7);
                stage2(w2, slot, lw, lds + L_WD0 + lw * 8192, lds + L_WD1 + lw * 8192, abuf, lane);
                if (wid == 5) STAMP(11);
            }
            FLUSH();
            return;
        }
    }

    P2_DEFS;
    LAS float* red = (LAS float*)(lds + L_RED);
    if (sidx < 112) {
        const int slot = sidx, grp = slot >> 3, kq = slot & 7;
        const __amdgpu_buffer_rsrc_t rG = mk_rsrc(a.wg, D_LEN * F_LEN * 2), rU = mk_rsrc(a.wu, D_LEN * F_LEN * 2);
        auto pair = [&](int i) {
            const int d = wid * 16 + i * 2 + (lane >> 5), ky = (d & 3) | ((d >> 1) & 4), c = lane & 31;
            const int vgu = ((kq * 128 + d) * F_LEN + grp * 256 + (((c >> 1) ^ ky) * 16) + (c & 1) * 8) * 2;
            dma_nt(rG, (unsigned)(size_t)lds + wid * 8192 + i * 1024, vgu, 0);
            dma_nt(rU, (unsigned)(size_t)lds + 65536 + wid * 8192 + i * 1024, vgu ^ 512, 0);
        };
        if (wid >= 4) {
            __builtin_amdgcn_s_sleep(W_PRE_SL);
#pragma unroll
            for (int i = 0; i < W_PRE; ++i) pair(i);
        }
        BARRIER();
        if (wid < 4) {
#pragma unroll
            for (int i = 0; i < W_PRE; ++i) pair(i);
        }
#pragma unroll
        for (int i = W_PRE; i < 8; ++i) pair(i);
        const int bq_ = wid == 7 ? bquarter(0) : wid == 6 ? bquarter(1) : wid == 5 ? bquarter(2) : bquarter(3);
        u32x4 wv[16];
        if (wid >= 8 - BQ) {
            const __amdgpu_buffer_rsrc_t rD = mk_rsrc(a.wd + (size_t)slot * 32 * D_LEN, 32 * D_LEN * 2);
            if (wid == 7) {
#pragma unroll
                for (int i = 0; i < 16; ++i)
                    dma_nt(rD, (unsigned)(size_t)lds + L_YB + 16384 + i * 1024, wd_off(bq_, i, lane), i * (4 * D_LEN));
            } else {
#pragma unroll
                for (int i = 0; i < 16; ++i)
                    asm volatile("buffer_load_dwordx4 %0, %1, %2, %3 offen nt" : "=&v"(wv[i]) : "v"(wd_off(bq_, i, lane)), "s"(rD), "s"(i * (4 * D_LEN)) : "memory");
            }
            if (lane < 16) *(LAS unsigned*)(lds + L_YB + 4096 + (7 - wid) * 128 + 64 + lane * 4) = 0u;
        }
        const __amdgpu_buffer_rsrc_t rK = mk_rsrc(w2, wid == 0 ? (unsigned)(WS_CAND + 32 * 4096) : 0u);
        const int ok_ = lane < 40 ? (int)WS_KEYS + (xcd >> 2) * 8192 + (xcd & 3) * 256 + (lane - 32) * 16 : (int)(WS_CAND + 32 * 4096);
        const int oc = (int)WS_CAND + 2048 + kq * 256 + n * 16;
        int best = 0;
        u32x4 v = {0u, 0u, 0u, 0u};
        auto request = [&]() {
            asm volatile("" ::: "memory");
            v = LD16_SC1(rK, lane < 32 ? oc + best * 4096 : ok_);
            SB();
        };
        auto settled = [&]() {
            const unsigned mx = umax4(v), mn = min(min(v[0], v[1]), min(v[2], v[3]));
            unsigned fv = lane < 32 ? ~mx : mn | (unsigned)(lane >> 3 ^ 4);
            asm volatile("" : "+v"(fv));
            const bool fine = fv != 0u;
            unsigned km = max(mx, dppu<0xB1>(mx));
            km = max(km, dppu<0x4E>(km));
            km = max(km, dppu<0x141>(km));
            const int nb = (int)(((0xffffu - ((unsigned)__builtin_amdgcn_readlane((int)km, 32) & 0xffffu)) >> 6) & 31u);
            const bool same = nb == best;
            best = nb;
            return __all(fine) && same;
        };
        if (wid == 0) {
            request();
            WAIT_VM(0);
            settled();
            request();
        } else if (wid >= 8 - BQ) WAIT_VM(16);
        else WAIT_VM(0);
        BARRIER();
        if (wid == 0) STAMP(8);
        bf16x8 wfr[4][4];
        {
            const int q = (lane >> 2) & 3, ky = q | ((g & 1) << 2);
#pragma unroll
            for (int u = 0; u < 4; ++u) {
                const int tro = (u >> 1) * 65536 + (8 * g + q) * 512 + (((wid * 2 + (u & 1)) ^ ky) * 32) + (lane & 3) * 8;
#pragma unroll
                for (int ks = 0; ks < 4; ++ks) {
                    const bf16x4 w0 = __builtin_amdgcn_ds_read_tr16_b64_v4i16((LAS bf16x4*)(lds + tro + ks * 16384));
                    const bf16x4 w1 = __builtin_amdgcn_ds_read_tr16_b64_v4i16((LAS bf16x4*)(lds + tro + ks * 16384 + 2048));
                    wfr[u][ks] = __builtin_shufflevector(w0, w1, 0, 1, 2, 3, 4, 5, 6, 7);
                }
            }
            SB();
#pragma unroll
            for (int i = 0; i < 16; ++i) asm volatile("" : "+v"(wfr[i >> 2][i & 3]));
        }
        LAS unsigned char* img = lds + (wid == 7 ? L_YB + 16384 : (wid - 4) * 16384);
        if (BQ > 1) {
            WAIT_LGKM0();
            BARRIER();
            if (wid >= 8 - BQ && wid != 7) {
                WAIT_VM(0);
#pragma unroll
                for (int i = 0; i < 16; ++i) { asm volatile("" : "+v"(wv[i])); *(LAS u32x4*)(img + i * 1024 + lane * 16) = wv[i]; }
            }
        }
        const __amdgpu_buffer_rsrc_t rPo = mk_rsrc(w2 + WS_PRE, 112 * 2048);
        int po = n < 4 ? pre_off((grp ^ (n >> 1)) * 8 + wid) + kq * 256 + n * 64 + g * 16 : 112 * 2048;
        asm volatile("" : "+v"(po));
        if (wid == 0) {
#pragma unroll 1
            for (int spin = 0; spin <= SPIN_CAP && !settled(); ++spin) {
                __builtin_amdgcn_s_sleep(1);
                request();
            }
            __builtin_amdgcn_s_waitcnt(0x0F70);
            if (lane < 16) *(LAS u32x4*)(lds + L_RED + lane * 16) = v;
            WAIT_LGKM0();
            STAMP(9);
        }
        BARRIER();
        {
            f32x4 ac[4];
            bf16x8 xf[4];
#pragma unroll
            for (int ks = 0; ks < 4; ++ks) xf[ks] = *(const LAS bf16x8*)(lds + L_RED + ks * 64 + g * 16);
            SB();
#pragma unroll
            for (int u = 0; u < 4; ++u) ac[u] = (f32x4){0.f, 0.f, 0.f, 0.f};
#pragma unroll
            for (int ks = 0; ks < 4; ++ks)
#pragma unroll
                for (int u = 0; u < 4; ++u) ac[u] = __builtin_amdgcn_mfma_f32_16x16x32_bf16(wfr[u][ks], xf[ks], ac[u], 0, 0, 0);
            u32x4 sv;
#pragma unroll
            for (int r = 0; r < 4; ++r) sv[r] = min(__float_as_uint(sel4(n, ac[0][r], ac[1][r], ac[2][r], ac[3][r])), SENT - 1u);
            ST16_SC1(sv, rPo, po);
            SB();
            asm volatile("" :: "v"(v));
        }
        if (wid == 0) STAMP(14);
        if (wid >= 8 - BQ) {
            WAIT_VM(1);
            if (wid == 8 - BQ) STAMP(10);
            stage2(w2, slot, bq_, img, img + 8192, lds + L_YB + 4096 + (7 - wid) * 128, lane);
            if (wid == 8 - BQ) STAMP(15);
        }
    } else {
        const int rj = sidx - 112;
        if (wid == 4) {
            unsigned kk;
            const unsigned k = poll_winner(w2, xcd, lane, kk);
            if (lane == 0) ctl[4] = k;
            if (lane < 32) ctl[8 + lane] = kk;
            WAIT_LGKM0();
        }
        BARRIER();
        const unsigned kw = CTL(4);
        if (wid == 0) STAMP(16);
        {
            const unsigned kc = ctl[8 + wid * 4 + g];
            const __amdgpu_buffer_rsrc_t rL = mk_rsrc(w2 + WS_CAND + (size_t)wid * 16384, 16384);
            u32x2 hv2, nv2;
            for (int spin = 0;; ++spin) {
                asm volatile("" ::: "memory");
                hv2 = __builtin_amdgcn_raw_buffer_load_b64(rL, g * 4096 + (rj * 64 + n * 4) * 2, 0, 16);
                nv2 = __builtin_amdgcn_raw_buffer_load_b64(rL, g * 4096 + 2048 + (rj * 64 + n * 4) * 2, 0, 16);
                if (__all(max(max(hv2[0], hv2[1]), max(nv2[0], nv2[1])) != SENT) || spin > SPIN_CAP) break;
                __builtin_amdgcn_s_sleep(1);
            }
            u32x2 ov;
#pragma unroll
            for (int i = 0; i < 2; ++i) {
                const unsigned u = hv2[i], q = nv2[i];
                ov[i] = pk_bf16(bflo(u) + bflo(q), bfhi(u) + bfhi(q));
            }
            const int crow = (int)(0xffffu - (kc & 0xffffu)) & (S_LEN - 1);
            if (kc != kw) *(u32x2*)(a.out + (size_t)crow * D_LEN + rj * 64 + n * 4) = ov;
        }
        const int wrow = (int)(0xffffu - (kw & 0xffffu)) & (S_LEN - 1);
        const __amdgpu_buffer_rsrc_t rC = mk_rsrc(w2 + WS_CAND + (size_t)(wrow >> 6) * 4096, 4096);
        const __amdgpu_buffer_rsrc_t rPt = mk_rsrc(w2 + WS_PART, 112 * 4096);
        const int ocol = (rj >> 2) * 256 + ((lane >> 3) & 3) * 64 + (rj & 3) * 16 + (lane & 7) * 2, opos = (lane & 7) * 8 + ((lane >> 3) & 3);
        unsigned hv, hnv;
        for (int spin = 0;; ++spin) {
            asm volatile("" ::: "memory");
            hv = __builtin_amdgcn_raw_buffer_load_b32(rC, ocol * 2, 0, 16);
            hnv = __builtin_amdgcn_raw_buffer_load_b32(rC, 2048 + ocol * 2, 0, 16);
            if (__all(max(hv, hnv) != SENT) || spin > SPIN_CAP) break;
        }
        float sgm = rbf(1.0f / (1.0f + __expf(-key2r(kw))));
        GAS unsigned* optr = (GAS unsigned*)(a.out + (size_t)wrow * D_LEN + ocol);
        asm volatile("" : "+v"(sgm), "+v"(optr));
        u32x4 pv[4];
#pragma unroll 1
        for (int spin = 0;; ++spin) {
            asm volatile("" ::: "memory");
#pragma unroll
            for (int i = 0; i < 4; ++i) pv[i] = LD16_SC1(rPt, (wid + 8 * i) * 16384 + (rj >> 3) * 8192 + g * 2048 + ((rj & 7) * 64 + n * 4) * 4);
            if (__all(max(max(umax4(pv[0]), umax4(pv[1])), max(umax4(pv[2]), umax4(pv[3]))) != SENT) || spin > SPIN_CAP) break;
            __builtin_amdgcn_s_sleep(1);
        }
        if (wid == 0) STAMP(17);
        f32x4 sm;
#pragma unroll
        for (int j = 0; j < 4; ++j) {
            const unsigned u0 = pv[0][j], u1 = pv[1][j], u2 = pv[2][j], u3 = pv[3][j];
            sm[j] = sum_rows((__uint_as_float(u0) + __uint_as_float(u1)) + (__uint_as_float(u2) + __uint_as_float(u3)));
        }
        if (lane < 16) *(LAS f32x4*)(red + wid * 64 + lane * 4) = sm;
        WAIT_LGKM0();
        BARRIER();
        if (wid == 0) {
            float t0 = 0.f, t1 = 0.f;
#pragma unroll
            for (int w = 0; w < 8; ++w) { t0 += red[w * 64 + opos]; t1 += red[w * 64 + opos + 4]; }
            const float y0 = rbf(rbf(t0) * sgm), y1 = rbf(rbf(t1) * sgm);
            const unsigned o = pk_bf16(bflo(hv) + rbf(bflo(hnv) + y0), bfhi(hv) + rbf(bfhi(hnv) + y1));
            if (lane < 32) *optr = o;
            STAMP(18);
        }
        __builtin_amdgcn_s_waitcnt(0x0F70);
    }
    FLUSH();
}

extern "C" void kernel_launch(void* const* d_in, const int* in_sizes, int n_in, void* d_out, int out_size, void* d_ws,
                              size_t ws_size, hipStream_t stream) {
    Args a;
    a.x = (const bf16_t*)d_in[0];
    a.cr = (const bf16_t*)d_in[1];
    a.ci = (const bf16_t*)d_in[2];
    a.wre = (const bf16_t*)d_in[3];
    a.wim = (const bf16_t*)d_in[4];
    a.w_r = (const bf16_t*)d_in[6];
    a.wg = (const bf16_t*)d_in[7];
    a.wu = (const bf16_t*)d_in[8];
    a.wd = (const bf16_t*)d_in[9];
    a.out = (bf16_t*)d_out;
    a.ws = (char*)d_ws + (WS_END ? ((ws_size - ((size_t)24 << 20)) & ~(size_t)255) : 0);
    (void)hipFuncSetAttribute((const void*)fused_kernel, hipFuncAttributeMaxDynamicSharedMemorySize, LDS_BYTES);
    fused_kernel<<<256, 768, LDS_BYTES, stream>>>(a);
}
```

```cpp
#include <hip/hip_runtime.h>
#include <hip/hip_bf16.h>
#include <stdint.h>

typedef unsigned short bf16_t;
typedef short bf16x8 __attribute__((ext_vector_type(8)));
typedef short bf16x4 __attribute__((ext_vector_type(4)));
typedef float f32x4 __attribute__((ext_vector_type(4)));
typedef unsigned u32x4 __attribute__((ext_vector_type(4)));
typedef unsigned u32x2 __attribute__((ext_vector_type(2)));
typedef unsigned u32x3 __attribute__((ext_vector_type(3)));
typedef unsigned long long u64;
#define LAS __attribute__((address_space(3)))
#define GAS __attribute__((address_space(1)))

constexpr int S_LEN = 2048, D_LEN = 1024, F_LEN = 3584;
constexpr unsigned SENT = 0xFFFFFFFFu;
constexpr int SPIN_CAP = 1 << 17;

constexpr size_t WS_KEYS = 0;
constexpr size_t WS_FLAGS = 16384;
constexpr size_t WS_XRD = 524288;
constexpr size_t WS_FLAG1 = 262144;
constexpr size_t WS_TAG = 131072;
constexpr size_t WS_VOTE = WS_TAG + 4096;
constexpr size_t WS_PROBE = (size_t)20 << 20;
constexpr size_t WS_AT = (size_t)4 << 20;
constexpr size_t WS_Y = (size_t)12 << 20;
constexpr size_t WS_STATS = (size_t)16 << 20;
constexpr size_t WS_PART = WS_STATS + (512u << 10);
constexpr size_t WS_CAND = WS_STATS + (1024u << 10);
constexpr size_t WS_PRE = WS_STATS + (1152u << 10);
constexpr size_t FILL_BYTES = (size_t)1408 << 10;

__device__ __forceinline__ float bflo(unsigned v) { return __uint_as_float(v << 16); }
__device__ __forceinline__ float bfhi(unsigned v) { return __uint_as_float(v & 0xffff0000u); }
__device__ __forceinline__ unsigned pk_bf16(float lo, float hi) {
    typedef __bf16 bf2 __attribute__((ext_vector_type(2)));
    typedef float f2 __attribute__((ext_vector_type(2)));
    f2 v = {lo, hi};
    bf2 r = __builtin_convertvector(v, bf2);
    return __builtin_bit_cast(unsigned, r);
}
__device__ __forceinline__ float rbf(float x) { return bflo(pk_bf16(x, 0.f) & 0xffffu); }

template <int CTRL> __device__ __forceinline__ unsigned dppu(unsigned v) {
    return (unsigned)__builtin_amdgcn_update_dpp(0, (int)v, CTRL, 0xf, 0xf, true);
}
template <int CTRL> __device__ __forceinline__ float dppf(float v) { return __uint_as_float(dppu<CTRL>(__float_as_uint(v))); }
__device__ __forceinline__ void swap16(unsigned v, unsigned& a, unsigned& b) {
    auto r = __builtin_amdgcn_permlane16_swap(v, v, false, false);
    a = r[0]; b = r[1];
}
__device__ __forceinline__ void swap32(unsigned v, unsigned& a, unsigned& b) {
    auto r = __builtin_amdgcn_permlane32_swap(v, v, false, false);
    a = r[0]; b = r[1];
}
__device__ __forceinline__ float sum_rows(float v) {
    unsigned a, b;
    swap16(__float_as_uint(v), a, b); v = __uint_as_float(a) + __uint_as_float(b);
    swap32(__float_as_uint(v), a, b); v = __uint_as_float(a) + __uint_as_float(b);
    return v;
}
__device__ __forceinline__ float wave_sum(float v) {
    v += dppf<0xB1>(v);
    v += dppf<0x4E>(v);
    v += dppf<0x141>(v);
    v += dppf<0x140>(v);
    return sum_rows(v);
}
__device__ __forceinline__ unsigned wave_max_u32(unsigned v) {
    v = max(v, dppu<0xB1>(v));
    v = max(v, dppu<0x4E>(v));
    v = max(v, dppu<0x141>(v));
    v = max(v, dppu<0x140>(v));
    unsigned a, b;
    swap16(v, a, b); v = max(a, b);
    swap32(v, a, b); v = max(a, b);
    return v;
}
__device__ __forceinline__ float sel4(int g, float a, float b, float c, float d) {
    const float t0 = (g & 1) ? b : a, t1 = (g & 1) ? d : c;
    return (g & 2) ? t1 : t0;
}
__device__ __forceinline__ unsigned umax4(u32x4 v) { return max(max(v[0], v[1]), max(v[2], v[3])); }

__device__ __forceinline__ void touch_line(const void* p, unsigned lds_dst) {
    unsigned keep;
    asm volatile("s_mov_b32 %0, m0\n\ts_mov_b32 m0, %2\n\ts_nop 0\n\tglobal_load_lds_dword %1, off\n\ts_mov_b32 m0, %0"
                 : "=&s"(keep) : "v"(p), "s"(lds_dst) : "memory");
}
__device__ __forceinline__ __amdgpu_buffer_rsrc_t mk_rsrc(const void* p, unsigned bytes) {
    return __builtin_amdgcn_make_buffer_rsrc((void*)p, 0, (int)bytes, 0x00020000);
}
#define LDS_LOAD(w, v, o, pol) "buffer_load_" w " " v ", %0, %2 offen" o pol " lds"
#define LDS_M0 "s_mov_b32 m0, %1\n\ts_nop 0\n\t"
__device__ __forceinline__ void dma_nt(__amdgpu_buffer_rsrc_t r, unsigned dst, int voff, int soff) {
    asm volatile(LDS_M0 LDS_LOAD("dwordx4", "%3", "", " nt") :: "s"(r), "s"(dst), "s"(soff), "v"(voff) : "memory", "m0");
}
template <bool SC1> __device__ __forceinline__ void dma_word(__amdgpu_buffer_rsrc_t r, unsigned dst, int voff) {
    if constexpr (SC1) asm volatile(LDS_M0 LDS_LOAD("dword", "%3", "", " sc1") :: "s"(r), "s"(dst), "n"(0), "v"(voff) : "memory", "m0");
    else asm volatile(LDS_M0 LDS_LOAD("dword", "%3", "", "") :: "s"(r), "s"(dst), "n"(0), "v"(voff) : "memory", "m0");
}
__device__ __forceinline__ void dma_2k(__amdgpu_buffer_rsrc_t r, unsigned dst, int v0, int v1, int soff) {
    asm volatile(LDS_M0 LDS_LOAD("dwordx4", "%3", "", " sc1") "\n\t" LDS_LOAD("dwordx4", "%4", " offset:1024", " sc1")
                 :: "s"(r), "s"(dst), "s"(soff), "v"(v0), "v"(v1) : "memory", "m0");
}
template <bool SC1> __device__ __forceinline__ void dma_4k(__amdgpu_buffer_rsrc_t r, unsigned dst, const int (&v)[4], int soff) {
#define FOUR(pol) asm volatile(LDS_M0 LDS_LOAD("dwordx4", "%3", "", pol) "\n\t" LDS_LOAD("dwordx4", "%4", " offset:1024", pol) "\n\t" \
        LDS_LOAD("dwordx4", "%5", " offset:2048", pol) "\n\t" LDS_LOAD("dwordx4", "%6", " offset:3072", pol) \
        :: "s"(r), "s"(dst), "s"(soff), "v"(v[0]), "v"(v[1]), "v"(v[2]), "v"(v[3]) : "memory", "m0")
    if constexpr (SC1) FOUR(" sc1"); else FOUR("");
#undef FOUR
}
#define CODE_BYTES 26752
__device__ __forceinline__ __amdgpu_buffer_rsrc_t code_rsrc() {
    unsigned long long p; unsigned d;
    asm volatile("s_getpc_b64 %0\n1:\n\ts_mov_b32 %1, 1b-_Z12fused_kernel4Args" : "=s"(p), "=s"(d));
    return __builtin_amdgcn_make_buffer_rsrc((void*)(p - d), 0, CODE_BYTES, 0x00020000);
}
#define LD16_SC1(rs, off) __builtin_amdgcn_raw_buffer_load_b128(rs, off, 0, 16)
#define ST16_SC1(v, rs, off) __builtin_amdgcn_raw_buffer_store_b128(v, rs, off, 0, 16)

constexpr int BT = 128, BK = 64, NSTAGE = 4;
constexpr int TILE_BYTES = BT * BK * 2;
constexpr int STAGE_BYTES = 2 * TILE_BYTES;
constexpr int RING_BYTES = NSTAGE * STAGE_BYTES;
constexpr int L_DUMMY = RING_BYTES;
constexpr int L_CTL = RING_BYTES + 256;
constexpr int L_RED = RING_BYTES + 512;

#define WAIT_VM(n) asm volatile("s_waitcnt vmcnt(" #n ")" ::: "memory")
#define WAIT_VM0() do { asm volatile("" ::: "memory"); __builtin_amdgcn_s_waitcnt(0x0F70); asm volatile("s_waitcnt vmcnt(0)" ::: "memory"); } while (0)
#define WAIT_LGKM0() asm volatile("s_waitcnt lgkmcnt(0)" ::: "memory")
#define BARRIER() do { asm volatile("" ::: "memory"); __builtin_amdgcn_s_barrier(); asm volatile("" ::: "memory"); } while (0)

__device__ __forceinline__ int prow(int rho) {
    const int w = rho >> 6, m = (rho >> 4) & 3, q = (rho >> 2) & 3, r = rho & 3;
    return w * 64 + (m >> 1) * 32 + q * 8 + (m & 1) * 4 + r;
}
__device__ __forceinline__ unsigned r2key(float r, int row) {
    const unsigned u = pk_bf16(r, 0.f) & 0xffffu;
    const unsigned o = (u & 0x8000u) ? (~u & 0xffffu) : (u | 0x8000u);
    return (o << 16) | (0xffffu - (unsigned)row);
}
__device__ __forceinline__ float key2r(unsigned k) {
    const unsigned o = k >> 16;
    return bflo((o & 0x8000u) ? (o & 0x7fffu) : (~o & 0xffffu));
}
#define RESET_T 2
#define RESET_U 3
#define VOTE_T 4
#define NSAMP 4
#define PSM 0x008800
#define FIN_PRIO 3
#define W_PRE 2
#define W_PRE_SL 20
#define AQ 10
constexpr int BQ = 4 - __builtin_popcount(AQ);
constexpr int bquarter(int k) {
    for (int q = 3; q >= 0; --q) if (!((AQ >> q) & 1) && k-- == 0) return q;
    return 0;
}
#define POLL_T 12
#define AUX_SLEEP 6
#define WD_EARLY 4
#define TAIL_PRIO 3
#define STAT_DELAY 6
#define CODE_WG 1
#define AUX_STAY 1
#define T_DELAY 0
#define WS_END 1
#define CATCH1 4
#define CPF_MASK1 0x1080u
#define CPF_MASK2 0x80u
#define CPF_STAG 5
#define CPF_FIX 0x1000u
#define OUT_AUX 0
#define POLL_SLEEP 4
__device__ __forceinline__ float dot2bf(unsigned a, unsigned b, float c) {
    typedef __bf16 bf2 __attribute__((ext_vector_type(2)));
    return __builtin_amdgcn_fdot2_f32_bf16(__builtin_bit_cast(bf2, a), __builtin_bit_cast(bf2, b), c, false);
}

#define STAMP(i) do { } while (0)
#define CSTAMP(i) do { } while (0)
#define FLUSH() do { } while (0)
#define TSV do { } while (0)

#define KSHIFT 6

constexpr int L_HT = ((32 + KSHIFT) % NSTAGE) * STAGE_BYTES;
constexpr int L_WD0 = ((33 + KSHIFT) % NSTAGE) * STAGE_BYTES;
constexpr int L_WD1 = ((34 + KSHIFT) % NSTAGE) * STAGE_BYTES;
constexpr int L_YB = RING_BYTES;
constexpr int L_ATAB = L_YB;
constexpr int LDS_BYTES = RING_BYTES + 32768;

#define SB() __builtin_amdgcn_sched_barrier(0)
template <int N> __device__ __forceinline__ void wait_vm() { asm volatile("s_waitcnt vmcnt(%0)" :: "n"(N) : "memory"); }
__device__ __forceinline__ int at_tile(int c, int par) { return ((c << 8) & 0x78000) | (((c << 2) & 0x2000) ^ par); }
__device__ __forceinline__ int at_panel(int ep) { return ep << 19; }
__device__ __forceinline__ int pre_off(int slot) { const int s_ = slot >= 56, l = slot - 56 * s_; return (l >> 2) * 16384 + s_ * 8192 + (l & 3) * 2048; }
static_assert(KSHIFT >= 2 && KSHIFT <= 8 && KSHIFT % 2 == 0, "flag samples must have replaced older contents of their slot; even tile counts");
struct Ring {
    __amdgpu_buffer_rsrc_t rP, rQ, rF;
    u64 bq[2];
    int voP[4], voQ[4], voF;
    LAS unsigned char* fl;
    unsigned lb;
    int sp, sq;
    int poff, par;
    int tp;
    int tq, ti;
    bool img;
    template <int LD, bool PIMG> __device__ __forceinline__ void setup(int lw, int lane) {
        const int r = lane >> 3, v0 = (PIMG ? r : (r & 3) + (r & 4) * 2) * (LD * 2) + (((lane & 7) ^ (lane >> 4)) << 4);
#pragma unroll
        for (int j = 0; j < 4; ++j) {
            voQ[j] = (v0 ^ ((j & 1) * 64)) + (PIMG ? j * 8 : (j & 1) * 16 + (j >> 1) * 4) * (LD * 2) - j * 1024;
            voP[j] = PIMG ? (lw >> 1) * 16384 + (lw & 1) * 4096 + lane * 16 : voQ[j];
        }
        img = PIMG;
    }
    __device__ __forceinline__ void seek(int col, int pof) {
        sq = col; poff = pof;
        sp = (img ? at_tile(sq, par) : sq) + poff;
    }
    __device__ __forceinline__ void step() {
        sq = (sq + BK * 2) & 4095;
        sp = (img ? at_tile(sq, par) : sq) + poff;
        asm volatile("" : "+s"(sq), "+s"(sp));
    }
    __device__ __forceinline__ void sample() { dma_word<true>(rF, (unsigned)(size_t)fl, voF); }
    __device__ __forceinline__ unsigned peek(unsigned addr) {
        unsigned v;
        asm volatile("ds_read_b32 %0, %1\n\ts_waitcnt lgkmcnt(0)" : "=v"(v) : "v"(addr) : "memory");
        return v;
    }
    __device__ __forceinline__ bool seen() { return __builtin_amdgcn_readfirstlane(peek((unsigned)(size_t)fl)) == 1u; }
    __device__ __forceinline__ void gate(unsigned want, unsigned sa, unsigned need, unsigned& have) {
#pragma unroll 1
        for (int spin = 0; spin <= SPIN_CAP && (need & ~have) != 0u; ++spin) {
            if (spin) { sample(); WAIT_VM(0); __builtin_amdgcn_s_sleep(1); }
            const unsigned long long m = __ballot(peek(sa) == want);
            have = (unsigned)m & (unsigned)(m >> 32);
        }
    }
    template <int AUX> __device__ __forceinline__ void issueP(LAS unsigned char* base) { dma_4k<AUX != 0>(rP, (unsigned)(size_t)base, voP, sp); }
    __device__ __forceinline__ void issueQ(LAS unsigned char* base) { dma_4k<false>(rQ, (unsigned)(size_t)base + TILE_BYTES, voQ, sq); }
    __device__ __forceinline__ void issue(LAS unsigned char* lds) {
        issueP<0>(lds + lb);
        issueQ(lds + lb);
        SB();
        lb = (lb + STAGE_BYTES) & (RING_BYTES - 1);
        asm volatile("" : "+s"(lb));
        SB();
    }
    __device__ __forceinline__ void next1() {
        ++ti;
        sp = sq = __builtin_amdgcn_readlane(tq, ti);
        asm volatile("s_nop 4" : "+s"(sq), "+s"(sp), "+s"(ti));
        SB();
    }
    template <int CAT> __device__ __forceinline__ void ramp(LAS unsigned char* lds) {
#pragma unroll 1
        for (int i = 1; i <= CAT + 1; ++i) {
            for (int z = (PSM >> (4 * i)) & 15; z > 0; --z) __builtin_amdgcn_s_sleep(1);
            issue(lds);
            next1();
            if (i == CAT + 1) continue;
            WAIT_VM(8);
            BARRIER();
        }
    }
    template <bool P2, bool SAMP, int W = 16> __device__ __forceinline__ void steady(int cnt) {
        int t, u;
#define LD(v, r, s, o) "buffer_load_dwordx4 " v ", " r ", " s " offen" o " lds\n\t"
        if constexpr (P2) {
#define BODY(SMP) asm volatile("s_mov_b32 m0, %0\n\ts_branch 2f\n1:\n\ts_waitcnt vmcnt(%15)\n\ts_barrier\n2:\n\t" \
            LD("%7", "%6", "%2", " sc1") "s_bitcmp1_b32 %1, 12\n\ts_cselect_b64 s[96:97], %20, %19\n\t" \
            LD("%7", "%6", "%2", " offset:1024 sc1") "s_and_b32 %4, %1, 0xfff\n\ts_add_i32 %3, %3, 1\n\t" \
            LD("%7", "%6", "%2", " offset:2048 sc1") "v_readlane_b32 %1, %12, %3\n\t" LD("%7", "%6", "%2", " offset:3072 sc1") \
            SMP "s_add_i32 m0, %0, 0x4000\n\ts_add_i32 %0, %0, 0x8000\n\t" \
            LD("%8", "%5", "%4", "") "v_readlane_b32 %2, %13, %3\n\ts_and_b32 %0, %0, 0x1ffff\n\t" LD("%9", "%5", "%4", " offset:1024") "s_cmp_lg_u32 %3, %14\n\t" \
            LD("%10", "%5", "%4", " offset:2048") LD("%11", "%5", "%4", " offset:3072") "s_mov_b32 m0, %0\n\ts_cbranch_scc1 1b\n\ts_waitcnt vmcnt(%15)\n\ts_barrier" \
            : "+s"(lb), "+s"(sq), "+s"(sp), "+s"(ti), "=&s"(t), "+{s[96:99]}"(rQ) \
            : "s"(rP), "v"(voP[0]), "v"(voQ[0]), "v"(voQ[1]), "v"(voQ[2]), "v"(voQ[3]), "v"(tq), "v"(tp), "s"(cnt), "n"(W), \
              "s"(rF), "v"(voF), "s"((unsigned)(size_t)fl), "s"(bq[0]), "s"(bq[1]) : "memory", "scc", "m0")
            if constexpr (SAMP) BODY("s_mov_b32 m0, %18\n\ts_nop 0\n\tbuffer_load_dword %17, %16, 0 offen sc1 lds\n\t");
            else BODY("");
#undef BODY
            (void)u;
        } else {
            asm volatile("s_mov_b32 m0, %0\n\ts_branch 2f\n1:\n\ts_waitcnt vmcnt(16)\n\ts_barrier\n2:\n\t"
                LD("%6", "%4", "%1", "") LD("%7", "%4", "%1", " offset:1024") LD("%8", "%4", "%1", " offset:2048") LD("%9", "%4", "%1", " offset:3072")
                "s_add_i32 m0, %0, 0x4000\n\ts_mov_b32 %3, %1\n\t"
                LD("%6", "%5", "%3", "") "s_add_i32 %2, %2, 1\n\ts_add_i32 %0, %0, 0x8000\n\t"
                LD("%7", "%5", "%3", " offset:1024") "s_and_b32 %0, %0, 0x1ffff\n\tv_readlane_b32 %1, %10, %2\n\t"
                LD("%8", "%5", "%3", " offset:2048") "s_cmp_lg_u32 %2, %11\n\t"
                LD("%9", "%5", "%3", " offset:3072") "s_mov_b32 m0, %0\n\ts_cbranch_scc1 1b\n\ts_waitcnt vmcnt(16)\n\ts_barrier"
                : "+s"(lb), "+s"(sq), "+s"(ti), "=&s"(t)
                : "s"(rP), "s"(rQ), "v"(voQ[0]), "v"(voQ[1]), "v"(voQ[2]), "v"(voQ[3]), "v"(tq), "n"(D_LEN / BK)
                : "memory", "scc", "m0");
            (void)cnt;
            (void)u;
        }
#undef LD
    }
};

template <bool PLIN, bool FIRST, class Pre>
__device__ __forceinline__ void compute_loop(const LAS unsigned char* lds, f32x4 (&acc)[4][4], bf16x8 (&p0)[4], bf16x8 (&p1)[4], int NT, const unsigned (&ea)[2],
                                             int wp, int wq, int lane, Pre pre) {
    const int fr = lane & 15, fq = lane >> 4, key = (fr >> 1) & 7;
    const unsigned l0 = (unsigned)(size_t)lds;
    const unsigned oP0 = l0 + (PLIN ? wp * 8192 + lane * 16 : (wp * 64 + fr) * 128 + ((fq ^ key) * 16));
    const unsigned oP1 = l0 + (PLIN ? wp * 8192 + 1024 + lane * 16 : (wp * 64 + fr) * 128 + (((4 + fq) ^ key) * 16));
    const unsigned oQ0 = l0 + TILE_BYTES + (wq * 64 + fr) * 128 + ((fq ^ key) * 16);
    const unsigned oQ1 = l0 + TILE_BYTES + (wq * 64 + fr) * 128 + (((4 + fq) ^ key) * 16);
    unsigned aP0 = oP0, aQ0 = oQ0, aP1 = oP1, aQ1 = oQ1;
    bf16x8 r0[4], r1[4], q0[4], q1[4];

#pragma unroll
    for (int i = 0; i < 16; ++i) asm volatile("" : "+v"(acc[i >> 2][i & 3]));
    asm volatile("" : "+v"(aP0), "+v"(aQ0), "+v"(aP1), "+v"(aQ1));
#define MM(pp, qq, m, n) do { acc[m][n] = __builtin_amdgcn_mfma_f32_16x16x32_bf16(pp[m], qq[n], acc[m][n], 0, 0, 0); SB(); } while (0)
#define RD(dst, i, a, st) do { dst[i] = *(const LAS bf16x8*)((const LAS unsigned char*)(size_t)(a) + (st) * STAGE_BYTES + (i) * 2048); SB(); } while (0)
#define ADR(a) do { a ^= 2 * STAGE_BYTES; asm volatile("" : "+v"(a)); SB(); } while (0)
#define ADP(a, e) do { if (PLIN) { a = t == 0 ? e : a; asm volatile("" : "+v"(a)); } SB(); } while (0)
    __builtin_amdgcn_s_waitcnt(0xC07F);
    SB();
    if (FIRST) BARRIER();
    SB();
    RD(p0, 0, aP0, 0); RD(q0, 0, aQ0, 0); RD(p1, 0, aP1, 0); RD(q1, 0, aQ1, 0); RD(p1, 1, aP1, 0); RD(p0, 1, aP0, 0);
    RD(p0, 2, aP0, 0); RD(p1, 2, aP1, 0); RD(p1, 3, aP1, 0); RD(p0, 3, aP0, 0);
    pre();
    SB();
    int t = NT;
#pragma unroll 1
    do {
        MM(p0, q0, 0, 0); MM(p1, q1, 0, 0); RD(q0, 1, aQ0, 0);
        MM(p1, q1, 1, 0); MM(p0, q0, 1, 0); RD(q1, 1, aQ1, 0);
        MM(p0, q0, 2, 0); MM(p1, q1, 2, 0); RD(q0, 2, aQ0, 0);
        MM(p1, q1, 3, 0); MM(p0, q0, 3, 0); RD(q1, 2, aQ1, 0);
        MM(p0, q0, 3, 1); MM(p1, q1, 3, 1); RD(q0, 3, aQ0, 0);
        MM(p1, q1, 2, 1); MM(p0, q0, 2, 1); RD(q1, 3, aQ1, 0);
        MM(p0, q0, 1, 1); MM(p1, q1, 1, 1);
        MM(p1, q1, 0, 1); MM(p0, q0, 0, 1);
        MM(p0, q0, 0, 2); MM(p1, q1, 0, 2); __builtin_amdgcn_s_waitcnt(0xC07F); SB(); BARRIER(); SB();
        MM(p1, q1, 1, 2); RD(r0, 0, aP0, 1); MM(p0, q0, 1, 2); RD(q0, 0, aQ0, 1);
        MM(p0, q0, 2, 2); RD(r1, 0, aP1, 1); MM(p1, q1, 2, 2); RD(q1, 0, aQ1, 1);
        MM(p1, q1, 3, 2); MM(p0, q0, 3, 2); RD(r1, 1, aP1, 1);
        MM(p0, q0, 3, 3); RD(r0, 1, aP0, 1); MM(p1, q1, 3, 3);
        MM(p1, q1, 2, 3); RD(r0, 2, aP0, 1); MM(p0, q0, 2, 3); RD(r1, 2, aP1, 1);
        MM(p0, q0, 1, 3); MM(p1, q1, 1, 3); RD(r1, 3, aP1, 1);
        MM(p1, q1, 0, 3); RD(r0, 3, aP0, 1); MM(p0, q0, 0, 3);

        MM(r0, q0, 0, 0); MM(r1, q1, 0, 0); RD(q0, 1, aQ0, 1);
        MM(r1, q1, 1, 0); MM(r0, q0, 1, 0); RD(q1, 1, aQ1, 1);
        MM(r0, q0, 2, 0); MM(r1, q1, 2, 0); RD(q0, 2, aQ0, 1);
        MM(r1, q1, 3, 0); MM(r0, q0, 3, 0); RD(q1, 2, aQ1, 1);
        MM(r0, q0, 3, 1); MM(r1, q1, 3, 1); RD(q0, 3, aQ0, 1);
        MM(r1, q1, 2, 1); t -= 2; asm volatile("" : "+s"(t)); SB(); MM(r0, q0, 2, 1); RD(q1, 3, aQ1, 1);
        MM(r0, q0, 1, 1); ADR(aP0); MM(r1, q1, 1, 1); ADR(aP1);
        MM(r1, q1, 0, 1); ADP(aP0, ea[0]); ADR(aQ0); MM(r0, q0, 0, 1); ADP(aP1, ea[1]);
        MM(r0, q0, 0, 2); ADR(aQ1); MM(r1, q1, 0, 2); __builtin_amdgcn_s_waitcnt(0xC07F); SB(); BARRIER(); SB();
        MM(r1, q1, 1, 2); RD(p0, 0, aP0, 0); MM(r0, q0, 1, 2); RD(q0, 0, aQ0, 0);
        MM(r0, q0, 2, 2); RD(p1, 0, aP1, 0); MM(r1, q1, 2, 2); RD(q1, 0, aQ1, 0);
        MM(r1, q1, 3, 2); MM(r0, q0, 3, 2); RD(p1, 1, aP1, 0);
        MM(r0, q0, 3, 3); RD(p0, 1, aP0, 0); MM(r1, q1, 3, 3);
        MM(r1, q1, 2, 3); RD(p0, 2, aP0, 0); MM(r0, q0, 2, 3); RD(p1, 2, aP1, 0);
        MM(r0, q0, 1, 3); MM(r1, q1, 1, 3); RD(p1, 3, aP1, 0);
        MM(r1, q1, 0, 3); RD(p0, 3, aP0, 0); MM(r0, q0, 0, 3);
    } while (t != 0);
    SB();
#undef MM
#undef RD
#undef ADR
#undef ADP
}

struct Args {
    const bf16_t* x; const bf16_t* wre; const bf16_t* wim; char* ws; const bf16_t* cr; const bf16_t* ci;
    const bf16_t* w_r; bf16_t* out;
    const bf16_t* wg; const bf16_t* wu; const bf16_t* wd;
};
struct Late { const bf16_t* wg; const bf16_t* wu; const bf16_t* wd; };
__device__ __forceinline__ Late late() {
    typedef const __attribute__((address_space(4))) Args* KP;
    KP p = (KP)__builtin_amdgcn_kernarg_segment_ptr();
    asm volatile("" : "+s"(p));
    Late l = {p->wg, p->wu, p->wd};
    asm volatile("" : "+s"(l.wg), "+s"(l.wu), "+s"(l.wd));
    return l;
}

__device__ __forceinline__ unsigned poll_winner(const char* ws, int rep, int lane, unsigned& mine) {
    GAS unsigned* kp = (GAS unsigned*)(ws + WS_KEYS + (size_t)(rep >> 2) * 8192 + (rep & 3) * 256) + (lane & 31);
    unsigned kw;
    for (int spin = 0;; ++spin) {
            asm volatile("" ::: "memory");
        kw = __hip_atomic_load(kp, __ATOMIC_RELAXED, __HIP_MEMORY_SCOPE_AGENT);
        if (__all(kw != 0u) || spin > SPIN_CAP) break;
        __builtin_amdgcn_s_sleep(POLL_SLEEP);
    }
    mine = kw;
    return __builtin_amdgcn_readfirstlane(wave_max_u32(kw));
}

__device__ __forceinline__ void stage2(char* w2, int slot, int lw, LAS unsigned char* img0, LAS unsigned char* img1, LAS unsigned char* abuf, int lane) {
    const int g = lane >> 4, n = lane & 15;
    const __amdgpu_buffer_rsrc_t rPi = mk_rsrc(w2 + WS_PRE + pre_off(slot), 2048);
    unsigned pp[8];
    auto request = [&]() {
        asm volatile("" ::: "memory");
#pragma unroll
        for (int k = 0; k < 8; ++k) pp[k] = __builtin_amdgcn_raw_buffer_load_b32(rPi, k * 256 + lane * 4, 0, 16);
        SB();
    };
    request();
    bf16x8 wf[16];
    {
        const int q = (lane >> 2) & 3, ky = q | ((g & 1) << 2);
#pragma unroll
        for (int j = 0; j < 16; ++j) {
            LAS unsigned char* t = (g < 2 ? img0 : img1) + (8 * (g & 1) + q) * 512 + (((j & 7) ^ ky) + (j & 8)) * 32 + (lane & 3) * 8;
            const bf16x4 w0 = __builtin_amdgcn_ds_read_tr16_b64_v4i16((LAS bf16x4*)t);
            const bf16x4 w1 = __builtin_amdgcn_ds_read_tr16_b64_v4i16((LAS bf16x4*)(t + 2048));
            wf[j] = __builtin_shufflevector(w0, w1, 0, 1, 2, 3, 4, 5, 6, 7);
        }
        SB();
#pragma unroll
        for (int j = 0; j < 16; ++j) asm volatile("" : "+v"(wf[j]));
    }
    unsigned aa[5], pto = lane * 16;
#pragma unroll
    for (int m = 0; m < 4; ++m) aa[m] = (unsigned)(size_t)abuf + ((n >> 2) == m ? 0 : 64) + g * 16;
    aa[4] = (unsigned)(size_t)abuf + (lane & 31) * 2;
    __amdgpu_buffer_rsrc_t rPt = mk_rsrc(w2 + WS_PART + __builtin_amdgcn_readfirstlane((slot >> 2) * 16384 + (lw >> 1) * 8192 + (slot & 3) * 2048 + (lw & 1) * 1024), 1024);
    asm volatile("" : "+v"(aa[0]), "+v"(aa[1]), "+v"(aa[2]), "+v"(aa[3]), "+v"(aa[4]), "+v"(pto));
    asm volatile("" : "+s"(rPt));
    SB();
#pragma unroll 1
    for (int spin = 0; spin <= SPIN_CAP && !__all(max(max(max(pp[0], pp[1]), max(pp[2], pp[3])), max(max(pp[4], pp[5]), max(pp[6], pp[7]))) != SENT); ++spin) request();
    const float sv = ((__uint_as_float(pp[0]) + __uint_as_float(pp[1])) + (__uint_as_float(pp[2]) + __uint_as_float(pp[3]))) +
                     ((__uint_as_float(pp[4]) + __uint_as_float(pp[5])) + (__uint_as_float(pp[6]) + __uint_as_float(pp[7])));
    unsigned ug, uu;
    swap32(__float_as_uint(sv), ug, uu);
    const float gg = rbf(__uint_as_float(ug)), up = rbf(__uint_as_float(uu));
    const unsigned act = pk_bf16(rbf(gg * __builtin_amdgcn_rcpf(1.0f + __expf(-gg))) * up, 0.f);
    *(volatile LAS unsigned short*)(size_t)aa[4] = (unsigned short)act;
    bf16x8 af[4];
#pragma unroll
    for (int m = 0; m < 4; ++m) af[m] = *(volatile LAS bf16x8*)(size_t)aa[m];
    u32x4 pv;
#pragma unroll
    for (int q = 0; q < 4; ++q) {
        f32x4 d = {0.f, 0.f, 0.f, 0.f};
#pragma unroll
        for (int m = 0; m < 4; ++m) d = __builtin_amdgcn_mfma_f32_16x16x32_bf16(af[m], wf[4 * q + m], d, 0, 0, 0);
        const float e = d[0];
        pv[q] = min(__float_as_uint(e), SENT - 1u);
    }
    ST16_SC1(pv, rPt, pto);
}
__device__ __forceinline__ int wd_off(int lw, int i, int lane) {
    const int r = 2 * (i & 5) + (lane >> 5), ky = (r & 3) | ((r >> 1) & 4), c = lane & 31;
    return (lane >> 5) * (D_LEN * 2) + lw * 512 + (((c >> 1) ^ ky) * 2 + (c & 1)) * 16;
}

__global__ __launch_bounds__(768, 3) void fused_kernel(const Args a) {
    extern __shared__ __attribute__((aligned(16))) unsigned char smem[];
    constexpr int K = S_LEN, NT1 = D_LEN / BK;
    const int tid = threadIdx.x, wid = __builtin_amdgcn_readfirstlane(tid >> 6), lane = tid & 63;
    const int xcd = blockIdx.x & 7, idx = blockIdx.x >> 3;
    TSV;
    LAS unsigned char* lds = (LAS unsigned char*)smem;
    if (idx < CODE_WG && wid < 4) dma_word<false>(code_rsrc(), (unsigned)(size_t)lds + L_DUMMY, tid * 128);
    SB();
    const int h = xcd & 1, xq = xcd >> 1, rt = xq * 4 + (idx & 3), ct = idx >> 2;
    volatile LAS unsigned* ctl1 = (volatile LAS unsigned*)(lds + L_CTL);
#define NBR(k) ((rt + (k)) & 3)
    unsigned kx;
    Late lt;
    Ring ring;
    if (__builtin_expect((wid & 12) == 4, 1)) {
        __builtin_amdgcn_s_setprio(3);
        ring.template setup<D_LEN, false>(wid - 4, lane);
        ring.lb = (wid - 4) * 4096;
        auto ord1 = [&](int i) {
            const int q = (i - idx - (idx >> 2)) & 3;
            return ((xq * 4 + h * 8 + i + 4 - (q == 0 ? 0 : q == 2 ? 4 : 2)) & 15) * 128;
        };
        ring.poff = 0;
        ring.sp = ring.sq = ord1(0);
        unsigned ox = (unsigned)(rt * 4 + wid - 4) * (32 * D_LEN * 2), ow = (unsigned)(ct * 4 + wid - 4) * (32 * D_LEN * 2);
        asm volatile("" : "+s"(ring.sq), "+s"(ring.lb), "+s"(ox), "+s"(ow), "+v"(ring.voQ[0]), "+v"(ring.voQ[1]), "+v"(ring.voQ[2]), "+v"(ring.voQ[3]));
        SB();
        ring.sp = ring.sq;
#pragma unroll
        for (int j = 0; j < 4; ++j) ring.voP[j] = ring.voQ[j];
        ring.rP = mk_rsrc((const char*)a.x + ox, 32 * D_LEN * 2);
        ring.rQ = mk_rsrc((const char*)(h == 0 ? a.wre : a.wim) + ow, 32 * D_LEN * 2);
        ring.issue(lds);
        __builtin_amdgcn_s_setprio(0);
        ring.tq = ord1(lane);
        ring.ti = 0;
        ring.next1();
        ring.par = (ct & 4) << 11;
        ring.template ramp<CATCH1>(lds);
        ring.template steady<false, false>(NT1 - 2 - CATCH1);
        ring.template setup<K, true>(wid - 4, lane);
        ring.rQ = mk_rsrc((h == 0 ? a.cr : a.ci) + (size_t)(rt * 4 + wid - 4) * 32 * K, 32 * K * 2);
        ring.seek(rt * 256, 0);
        LAS unsigned char* l0 = lds + (wid - 4) * 4096;
        ring.issueQ(l0); ring.step();
        WAIT_VM(12); BARRIER();
        ring.issueQ(l0 + STAGE_BYTES);
        ring.seek((xq * 4 + NBR(1)) * 256, 0);
        WAIT_VM(8); BARRIER();
        ring.issueQ(l0 + 2 * STAGE_BYTES);
        ring.seek((xq * 4 + NBR(2)) * 256, 0);
        BARRIER();
        ring.issueQ(l0 + 3 * STAGE_BYTES);
        kx = (unsigned)__builtin_amdgcn_readfirstlane((int)ctl1[4]);
        lt = late();
        SB();
    }
    char* ws = a.ws + ((0ull - (unsigned long long)a.ws) & 0x1fffffull);
    const int g = lane >> 4, n = lane & 15;
    LAS float* tab = (LAS float*)(lds + L_RED);
    if (wid >= 8) {
        const int aw = wid - 8;
        GAS u64* tagp = (GAS u64*)(ws + WS_TAG);
        if (AUX_SLEEP) __builtin_amdgcn_s_sleep(AUX_SLEEP);
        u64 tag0 = __hip_atomic_load(tagp, __ATOMIC_RELAXED, __HIP_MEMORY_SCOPE_AGENT);
        SB();
        const int rho = (aw < 2 ? ct : (xq & 1) * 4 + (idx & 3)) * 16 + (aw & 1) * 8 + (n >> 1);
        unsigned fa = (unsigned)(size_t)lds + (aw >> 1) * TILE_BYTES + rho * 128 + ((((n & 1) * 4 + g) ^ ((rho >> 1) & 7)) * 16);
        constexpr int NIL = 1 << 30;
        const int at = tid - 512;
        __amdgpu_buffer_rsrc_t rZ = mk_rsrc(ws, 18u << 20);
        int z0 = (int)WS_STATS + blockIdx.x * (int)(FILL_BYTES / 256) + at * 16;
        int z1 = aw < 2 ? (at < (int)(FILL_BYTES / 4096) - 256 ? z0 + 4096 : NIL) : aw == 2 ? (lane < 10 ? blockIdx.x * 160 + lane * 16 : NIL)
               : lane < 32 ? (int)WS_FLAG1 + (lane >> 4) * 65536 + h * 32768 + (rt >> 3) * 16384 + ((lane >> 3) & 1) * 8192 + ct * 1024 + (rt & 7) * 128 + (lane & 7) * 16
               : NIL;
        u32x4 zv = {SENT, SENT, SENT, SENT};
        asm volatile("" : "+v"(fa), "+v"(z0), "+v"(z1), "+v"(zv), "+s"(rZ));
        f32x4 d1 = {0.f, 0.f, 0.f, 0.f}, d2 = {0.f, 0.f, 0.f, 0.f};
        u32x4 ones = {0x3F803F80u, 0x3F803F80u, 0x3F803F80u, 0x3F803F80u};
        asm volatile("" : "+v"(ones), "+v"(d1), "+v"(d2));
        if (aw == 0) {
            const int j = lane < 4 ? ct * 4 + lane : (idx & 3) * 8 + lane - 4;
            dma_word<true>(rZ, (unsigned)(size_t)lds + L_DUMMY, lane >= 12 ? NIL : (int)WS_XRD + (j >> 4) * 65536 + (lane < 4 ? rt : 16 + h * 8 + ct) * 2048 + (j & 15) * 128);
        }
        const char* cb;
        __amdgpu_buffer_rsrc_t rR, rS, rV;
        unsigned mask, tb, wl, wh;
        int ti, ro, o0, o1;
        auto warm = [&](int t) { if (t >= 4 && ((mask >> t) & 1)) { touch_line(cb + (ti & 7) * 512, L_DUMMY); ti += 2; } };
        u32x4 w0v, w1v;
        float wsr = 0.f;
#pragma unroll
        for (int t = 0; t < 8; ++t) {
            SB();
            BARRIER(); SB();
            const bf16x8 f = *(const LAS bf16x8*)((const LAS unsigned char*)(size_t)fa + (t & 1) * STAGE_BYTES);
            SB();
            if (aw == 1) { STAMP(40 + t); if (!(t & 1)) CSTAMP(57 + t / 2); }
            if (t == 0) {
                rR = mk_rsrc(a.w_r, aw == 3 && h == 0 ? D_LEN * 2 : 0);
                rS = mk_rsrc(ws + WS_XRD, 131072);
                rV = mk_rsrc(ws + WS_VOTE, 512);
                asm volatile("" : "+s"(rR), "+s"(rS), "+s"(rV));
            }
            if (t == 1) {
                cb = (const char*)(h ? a.ci : a.cr) + (size_t)rt * (512u << 10) + ct * 65536 + (lane >> 2) * 4096 + (lane & 3) * 128;
                mask = aw == 1 ? CPF_MASK1 : aw == 2 ? CPF_MASK2 : 0u;
                mask = (mask & CPF_FIX) | ((mask & ~CPF_FIX) << (idx % CPF_STAG));
                ti = 2 * xq + aw - 1;
                asm volatile("" : "+v"(cb), "+s"(mask), "+s"(ti));
            }
            if (t == 2) {
                w0v = __builtin_amdgcn_raw_buffer_load_b128(rR, lane * 16, 0, 0); w1v = __builtin_amdgcn_raw_buffer_load_b128(rR, lane * 16, 1024, 0);
                ro = (n | 2) != g * 4 + 2 ? 131072 : (aw < 2 ? h * 65536 + rt * 2048 : (xq >> 1) * 65536 + (16 + h * 8 + ct) * 2048) + rho * 16;
                asm volatile("" : "+v"(ro));
            }
            if (t == 3) {
                const int hi = aw == 3 ? 64 : 0;
                o0 = rt * 2048 + (hi + lane) * 16;
                o1 = o0 + (16 + h * 8 + ct - rt) * 2048;
                tb = (unsigned)(size_t)(tab + prow(hi + lane));
                asm volatile("" : "+v"(o0), "+v"(o1), "+v"(tb));
            }
            if (t == RESET_T) ST16_SC1(zv, rZ, z0);
            if (t == RESET_U) { if (aw >= 2) zv = (u32x4){0u, 0u, 0u, 0u}; ST16_SC1(zv, rZ, z1); }
            warm(t);
            if (t & 1) fa ^= 2 * STAGE_BYTES;
            d1 = __builtin_amdgcn_mfma_f32_16x16x32_bf16(f, __builtin_bit_cast(bf16x8, ones), d1, 0, 0, 0);
            if (aw < 2) d2 = __builtin_amdgcn_mfma_f32_16x16x32_bf16(f, f, d2, 0, 0, 0);
            if (t == 5) {
#pragma unroll
                for (int i = 0; i < 4; ++i) { wsr = dot2bf(w0v[i], 0x3F803F80u, wsr); wsr = dot2bf(w1v[i], 0x3F803F80u, wsr); }
                asm volatile("" : "+v"(wsr));
            }
            if (t == 1) {
                asm volatile("" : "+v"(tag0));
                const u64 want = tag0 * 6364136223846793005ull + 1442695040888963407ull;
                wl = (unsigned)want; wh = (unsigned)(want >> 32);
                asm volatile("" : "+v"(wl), "+v"(wh));
            }
            if (t == VOTE_T && aw == 3 && idx < 8) __builtin_amdgcn_raw_buffer_store_b64((u32x2){ctl1[5], wl}, rV, blockIdx.x * 8 + lane * 512, 0, 16);
            if (t == 6) WAIT_VM0();
            asm volatile("" : "+v"(d1), "+v"(d2), "+v"(fa));
        }
        SB(); BARRIER(); SB();
        {
            const float sa = d1[0] + d1[1], sb = d1[2] + d1[3];
            float sq = sel4(n, d2[0], d2[1], d2[2], d2[3]);
            sq += dppf<0xB1>(sq);
            const u32x4 rec = {__float_as_uint((n & 2) ? sb : sa), wl, __float_as_uint(sq), wh};
            ST16_SC1(rec, rS, ro);
        }
        if (aw == 1) { STAMP(48); CSTAMP(61); }
        if (aw == 1 || aw == 2) {
            unsigned kw_;
            asm volatile("s_load_dword %0, %1, 0x40" : "=s"(kw_) : "s"(__builtin_amdgcn_kernarg_segment_ptr()));
            warm(8);
#pragma unroll 1
            for (int t = 9; t <= NT1 + AUX_STAY; ++t) {
                BARRIER();
                if (aw == 1 && t <= 16) { STAMP(40 + t); if (!(t & 1)) CSTAMP(t <= 12 ? 57 + t / 2 : 25 + t / 2); }
                warm(t);
            }
            asm volatile("s_waitcnt lgkmcnt(0)" :: "s"(kw_));
            FLUSH();
            return;
        }
        SB(); BARRIER(); SB();
        if (aw == 3) {
            wsr = wave_sum(wsr);
            if (lane == 0) ctl1[1] = __float_as_uint(wsr);
        } else {
            if (lane == 0) { ctl1[0] = wl | 2u; ctl1[2] = wl; ctl1[3] = wh; }
            *(LAS u32x2*)(lds + L_YB + 6144 + 256 + lane * 8) = (u32x2){0u, 0u};
        }
#pragma unroll
        for (int t = 10; t <= POLL_T; ++t) BARRIER();
        SB();
        u32x4 q0, q1, q2, q3;
        u32x2 qv;
        auto request = [&]() {
            asm volatile("" ::: "memory");
            q0 = LD16_SC1(rS, o0); q1 = LD16_SC1(rS, o0 + 65536); q2 = LD16_SC1(rS, o1); q3 = LD16_SC1(rS, o1 + 65536);
            qv = __builtin_amdgcn_raw_buffer_load_b64(rV, lane * 8, 0, 16);
            SB();
        };
        if (FIN_PRIO) __builtin_amdgcn_s_setprio(FIN_PRIO);
        request();
        int b = POLL_T + 1;
        for (int spin = 0; spin <= SPIN_CAP; ++spin) {
            if (b < NT1) { BARRIER(); ++b; }
            SB();
            unsigned d = ((q0[1] ^ wl) | (q1[1] ^ wl)) | ((q2[1] ^ wl) | (q3[1] ^ wl)) | ((q0[3] ^ wh) | (q1[3] ^ wh)) | ((q2[3] ^ wh) | (q3[3] ^ wh)) | (qv[1] ^ wl);
            asm volatile("" : "+v"(d));
            if (__all(d == 0u)) break;
            request();
        }
        {
            const unsigned a0 = q0[0], a1 = q1[0], b0 = q0[2], b1 = q1[2], c0 = q2[0], c1_ = q3[0];
            const float mean = (__uint_as_float(a0) + __uint_as_float(a1)) * (1.0f / D_LEN);
            const float var = fmaxf((__uint_as_float(b0) + __uint_as_float(b1)) * (1.0f / D_LEN) - mean * mean, 0.f);
            float rden = __builtin_amdgcn_rcpf(rbf(__builtin_amdgcn_sqrtf(rbf(rbf(var) + 1e-5f))));
            if (h != 0) rden = -rden;
            float mr = rbf(mean) * rden;
            float wv = __uint_as_float(c0) + __uint_as_float(c1_);
            const unsigned vt = qv[0];
            unsigned kv = __popcll(__ballot(vt == 2u)) > __popcll(__ballot(vt == 1u)) ? 8192u : 0u;
            asm volatile("" : "+v"(rden), "+v"(mr), "+v"(wv), "+v"(kv));
            SB();
            if (b < NT1) BARRIER();
            SB();
            LAS float* tp = (LAS float*)(size_t)tb;
            tp[0] = rden; tp[128] = mr; tp[256] = wv;
            ctl1[4] = kv;
        }
        WAIT_LGKM0();
        return;
    }
#define CTL(i) ((unsigned)__builtin_amdgcn_readfirstlane((int)ctl[i]))
    const int wp = wid & 1, wq = (wid >> 1) & 1;
#define Y_OFF ((int)WS_Y + ((rt & 7) * 8 + ct) * 65536 + (wid & 3) * 16384 + (((rt >> 3) ^ (rt >> 2)) & 1) * 8192)
#define P2_DEFS \
    char* w2 = ws + kx; \
    const int sidx = idx < 28 ? xq * 28 + idx : 112 + xq * 4 + idx - 28;     \
    volatile LAS unsigned* ctl = (volatile LAS unsigned*)(lds + (h == 0 ? L_ATAB : L_CTL)); \
    const int NT = h == 0 ? 32 + KSHIFT : 32 - KSHIFT; \
    LAS float* rstat = (LAS float*)(lds + L_ATAB + 256);     \
    const int hp_ = rt * 2 + wq;                             \
    (void)sidx; (void)ctl; (void)NT; (void)rstat; (void)hp_; (void)w2
    if (__builtin_expect(wid < 4, 1)) {
        if (wid == 3 && idx < 8) {
            const unsigned r = (((unsigned)__builtin_amdgcn_s_memrealtime() * 2654435761u + blockIdx.x * 40503u) >> 9) & 0x1fdf80u;
            const unsigned first = (idx & 1) << 13;
            const char* pb = ws + WS_PROBE;
            unsigned va, vb, left;
            asm volatile("s_waitcnt lgkmcnt(0)\n\ts_load_dword %0, %3, %4\n\ts_load_dword %1, %3, %5\n\ts_waitcnt lgkmcnt(1)\n\t"
                         "s_nop 15\n\ts_getreg_b32 %2, hwreg(HW_REG_IB_STS, 8, 4)\n\ts_waitcnt lgkmcnt(0)"
                         : "=&s"(va), "=&s"(vb), "=&s"(left) : "s"(pb), "s"(r | first), "s"(r | (first ^ 8192u)) : "memory");
            const unsigned nearbit = (left != 0u ? first : first ^ 8192u) >> 13;
            if (lane == 0) ctl1[5] = nearbit == (unsigned)(xcd >> 2) ? 1u : 2u;
        }
        f32x4 acc[4][4];
#pragma unroll
        for (int i = 0; i < 16; ++i) acc[i >> 2][i & 3] = (f32x4){0.f, 0.f, 0.f, 0.f};
        unsigned tc = (unsigned)(size_t)(tab + wp * 64 + g * 8), tw = (unsigned)(size_t)(tab + 256 + prow(wq * 64 + n));
        unsigned ta = (unsigned)(size_t)lds + wp * STAGE_BYTES + wq * 8192 + lane * 16;
        constexpr int NIL = 1 << 30;
        __amdgpu_buffer_rsrc_t rW = mk_rsrc(ws, 18u << 20);
        const int hpan = rt * 2 + wq;
        int sk = (int)WS_AT + at_panel(h * 8 + ct) + at_tile((rt * 2 + wp) * 128, (ct & 4) << 11) + wq * 16384 + lane * 16;
        int st = lane != 0 ? NIL : (int)WS_FLAG1 + (1 - wp) * 65536 + h * 32768 + (rt >> 3) * 24576 + ct * 1024 + (rt & 7) * 128 + wq * 4;
        int so = h != 0 ? Y_OFF + lane * 16 : g != (n >> 2) ? NIL
               : (int)WS_STATS + (hpan & 15) * 32768 + (ct >> 2) * 16384 + (hpan >> 4) * 8192 + ((ct & 3) * 2 + wp) * 1024 + n * 16;
        int sf = lane != 0 ? NIL : (int)WS_FLAGS + (rt * 8 + ct) * 128 + wid * 4;
        u32x4 sel[2], aw[2];
        unsigned one[2];
#pragma unroll
        for (int i = 0; i < 4; ++i) {
            one[i & 1] = g == (n >> 2) && (i & 1) == ((n >> 1) & 1) ? ((n & 1) ? 0x3F800000u : 0x00003F80u) : 0u;
            aw[0][i] = aw[1][i] = (n & 3) == 0 ? 0x3F803F80u : 0u;
        }
        if ((n & 3) == 1) {
            const bf16_t* wr = a.w_r + ct * BT + wp * 64 + g * 8;
            aw[0] = *(const u32x4*)wr; aw[1] = *(const u32x4*)(wr + 32);
        }
        bf16x8 e0[4], e1[4];
        unsigned ea[2];
#pragma unroll
        for (int mp = 0; mp < 2; ++mp)
            ea[mp] = (unsigned)(size_t)lds + L_YB + wq * 16384 + wp * 8192 + (h == 0 ? mp * 1024 + lane * 16 : n * 128 + (((mp * 4 + g) ^ (n >> 1)) * 16));
        asm volatile("" : "+v"(ea[0]), "+v"(ea[1]));
        asm volatile("" : "+v"(tc), "+v"(tw), "+v"(ta), "+v"(sk), "+v"(st), "+v"(so), "+v"(sf), "+v"(one[0]), "+v"(one[1]));
        asm volatile("" : "+s"(rW));
        unsigned tg;
        if (wid == 0) STAMP(0);
        compute_loop<false, true>(lds, acc, e0, e1, NT1, ea, wp, wq, lane, [&]() { if (wid == 0) STAMP(24); if (wid == 0) STAMP(31); });
        if (wid == 0) STAMP(25);
        __builtin_amdgcn_s_waitcnt(0x0F70);
        {
            f32x4 rd[4], mr[4];
            float wsm[4];
#pragma unroll
            for (int m = 0; m < 4; ++m) {
                rd[m] = *(const LAS f32x4*)((const LAS float*)(size_t)tc + (m >> 1) * 32 + (m & 1) * 4);
                mr[m] = *(const LAS f32x4*)((const LAS float*)(size_t)tc + 128 + (m >> 1) * 32 + (m & 1) * 4);
                wsm[m] = *((const LAS float*)(size_t)tw + (m >> 1) * 32 + (m & 1) * 4);
            }
            const unsigned kv = ctl1[4];
            tg = ctl1[0];
            SB();
            sk += (int)kv; st ^= (int)kv; so += (int)kv; sf += (int)kv;
            kx = (unsigned)__builtin_amdgcn_readfirstlane((int)kv);
#pragma unroll
            for (int nn = 0; nn < 4; ++nn)
#pragma unroll
                for (int mp = 0; mp < 2; ++mp) {
                    const f32x4 u = acc[2 * mp][nn] * rd[2 * mp] - mr[2 * mp] * wsm[nn];
                    const f32x4 v = acc[2 * mp + 1][nn] * rd[2 * mp + 1] - mr[2 * mp + 1] * wsm[nn];
                    u32x4 w;
                    w[0] = pk_bf16(u[0], u[1]); w[1] = pk_bf16(u[2], u[3]);
                    w[2] = pk_bf16(v[0], v[1]); w[3] = pk_bf16(v[2], v[3]);
                    ST16_SC1(w, rW, sk + (nn * 2 + mp) * 1024);
                    *(LAS u32x4*)((LAS unsigned char*)(size_t)ta + (nn * 2 + mp) * 1024) = w;
                }
        }
        if (wid == 0) STAMP(26);
        WAIT_LGKM0();
        BARRIER();
        SB();
        {
#pragma unroll
            for (int i = 0; i < 16; ++i) { acc[i >> 2][i & 3] = (f32x4){0.f, 0.f, 0.f, 0.f}; asm volatile("" : "+v"(acc[i >> 2][i & 3])); }
            sel[0] = (u32x4){one[0], one[1], 0u, 0u}; sel[1] = (u32x4){0u, 0u, one[0], one[1]};
            asm volatile("" : "+v"(st), "+v"(so), "+v"(sf), "+v"(tg), "+v"(sel[0]), "+v"(sel[1]));
            lt = late();
            SB();
            WAIT_VM(0);
            __builtin_amdgcn_raw_buffer_store_b32(tg, rW, st, 0, 16);
            SB();
            if (wid == 0) STAMP(27);
        }
        compute_loop<true, false>(lds, acc, e0, e1, h == 0 ? 32 + KSHIFT : 32 - KSHIFT, ea, wp, wq, lane, [&]() {
            if (wid == 0) { STAMP(h == 0 ? 1 : 19); CSTAMP(22); }
        });
        P2_DEFS;
        if (wid == 0) { STAMP(h == 0 ? 2 : 12); CSTAMP(23); }
        u32x4 hq[4][2];
#define ADD(nn, m) do { acc[m][nn] = __builtin_amdgcn_mfma_f32_16x16x32_bf16(__builtin_bit_cast(bf16x8, sel[(m) & 1]), (m) < 2 ? e0[nn] : e1[nn], acc[m][nn], 0, 0, 0); SB(); } while (0)
#define PK(nn, m) do { const f32x4 c_ = acc[m][nn]; hq[nn][(m) >> 1][((m) & 1) * 2] = pk_bf16(c_[0], c_[1]); hq[nn][(m) >> 1][((m) & 1) * 2 + 1] = pk_bf16(c_[2], c_[3]); SB(); } while (0)
        if (__builtin_expect(h == 0, 1)) {
            f32x4 d1[4], d2[4];
            const f32x4 z4 = {0.f, 0.f, 0.f, 0.f};
#define S1(nn, mp) do { d1[nn] = __builtin_amdgcn_mfma_f32_16x16x32_bf16(__builtin_bit_cast(bf16x8, aw[mp]), __builtin_bit_cast(bf16x8, hq[nn][mp]), (mp) ? d1[nn] : z4, 0, 0, 0); SB(); } while (0)
#define S2(nn, mp) do { const bf16x8 f_ = __builtin_bit_cast(bf16x8, hq[nn][mp]); d2[nn] = __builtin_amdgcn_mfma_f32_16x16x32_bf16(f_, f_, (mp) ? d2[nn] : z4, 0, 0, 0); SB(); } while (0)
#define OUT(nn) do { u32x3 sv; sv[0] = min(__float_as_uint(d1[nn][0]), SENT - 1u); \
                sv[1] = min(__float_as_uint(sel4(n, d2[nn][0], d2[nn][1], d2[nn][2], d2[nn][3])), SENT - 1u); \
                sv[2] = min(__float_as_uint(d1[nn][1]), SENT - 1u); \
                __builtin_amdgcn_raw_buffer_store_b96(sv, rW, so + (nn) * 256, 0, 16); SB(); } while (0)
            ADD(0, 0); ADD(0, 1); ADD(0, 2); ADD(0, 3);
            ADD(1, 0); PK(0, 0); ADD(1, 1); PK(0, 1); ADD(1, 2); PK(0, 2); ADD(1, 3); PK(0, 3);
            ADD(2, 0); PK(1, 0); ADD(2, 1); PK(1, 1); ADD(2, 2); PK(1, 2); ADD(2, 3); PK(1, 3);
            ADD(3, 0); PK(2, 0); ADD(3, 1); PK(2, 1); ADD(3, 2); PK(2, 2); ADD(3, 3); PK(2, 3);
            S1(0, 0); PK(3, 0); S2(0, 0); PK(3, 1); S1(0, 1); PK(3, 2); S2(0, 1); PK(3, 3);
            S1(1, 0); S2(1, 0); S1(1, 1); S2(1, 1); S1(2, 0); OUT(0); S2(2, 0); S1(2, 1); S2(2, 1);
            S1(3, 0); OUT(1); S2(3, 0); S1(3, 1); S2(3, 1); OUT(2); OUT(3);
#undef S1
#undef S2
#undef OUT
            if (wid == 0) STAMP(3);
#pragma unroll
            for (int nn = 0; nn < 4; ++nn)
#pragma unroll
                for (int mp = 0; mp < 2; ++mp)
                    *(LAS u32x4*)(lds + L_HT + (wq * 64 + nn * 16 + n) * 256 + (((wp * 8 + mp * 4 + g) ^ n) * 16)) = hq[nn][mp];
            WAIT_LGKM0();
            BARRIER();
            BARRIER();
            if (wid == 0) STAMP(6);
            const int crow = (int)CTL(2 + wq) + wq * 64;
            const __amdgpu_buffer_rsrc_t rOut = mk_rsrc(a.out + (size_t)rt * BT * D_LEN, BT * D_LEN * 2);
#pragma unroll 2
            for (int j = 0; j < 8; ++j) {
                const int r = wq * 64 + wp * 32 + j * 4 + g;
                const float m_ = rstat[r * 2], d_ = rstat[r * 2 + 1];
                const u32x4 hh = *(const LAS u32x4*)(lds + L_HT + r * 256 + ((n ^ (r & 15)) * 16));
                u32x4 ov;
#pragma unroll
                for (int i = 0; i < 4; ++i) {
                    const unsigned u = hh[i];
                    const float h0 = bflo(u), h1 = bfhi(u);
                    const unsigned dd = pk_bf16(h0 - m_, h1 - m_);
                    const unsigned q = pk_bf16(bflo(dd) * d_, bfhi(dd) * d_);
                    ov[i] = pk_bf16(h0 + bflo(q), h1 + bfhi(q));
                }
                __builtin_amdgcn_raw_buffer_store_b128(ov, rOut, r == crow ? BT * D_LEN * 2 : (r * D_LEN + ct * BT + n * 8) * 2, 0, OUT_AUX);
            }
            FLUSH();
            return;
        }
#define YS(i) do { __builtin_amdgcn_raw_buffer_store_b128(hq[(i) >> 1][(i) & 1], rW, so, (i) * 1024, 16); SB(); } while (0)
        ADD(0, 0); ADD(0, 1); ADD(0, 2); ADD(0, 3);
        ADD(1, 0); PK(0, 0); ADD(1, 1); PK(0, 1); ADD(1, 2); PK(0, 2); YS(0); ADD(1, 3); PK(0, 3); YS(1);
        ADD(2, 0); PK(1, 0); ADD(2, 1); PK(1, 1); ADD(2, 2); PK(1, 2); YS(2); ADD(2, 3); PK(1, 3); YS(3);
        ADD(3, 0); PK(2, 0); ADD(3, 1); PK(2, 1); ADD(3, 2); PK(2, 2); YS(4); ADD(3, 3); PK(2, 3); YS(5);
        PK(3, 0); PK(3, 1); YS(6); PK(3, 2); PK(3, 3); YS(7);
#undef YS
#undef ADD
#undef PK
        WAIT_VM0();
        __builtin_amdgcn_raw_buffer_store_b32(1u, rW, sf, 0, 16);
        if (wid == 0) STAMP(13);
    } else {
        LAS unsigned char* l0 = lds + (wid - 4) * 4096;
        P2_DEFS;
        const __amdgpu_buffer_rsrc_t rD = mk_rsrc(lt.wd + (size_t)sidx * 32 * D_LEN, h == 0 && sidx < 112 && ((AQ << 4 >> wid) & 1) ? 32 * D_LEN * 2 : 0);
        auto load_wd = [&](int i) {
            dma_nt(rD, (unsigned)(size_t)lds + (i < 8 ? L_WD0 : L_WD1) + (wid & 3) * 8192 + (i & 7) * 1024, wd_off(wid & 3, i, lane), i * (4 * D_LEN));
        };
        const int lw = wid - 4;
        constexpr int NIL = 1 << 30;
        ring.rP = ring.rF = mk_rsrc(w2, (unsigned)WS_STATS);
        ring.voP[0] += (int)WS_AT + at_panel(h * 8 + ct);
        ring.voP[1] = ring.voP[2] = ring.voP[3] = ring.voP[0];
#define FLINE(k) (((k) >> 3) * (24576 - 2 * (int)kx) + ((k) & 7) * 128)
        const int ft = (g & 1) * 65536 + (g >> 1) * 4 + (int)WS_FLAG1 + (lw == 0 ? ct * 1024 : NIL);
        int vall = FLINE(n) + ft + h * 32768;
        ring.voF = (n >> 2) == xq ? vall : NIL;
        ring.fl = lds + L_YB + wq * 16384 + wp * 8192 + 6144 + 256;
        unsigned sa = (unsigned)(size_t)lds + L_YB + 6144 + 256 + lane * 4;
        const unsigned tg = (unsigned)__builtin_amdgcn_readfirstlane((int)ctl1[0]);
        float wsum = __uint_as_float(ctl1[1]);
        ring.lb = lw * 4096;
        int cq[3], cp[3];
        unsigned mk[3];
#pragma unroll
        for (int k = 0; k < 3; ++k) {
            const int nb = xq * 4 + NBR(k + 1);
            cq[k] = nb * 256; cp[k] = at_tile(cq[k], ring.par); mk[k] = 0x10000u << nb;
            asm volatile("" : "+s"(cq[k]), "+s"(cp[k]), "+s"(mk[k]));
        }
        unsigned m2 = (mk[0] | mk[1] | mk[2]) >> 16;
        asm volatile("" : "+s"(m2), "+s"(ring.rP));
        asm volatile("" : "+v"(vall), "+v"(ring.voF), "+v"(ring.voP[0]), "+v"(sa), "+v"(wsum));
        ring.rF = ring.rP;
        ring.voP[1] = ring.voP[2] = ring.voP[3] = ring.voP[0];
        WAIT_VM(8);
        BARRIER();
        SB();
        __amdgpu_buffer_rsrc_t rE = mk_rsrc(h == 0 ? (const void*)w2 : (const void*)a.x, (unsigned)WS_STATS);
        int ve[2];
        auto fetch = [&]() {
            if (wid == 4 && h == 0) STAMP(21);
            LAS unsigned char* base = lds + L_YB + wq * 16384 + wp * 8192;
            const int s2 = h == 0 ? 2048 : 16 * D_LEN * 2;
#pragma unroll
            for (int k = 0; k < 4; ++k) dma_2k(rE, (unsigned)(size_t)base + k * 2048, ve[0], ve[1], k * s2);
        };
#pragma unroll
        for (int j = 0; j < 2; ++j)
            ve[j] = h == 0 ? Y_OFF + lane * 16 : ((rt * BT + wq * 64 + j * 8 + (lane >> 3)) * D_LEN + ct * BT + wp * 64 + ((lane & 7) ^ (j * 4 + g)) * 8) * 2 - j * 1024;
        ring.bq[0] = (u64)((h == 0 ? a.cr : a.ci) + (size_t)(rt * 4 + lw) * 32 * K) & 0xffffffffffffull;
        ring.bq[1] = (u64)(a.ci + (size_t)(rt * 4 + lw) * 32 * K) & 0xffffffffffffull;
        int vT2 = h == 0 && n < (KSHIFT + 1) / 2 ? FLINE((xq * 4 - 1 - n) & 15) + ft + 32768 : NIL;
        int vY = lane == 0 ? (int)WS_FLAGS + (rt * 8 + ct) * 128 + lw * 4 : NIL;
        int n1 = h == 0 ? NT - 8 - NSAMP : NT - 8;
        {
            const int N = NT - 8, c = (idx + (idx >> 2)) & 3, r = (lane - 2 - c) & 3;
            int j = r == 0 ? lane + 2 : r == 3 ? lane : lane - 1;
            if (lane < 4 && !(lane >= 2 && r == 0)) j = (lane & 1) * 2 + ((c >> 1) ^ (lane >> 1)) - (lane == 3 && c == 0 ? 2 : 0);
            if (j >= N) j = lane; else if (lane == N - 1 && r == 1) j = lane;
            if (ct >= 4 && j >= 4) j = N + 3 - j;
            const int im = j >= 24;
            const int col = (xq * 1024 + 1024 + (j - im * KSHIFT) * 128) & 4095;
            ring.tq = col | (im << 12);
            ring.tp = at_tile(col, ring.par) + im * at_panel(8);
        }
        int c8 = __builtin_amdgcn_readlane(ring.tq, 0), p8 = __builtin_amdgcn_readlane(ring.tp, 0);
        asm volatile("" : "+s"(rE), "+s"(ring.bq[0]), "+s"(ring.bq[1]), "+s"(n1), "+s"(c8), "+s"(p8));
        asm volatile("" : "+v"(ring.tq), "+v"(ring.tp));
        asm volatile("" : "+v"(ve[0]), "+v"(ve[1]), "+v"(vT2), "+v"(vY));
        unsigned have = 0u;
        SB();
        if (T_DELAY) __builtin_amdgcn_s_sleep(T_DELAY);
#define GATE(m) do { if (__builtin_expect(((m) & ~have) != 0u, 0)) ring.gate(tg, sa, m, have); SB(); } while (0)
#define TILE(k, o, st) do { ring.sp = cp[k] + (o) * 256; ring.sq = cq[k] + (o); ring.template issueP<16>(l0 + (st) * STAGE_BYTES); \
                            ring.issueQ(l0 + (st) * STAGE_BYTES); ring.sample(); SB(); } while (0)
        ring.gate(tg, sa, mk[0], have);
        if (wid == 4) STAMP(28);
        ring.sp = cp[0]; ring.template issueP<16>(l0 + 2 * STAGE_BYTES); ring.sample();
        BARRIER();
        GATE(mk[1]);
        ring.sp = cp[1]; ring.template issueP<16>(l0 + 3 * STAGE_BYTES); ring.sample();
        WAIT_VM(6); BARRIER();
        if (wid == 4) STAMP(30);
        GATE(mk[2]);
        TILE(2, 0, 0);
        GATE(m2);
        ring.voF = vall;
        TILE(0, 128, 1);
        WAIT_VM(19); BARRIER();
        TILE(1, 128, 2);
        WAIT_VM(19); BARRIER();
        TILE(2, 128, 3);
        WAIT_VM(19); BARRIER();
#undef GATE
#undef TILE
        if (wid == 4) STAMP(29);
        if (have != 0xffffffffu) ring.gate(tg, sa, 0xffffffffu, have);
        ring.sq = c8; ring.sp = p8; ring.ti = 0;
        ring.voF = vT2;
        ring.fl += 256;
        ring.sample();
        ring.template steady<true, false, 17>(2);
        if (__builtin_expect(blockIdx.x == 255, 0) && tid == 256) {
            const u64 want = ((u64)ctl1[3] << 32) | ctl1[2];
            __hip_atomic_store((GAS u64*)(ws + WS_TAG), want, __ATOMIC_RELAXED, __HIP_MEMORY_SCOPE_AGENT);
        }
        if (h == 0) {
            have = 0u;
            ring.gate(tg, sa + 256, ((1u << ((KSHIFT + 1) / 2)) - 1u) * 0x10001u, have);
        }
        if (wid == 4) { CSTAMP(34); STAMP(36); }
        ring.template steady<true, false>(n1);
        if (wid == 4) { CSTAMP(35); STAMP(37); }
        if (h == 0) {
            ring.voF = vY;
            ring.fl -= 512;
            ring.template steady<true, true>(24 + KSHIFT);
        }
        if (h != 0 || ring.seen()) {
            fetch();
            WAIT_VM(16); BARRIER();
            WAIT_VM(8); BARRIER();
        } else {
            ring.sample();
            WAIT_VM(10); BARRIER();
            const bool have = ring.seen();
            if (have) { fetch(); WAIT_VM(9); } else { ring.sample(); WAIT_VM(2); }
            BARRIER();
            if (!have) {
#pragma unroll 1
                for (int spin = 0; spin <= SPIN_CAP && !ring.seen(); ++spin) { ring.sample(); WAIT_VM(0); }
                fetch();
            }
        }
        if (wid == 4 && h == 0) STAMP(20);
        WAIT_VM0();
        BARRIER();
        if (h == 0) {
            const int slot = sidx;
            LAS unsigned char* abuf = lds + L_ATAB + 2048 + lw * 128;
#pragma unroll
            for (int i = 0; i < WD_EARLY; ++i) load_wd(i);
            SB();
            BARRIER();
            __builtin_amdgcn_s_setprio(TAIL_PRIO);
            if (lane < 16) *(LAS unsigned*)(abuf + 64 + lane * 4) = 0u;
            __builtin_amdgcn_s_sleep(STAT_DELAY);
            if (wp == 0) {
                const __amdgpu_buffer_rsrc_t rS = mk_rsrc(w2 + WS_STATS + (hp_ & 15) * 32768 + (hp_ >> 4) * 8192, 24576);
                __amdgpu_buffer_rsrc_t rC = mk_rsrc(w2 + WS_CAND + (size_t)hp_ * 4096, 4096);
                __amdgpu_buffer_rsrc_t rKy = mk_rsrc(w2 + WS_KEYS + hp_ * 4, ct == 0 ? 8192 + 3 * 256 + 4 : 0);
                asm volatile("" : "+s"(rC), "+s"(rKy));
                float s1, s2, s3;
#pragma unroll 1
                for (int spin = 0;; ++spin) {
                    asm volatile("" ::: "memory");
                    u32x3 sv[16];
#pragma unroll
                    for (int j = 0; j < 16; ++j) sv[j] = __builtin_amdgcn_raw_buffer_load_b96(rS, lane * 16, (j >> 3) * 16384 + (j & 7) * 1024, 16);
                    SB();
                    s1 = 0.f; s2 = 0.f; s3 = 0.f;
#pragma unroll
                    for (int j = 0; j < 16; ++j) {
                        const unsigned u0 = sv[j][0], u1 = sv[j][1], u2 = sv[j][2];
                        s1 += __uint_as_float(u0); s2 += __uint_as_float(u1); s3 += __uint_as_float(u2);
                    }
                    const float z = (s1 - s1) + (s2 - s2) + (s3 - s3);
                    if (__builtin_expect(__all(z == 0.f), 1) || spin > SPIN_CAP) break;
                    unsigned mx = 0u;
#pragma unroll
                    for (int j = 0; j < 16; ++j) mx = max(max(mx, sv[j][0]), max(sv[j][1], sv[j][2]));
                    if (__all(mx != SENT)) break;
                }
                if (wid == 4) STAMP(4);
                const float mean = s1 * (1.0f / D_LEN);
                const float var = fmaxf(s2 * (1.0f / D_LEN) - mean * mean, 0.f);
                const float mb = rbf(mean), rden = __builtin_amdgcn_rcpf(rbf(__builtin_amdgcn_sqrtf(rbf(rbf(var) + 1e-5f))));
                const unsigned kb = wave_max_u32(r2key((s3 - mb * wsum) * rden, hp_ * 64 + lane));
                __builtin_amdgcn_raw_buffer_store_b32(kb, rKy, (lane >> 2) * 8192 + (lane & 3) * 256, 0, 16);
                {
                    const int cr_ = __builtin_amdgcn_readfirstlane((int)((0xffffu - (kb & 0xffffu)) & 63u));
                    const float m_ = __int_as_float(__builtin_amdgcn_readlane(__float_as_int(mb), cr_));
                    const float d_ = __int_as_float(__builtin_amdgcn_readlane(__float_as_int(rden), cr_));
                    u32x2 c = *(const LAS u32x2*)(lds + L_HT + (wq * 64 + cr_) * 256 + (((lane & 31) * 8) ^ ((cr_ & 15) * 16)));
#pragma unroll
                    for (int i = 0; i < 2; ++i) {
                        const unsigned u = c[i];
                        const unsigned dd = pk_bf16(bflo(u) - m_, bfhi(u) - m_);
                        const unsigned q = pk_bf16(bflo(dd) * d_, bfhi(dd) * d_);
                        c[i] = min(lane < 32 ? q : u, SENT - 1u);
                    }
                    __builtin_amdgcn_raw_buffer_store_b64(c, rC, (lane < 32 ? 2048 : 0) + ct * 256 + (lane & 31) * 8, 0, 16);
                }
                rstat[(wq * 64 + lane) * 2] = mb; rstat[(wq * 64 + lane) * 2 + 1] = rden;
                if (lane == 0) ctl[2 + wq] = (0xffffu - (kb & 0xffffu)) & 63u;
                WAIT_LGKM0();
                if (wid == 4) STAMP(5);
            }
            BARRIER();
#pragma unroll
            for (int i = WD_EARLY; i < 16; ++i) load_wd(i);
            if (slot < 112 && ((AQ << 4 >> wid) & 1)) {
                WAIT_VM0();
                if (wid == 5) STAMP(7);
                stage2(w2, slot, lw, lds + L_WD0 + lw * 8192, lds + L_WD1 + lw * 8192, abuf, lane);
                if (wid == 5) STAMP(11);
            }
            FLUSH();
            return;
        }
    }

    P2_DEFS;
    LAS float* red = (LAS float*)(lds + L_RED);
    if (sidx < 112) {
        const int slot = sidx, grp = slot >> 3, kq = slot & 7;
        const __amdgpu_buffer_rsrc_t rG = mk_rsrc(lt.wg, D_LEN * F_LEN * 2), rU = mk_rsrc(lt.wu, D_LEN * F_LEN * 2);
        auto pair = [&](int i) {
            const int d = wid * 16 + i * 2 + (lane >> 5), ky = (d & 3) | ((d >> 1) & 4), c = lane & 31;
            const int vgu = ((kq * 128 + d) * F_LEN + grp * 256 + (((c >> 1) ^ ky) * 16) + (c & 1) * 8) * 2;
            dma_nt(rG, (unsigned)(size_t)lds + wid * 8192 + i * 1024, vgu, 0);
            dma_nt(rU, (unsigned)(size_t)lds + 65536 + wid * 8192 + i * 1024, vgu ^ 512, 0);
        };
        if (wid >= 4) {
            __builtin_amdgcn_s_sleep(W_PRE_SL);
#pragma unroll
            for (int i = 0; i < W_PRE; ++i) pair(i);
        }
        BARRIER();
        if (wid < 4) {
#pragma unroll
            for (int i = 0; i < W_PRE; ++i) pair(i);
        }
#pragma unroll
        for (int i = W_PRE; i < 8; ++i) pair(i);
        const int bq_ = wid == 7 ? bquarter(0) : wid == 6 ? bquarter(1) : wid == 5 ? bquarter(2) : bquarter(3);
        u32x4 wv[16];
        if (wid >= 8 - BQ) {
            const __amdgpu_buffer_rsrc_t rD = mk_rsrc(lt.wd + (size_t)slot * 32 * D_LEN, 32 * D_LEN * 2);
            if (wid == 7) {
#pragma unroll
                for (int i = 0; i < 16; ++i)
                    dma_nt(rD, (unsigned)(size_t)lds + L_YB + 16384 + i * 1024, wd_off(bq_, i, lane), i * (4 * D_LEN));
            } else {
#pragma unroll
                for (int i = 0; i < 16; ++i)
                    asm volatile("buffer_load_dwordx4 %0, %1, %2, %3 offen nt" : "=&v"(wv[i]) : "v"(wd_off(bq_, i, lane)), "s"(rD), "s"(i * (4 * D_LEN)) : "memory");
            }
            if (lane < 16) *(LAS unsigned*)(lds + L_YB + 4096 + (7 - wid) * 128 + 64 + lane * 4) = 0u;
        }
        const __amdgpu_buffer_rsrc_t rK = mk_rsrc(w2, wid == 0 ? (unsigned)(WS_CAND + 32 * 4096) : 0u);
        const int ok_ = lane < 40 ? (int)WS_KEYS + (xcd >> 2) * 8192 + (xcd & 3) * 256 + (lane - 32) * 16 : (int)(WS_CAND + 32 * 4096);
        const int oc = (int)WS_CAND + 2048 + kq * 256 + n * 16;
        int best = 0;
        u32x4 v = {0u, 0u, 0u, 0u};
        auto request = [&]() {
            asm volatile("" ::: "memory");
            v = LD16_SC1(rK, lane < 32 ? oc + best * 4096 : ok_);
            SB();
        };
        auto settled = [&]() {
            const unsigned mx = umax4(v), mn = min(min(v[0], v[1]), min(v[2], v[3]));
            unsigned fv = lane < 32 ? ~mx : mn | (unsigned)(lane >> 3 ^ 4);
            asm volatile("" : "+v"(fv));
            const bool fine = fv != 0u;
            unsigned km = max(mx, dppu<0xB1>(mx));
            km = max(km, dppu<0x4E>(km));
            km = max(km, dppu<0x141>(km));
            const int nb = (int)(((0xffffu - ((unsigned)__builtin_amdgcn_readlane((int)km, 32) & 0xffffu)) >> 6) & 31u);
            const bool same = nb == best;
            best = nb;
            return __all(fine) && same;
        };
        if (wid == 0) {
            request();
            WAIT_VM(0);
            settled();
            request();
        } else if (wid >= 8 - BQ) WAIT_VM(16);
        else WAIT_VM(0);
        BARRIER();
        if (wid == 0) STAMP(8);
        bf16x8 wfr[4][4];
        {
            const int q = (lane >> 2) & 3, ky = q | ((g & 1) << 2);
#pragma unroll
            for (int u = 0; u < 4; ++u) {
                const int tro = (u >> 1) * 65536 + (8 * g + q) * 512 + (((wid * 2 + (u & 1)) ^ ky) * 32) + (lane & 3) * 8;
#pragma unroll
                for (int ks = 0; ks < 4; ++ks) {
                    const bf16x4 w0 = __builtin_amdgcn_ds_read_tr16_b64_v4i16((LAS bf16x4*)(lds + tro + ks * 16384));
                    const bf16x4 w1 = __builtin_amdgcn_ds_read_tr16_b64_v4i16((LAS bf16x4*)(lds + tro + ks * 16384 + 2048));
                    wfr[u][ks] = __builtin_shufflevector(w0, w1, 0, 1, 2, 3, 4, 5, 6, 7);
                }
            }
            SB();
#pragma unroll
            for (int i = 0; i < 16; ++i) asm volatile("" : "+v"(wfr[i >> 2][i & 3]));
        }
        LAS unsigned char* img = lds + (wid == 7 ? L_YB + 16384 : (wid - 4) * 16384);
        if (BQ > 1) {
            WAIT_LGKM0();
            BARRIER();
            if (wid >= 8 - BQ && wid != 7) {
                WAIT_VM(0);
#pragma unroll
                for (int i = 0; i < 16; ++i) { asm volatile("" : "+v"(wv[i])); *(LAS u32x4*)(img + i * 1024 + lane * 16) = wv[i]; }
            }
        }
        const __amdgpu_buffer_rsrc_t rPo = mk_rsrc(w2 + WS_PRE, 112 * 2048);
        int po = n < 4 ? pre_off((grp ^ (n >> 1)) * 8 + wid) + kq * 256 + n * 64 + g * 16 : 112 * 2048;
        asm volatile("" : "+v"(po));
        if (wid == 0) {
#pragma unroll 1
            for (int spin = 0; spin <= SPIN_CAP && !settled(); ++spin) {
                __builtin_amdgcn_s_sleep(1);
                request();
            }
            __builtin_amdgcn_s_waitcnt(0x0F70);
            if (lane < 16) *(LAS u32x4*)(lds + L_RED + lane * 16) = v;
            WAIT_LGKM0();
            STAMP(9);
        }
        BARRIER();
        {
            f32x4 ac[4];
            bf16x8 xf[4];
#pragma unroll
            for (int ks = 0; ks < 4; ++ks) xf[ks] = *(const LAS bf16x8*)(lds + L_RED + ks * 64 + g * 16);
            SB();
#pragma unroll
            for (int u = 0; u < 4; ++u) ac[u] = (f32x4){0.f, 0.f, 0.f, 0.f};
#pragma unroll
            for (int ks = 0; ks < 4; ++ks)
#pragma unroll
                for (int u = 0; u < 4; ++u) ac[u] = __builtin_amdgcn_mfma_f32_16x16x32_bf16(wfr[u][ks], xf[ks], ac[u], 0, 0, 0);
            u32x4 sv;
#pragma unroll
            for (int r = 0; r < 4; ++r) sv[r] = min(__float_as_uint(sel4(n, ac[0][r], ac[1][r], ac[2][r], ac[3][r])), SENT - 1u);
            ST16_SC1(sv, rPo, po);
            SB();
            asm volatile("" :: "v"(v));
        }
        if (wid == 0) STAMP(14);
        if (wid >= 8 - BQ) {
            WAIT_VM(1);
            if (wid == 8 - BQ) STAMP(10);
            stage2(w2, slot, bq_, img, img + 8192, lds + L_YB + 4096 + (7 - wid) * 128, lane);
            if (wid == 8 - BQ) STAMP(15);
        }
    } else {
        const int rj = sidx - 112;
        if (wid == 4) {
            unsigned kk;
            const unsigned k = poll_winner(w2, xcd, lane, kk);
            if (lane == 0) ctl[4] = k;
            if (lane < 32) ctl[8 + lane] = kk;
            WAIT_LGKM0();
        }
        BARRIER();
        const unsigned kw = CTL(4);
        if (wid == 0) STAMP(16);
        {
            const unsigned kc = ctl[8 + wid * 4 + g];
            const __amdgpu_buffer_rsrc_t rL = mk_rsrc(w2 + WS_CAND + (size_t)wid * 16384, 16384);
            u32x2 hv2, nv2;
            for (int spin = 0;; ++spin) {
                asm volatile("" ::: "memory");
                hv2 = __builtin_amdgcn_raw_buffer_load_b64(rL, g * 4096 + (rj * 64 + n * 4) * 2, 0, 16);
                nv2 = __builtin_amdgcn_raw_buffer_load_b64(rL, g * 4096 + 2048 + (rj * 64 + n * 4) * 2, 0, 16);
                if (__all(max(max(hv2[0], hv2[1]), max(nv2[0], nv2[1])) != SENT) || spin > SPIN_CAP) break;
                __builtin_amdgcn_s_sleep(1);
            }
            u32x2 ov;
#pragma unroll
            for (int i = 0; i < 2; ++i) {
                const unsigned u = hv2[i], q = nv2[i];
                ov[i] = pk_bf16(bflo(u) + bflo(q), bfhi(u) + bfhi(q));
            }
            const int crow = (int)(0xffffu - (kc & 0xffffu)) & (S_LEN - 1);
            if (kc != kw) *(u32x2*)(a.out + (size_t)crow * D_LEN + rj * 64 + n * 4) = ov;
        }
        const int wrow = (int)(0xffffu - (kw & 0xffffu)) & (S_LEN - 1);
        const __amdgpu_buffer_rsrc_t rC = mk_rsrc(w2 + WS_CAND + (size_t)(wrow >> 6) * 4096, 4096);
        const __amdgpu_buffer_rsrc_t rPt = mk_rsrc(w2 + WS_PART, 112 * 4096);
        const int ocol = (rj >> 2) * 256 + ((lane >> 3) & 3) * 64 + (rj & 3) * 16 + (lane & 7) * 2, opos = (lane & 7) * 8 + ((lane >> 3) & 3);
        unsigned hv, hnv;
        for (int spin = 0;; ++spin) {
            asm volatile("" ::: "memory");
            hv = __builtin_amdgcn_raw_buffer_load_b32(rC, ocol * 2, 0, 16);
            hnv = __builtin_amdgcn_raw_buffer_load_b32(rC, 2048 + ocol * 2, 0, 16);
            if (__all(max(hv, hnv) != SENT) || spin > SPIN_CAP) break;
        }
        float sgm = rbf(1.0f / (1.0f + __expf(-key2r(kw))));
        GAS unsigned* optr = (GAS unsigned*)(a.out + (size_t)wrow * D_LEN + ocol);
        asm volatile("" : "+v"(sgm), "+v"(optr));
        u32x4 pv[4];
#pragma unroll 1
        for (int spin = 0;; ++spin) {
            asm volatile("" ::: "memory");
#pragma unroll
            for (int i = 0; i < 4; ++i) pv[i] = LD16_SC1(rPt, (wid + 8 * i) * 16384 + (rj >> 3) * 8192 + g * 2048 + ((rj & 7) * 64 + n * 4) * 4);
            if (__all(max(max(umax4(pv[0]), umax4(pv[1])), max(umax4(pv[2]), umax4(pv[3]))) != SENT) || spin > SPIN_CAP) break;
            __builtin_amdgcn_s_sleep(1);
        }
        if (wid == 0) STAMP(17);
        f32x4 sm;
#pragma unroll
        for (int j = 0; j < 4; ++j) {
            const unsigned u0 = pv[0][j], u1 = pv[1][j], u2 = pv[2][j], u3 = pv[3][j];
            sm[j] = sum_rows((__uint_as_float(u0) + __uint_as_float(u1)) + (__uint_as_float(u2) + __uint_as_float(u3)));
        }
        if (lane < 16) *(LAS f32x4*)(red + wid * 64 + lane * 4) = sm;
        WAIT_LGKM0();
        BARRIER();
        if (wid == 0) {
            float t0 = 0.f, t1 = 0.f;
#pragma unroll
            for (int w = 0; w < 8; ++w) { t0 += red[w * 64 + opos]; t1 += red[w * 64 + opos + 4]; }
            const float y0 = rbf(rbf(t0) * sgm), y1 = rbf(rbf(t1) * sgm);
            const unsigned o = pk_bf16(bflo(hv) + rbf(bflo(hnv) + y0), bfhi(hv) + rbf(bfhi(hnv) + y1));
            if (lane < 32) *optr = o;
            STAMP(18);
        }
        __builtin_amdgcn_s_waitcnt(0x0F70);
    }
    FLUSH();
}

extern "C" void kernel_launch(void* const* d_in, const int* in_sizes, int n_in, void* d_out, int out_size, void* d_ws,
                              size_t ws_size, hipStream_t stream) {
    Args a;
    a.x = (const bf16_t*)d_in[0];
    a.cr = (const bf16_t*)d_in[1];
    a.ci = (const bf16_t*)d_in[2];
    a.wre = (const bf16_t*)d_in[3];
    a.wim = (const bf16_t*)d_in[4];
    a.w_r = (const bf16_t*)d_in[6];
    a.wg = (const bf16_t*)d_in[7];
    a.wu = (const bf16_t*)d_in[8];
    a.wd = (const bf16_t*)d_in[9];
    a.out = (bf16_t*)d_out;
    a.ws = (char*)d_ws + (WS_END ? ((ws_size - ((size_t)24 << 20)) & ~(size_t)255) : 0);
    (void)hipFuncSetAttribute((const void*)fused_kernel, hipFuncAttributeMaxDynamicSharedMemorySize, LDS_BYTES);
    fused_kernel<<<256, 768, LDS_BYTES, stream>>>(a);
}
```

```cpp
#include <hip/hip_runtime.h>
#include <hip/hip_bf16.h>
#include <stdint.h>

typedef unsigned short bf16_t;
typedef short bf16x8 __attribute__((ext_vector_type(8)));
typedef short bf16x4 __attribute__((ext_vector_type(4)));
typedef float f32x4 __attribute__((ext_vector_type(4)));
typedef unsigned u32x4 __attribute__((ext_vector_type(4)));
typedef unsigned u32x2 __attribute__((ext_vector_type(2)));
typedef unsigned u32x3 __attribute__((ext_vector_type(3)));
typedef unsigned long long u64;
#define LAS __attribute__((address_space(3)))
#define GAS __attribute__((address_space(1)))

constexpr int S_LEN = 2048, D_LEN = 1024, F_LEN = 3584;
constexpr unsigned SENT = 0xFFFFFFFFu;
constexpr int SPIN_CAP = 1 << 17;

constexpr size_t WS_KEYS = 0;
constexpr size_t WS_FLAGS = 16384;
constexpr size_t WS_XRD = 524288;
constexpr size_t WS_FLAG1 = 262144;
constexpr size_t WS_TAG = 131072;
constexpr size_t WS_VOTE = WS_TAG + 4096;
constexpr size_t WS_PROBE = (size_t)20 << 20;
constexpr size_t WS_AT = (size_t)4 << 20;
constexpr size_t WS_Y = (size_t)12 << 20;
constexpr size_t WS_STATS = (size_t)16 << 20;
constexpr size_t WS_PART = WS_STATS + (512u << 10);
constexpr size_t WS_CAND = WS_STATS + (1024u << 10);
constexpr size_t WS_PRE = WS_STATS + (1152u << 10);
constexpr size_t FILL_BYTES = (size_t)1408 << 10;

__device__ __forceinline__ float bflo(unsigned v) { return __uint_as_float(v << 16); }
__device__ __forceinline__ float bfhi(unsigned v) { return __uint_as_float(v & 0xffff0000u); }
__device__ __forceinline__ unsigned pk_bf16(float lo, float hi) {
    typedef __bf16 bf2 __attribute__((ext_vector_type(2)));
    typedef float f2 __attribute__((ext_vector_type(2)));
    f2 v = {lo, hi};
    bf2 r = __builtin_convertvector(v, bf2);
    return __builtin_bit_cast(unsigned, r);
}
__device__ __forceinline__ float rbf(float x) { return bflo(pk_bf16(x, 0.f) & 0xffffu); }

template <int CTRL> __device__ __forceinline__ unsigned dppu(unsigned v) {
    return (unsigned)__builtin_amdgcn_update_dpp(0, (int)v, CTRL, 0xf, 0xf, true);
}
template <int CTRL> __device__ __forceinline__ float dppf(float v) { return __uint_as_float(dppu<CTRL>(__float_as_uint(v))); }
__device__ __forceinline__ void swap16(unsigned v, unsigned& a, unsigned& b) {
    auto r = __builtin_amdgcn_permlane16_swap(v, v, false, false);
    a = r[0]; b = r[1];
}
__device__ __forceinline__ void swap32(unsigned v, unsigned& a, unsigned& b) {
    auto r = __builtin_amdgcn_permlane32_swap(v, v, false, false);
    a = r[0]; b = r[1];
}
__device__ __forceinline__ float sum_rows(float v) {
    unsigned a, b;
    swap16(__float_as_uint(v), a, b); v = __uint_as_float(a) + __uint_as_float(b);
    swap32(__float_as_uint(v), a, b); v = __uint_as_float(a) + __uint_as_float(b);
    return v;
}
__device__ __forceinline__ float wave_sum(float v) {
    v += dppf<0xB1>(v);
    v += dppf<0x4E>(v);
    v += dppf<0x141>(v);
    v += dppf<0x140>(v);
    return sum_rows(v);
}
__device__ __forceinline__ unsigned wave_max_u32(unsigned v) {
    v = max(v, dppu<0xB1>(v));
    v = max(v, dppu<0x4E>(v));
    v = max(v, dppu<0x141>(v));
    v = max(v, dppu<0x140>(v));
    unsigned a, b;
    swap16(v, a, b); v = max(a, b);
    swap32(v, a, b); v = max(a, b);
    return v;
}
__device__ __forceinline__ float sel4(int g, float a, float b, float c, float d) {
    const float t0 = (g & 1) ? b : a, t1 = (g & 1) ? d : c;
    return (g & 2) ? t1 : t0;
}
__device__ __forceinline__ unsigned umax4(u32x4 v) { return max(max(v[0], v[1]), max(v[2], v[3])); }

__device__ __forceinline__ void touch_line(const void* p, unsigned lds_dst) {
    unsigned keep;
    asm volatile("s_mov_b32 %0, m0\n\ts_mov_b32 m0, %2\n\ts_nop 0\n\tglobal_load_lds_dword %1, off\n\ts_mov_b32 m0, %0"
                 : "=&s"(keep) : "v"(p), "s"(lds_dst) : "memory");
}
__device__ __forceinline__ __amdgpu_buffer_rsrc_t mk_rsrc(const void* p, unsigned bytes) {
    return __builtin_amdgcn_make_buffer_rsrc((void*)p, 0, (int)bytes, 0x00020000);
}
#define LDS_LOAD(w, v, o, pol) "buffer_load_" w " " v ", %0, %2 offen" o pol " lds"
#define LDS_M0 "s_mov_b32 m0, %1\n\ts_nop 0\n\t"
__device__ __forceinline__ void dma_nt(__amdgpu_buffer_rsrc_t r, unsigned dst, int voff, int soff) {
    asm volatile(LDS_M0 LDS_LOAD("dwordx4", "%3", "", " nt") :: "s"(r), "s"(dst), "s"(soff), "v"(voff) : "memory", "m0");
}
template <bool SC1> __device__ __forceinline__ void dma_word(__amdgpu_buffer_rsrc_t r, unsigned dst, int voff) {
    if constexpr (SC1) asm volatile(LDS_M0 LDS_LOAD("dword", "%3", "", " sc1") :: "s"(r), "s"(dst), "n"(0), "v"(voff) : "memory", "m0");
    else asm volatile(LDS_M0 LDS_LOAD("dword", "%3", "", "") :: "s"(r), "s"(dst), "n"(0), "v"(voff) : "memory", "m0");
}
__device__ __forceinline__ void dma_2k(__amdgpu_buffer_rsrc_t r, unsigned dst, int v0, int v1, int soff) {
    asm volatile(LDS_M0 LDS_LOAD("dwordx4", "%3", "", " sc1") "\n\t" LDS_LOAD("dwordx4", "%4", " offset:1024", " sc1")
                 :: "s"(r), "s"(dst), "s"(soff), "v"(v0), "v"(v1) : "memory", "m0");
}
template <bool SC1> __device__ __forceinline__ void dma_4k(__amdgpu_buffer_rsrc_t r, unsigned dst, const int (&v)[4], int soff) {
#define FOUR(pol) asm volatile(LDS_M0 LDS_LOAD("dwordx4", "%3", "", pol) "\n\t" LDS_LOAD("dwordx4", "%4", " offset:1024", pol) "\n\t" \
        LDS_LOAD("dwordx4", "%5", " offset:2048", pol) "\n\t" LDS_LOAD("dwordx4", "%6", " offset:3072", pol) \
        :: "s"(r), "s"(dst), "s"(soff), "v"(v[0]), "v"(v[1]), "v"(v[2]), "v"(v[3]) : "memory", "m0")
    if constexpr (SC1) FOUR(" sc1"); else FOUR("");
#undef FOUR
}
#define CODE_BYTES 26624
__device__ __forceinline__ __amdgpu_buffer_rsrc_t code_rsrc() {
    unsigned long long p; unsigned d;
    asm volatile("s_getpc_b64 %0\n1:\n\ts_mov_b32 %1, 1b-_Z12fused_kernel4Args" : "=s"(p), "=s"(d));
    return __builtin_amdgcn_make_buffer_rsrc((void*)(p - d), 0, CODE_BYTES, 0x00020000);
}
#define LD16_SC1(rs, off) __builtin_amdgcn_raw_buffer_load_b128(rs, off, 0, 16)
#define ST16_SC1(v, rs, off) __builtin_amdgcn_raw_buffer_store_b128(v, rs, off, 0, 16)

constexpr int BT = 128, BK = 64, NSTAGE = 4;
constexpr int TILE_BYTES = BT * BK * 2;
constexpr int STAGE_BYTES = 2 * TILE_BYTES;
constexpr int RING_BYTES = NSTAGE * STAGE_BYTES;
constexpr int L_DUMMY = RING_BYTES;
constexpr int L_CTL = RING_BYTES + 256;
constexpr int L_RED = RING_BYTES + 512;

#define WAIT_VM(n) asm volatile("s_waitcnt vmcnt(" #n ")" ::: "memory")
#define WAIT_VM0() do { asm volatile("" ::: "memory"); __builtin_amdgcn_s_waitcnt(0x0F70); asm volatile("s_waitcnt vmcnt(0)" ::: "memory"); } while (0)
#define WAIT_LGKM0() asm volatile("s_waitcnt lgkmcnt(0)" ::: "memory")
#define BARRIER() do { asm volatile("" ::: "memory"); __builtin_amdgcn_s_barrier(); asm volatile("" ::: "memory"); } while (0)

__device__ __forceinline__ int prow(int rho) {
    const int w = rho >> 6, m = (rho >> 4) & 3, q = (rho >> 2) & 3, r = rho & 3;
    return w * 64 + (m >> 1) * 32 + q * 8 + (m & 1) * 4 + r;
}
__device__ __forceinline__ unsigned r2key(float r, int row) {
    const unsigned u = pk_bf16(r, 0.f) & 0xffffu;
    const unsigned o = (u & 0x8000u) ? (~u & 0xffffu) : (u | 0x8000u);
    return (o << 16) | (0xffffu - (unsigned)row);
}
__device__ __forceinline__ float key2r(unsigned k) {
    const unsigned o = k >> 16;
    return bflo((o & 0x8000u) ? (o & 0x7fffu) : (~o & 0xffffu));
}
#define RESET_T 2
#define RESET_U 3
#define VOTE_T 4
#define NSAMP 4
#define PSM 0x008800
#define FIN_PRIO 3
#define W_PRE 2
#define W_PRE_SL 20
#define BQ 2
#define POLL_T 12
#define AUX_SLEEP 6
#define WD_EARLY 4
#define TAIL_PRIO 3
#define STAT_DELAY 6
#define CODE_WG 1
#define AUX_STAY 1
#define T_DELAY 0
#define WS_END 1
#define CATCH1 4
#define CPF_MASK1 0x1080u
#define CPF_MASK2 0x80u
#define CPF_STAG 5
#define CPF_FIX 0x1000u
#define OUT_AUX 0
#define POLL_SLEEP 4
__device__ __forceinline__ float dot2bf(unsigned a, unsigned b, float c) {
    typedef __bf16 bf2 __attribute__((ext_vector_type(2)));
    return __builtin_amdgcn_fdot2_f32_bf16(__builtin_bit_cast(bf2, a), __builtin_bit_cast(bf2, b), c, false);
}

#define STAMP(i) do { } while (0)
#define CSTAMP(i) do { } while (0)
#define FLUSH() do { } while (0)
#define TSV do { } while (0)

#define KSHIFT 5

constexpr int L_HT = ((32 + KSHIFT) % NSTAGE) * STAGE_BYTES;
constexpr int L_WD0 = ((33 + KSHIFT) % NSTAGE) * STAGE_BYTES;
constexpr int L_WD1 = ((34 + KSHIFT) % NSTAGE) * STAGE_BYTES;
constexpr int L_YB = RING_BYTES;
constexpr int L_ATAB = L_YB;
constexpr int LDS_BYTES = RING_BYTES + 32768;

#define SB() __builtin_amdgcn_sched_barrier(0)
template <int N> __device__ __forceinline__ void wait_vm() { asm volatile("s_waitcnt vmcnt(%0)" :: "n"(N) : "memory"); }
__device__ __forceinline__ int at_tile(int c, int par) { return ((c << 8) & 0x78000) | (((c << 2) & 0x2000) ^ par); }
__device__ __forceinline__ int at_panel(int ep) { return ep << 19; }
__device__ __forceinline__ int pre_off(int slot) { const int s_ = slot >= 56, l = slot - 56 * s_; return (l >> 2) * 16384 + s_ * 8192 + (l & 3) * 2048; }
static_assert(KSHIFT >= 2 && KSHIFT <= 8 && NSAMP >= 3, "flag samples must have replaced older contents of their slot");
struct Ring {
    __amdgpu_buffer_rsrc_t rP, rQ, rF;
    u64 bq[2];
    int voP[4], voQ[4], voF;
    LAS unsigned char* fl;
    unsigned lb;
    int sp, sq;
    int poff, par;
    int tp;
    int tq, ti;
    bool img;
    template <int LD, bool PIMG> __device__ __forceinline__ void setup(int lw, int lane) {
        const int r = lane >> 3, v0 = (PIMG ? r : (r & 3) + (r & 4) * 2) * (LD * 2) + (((lane & 7) ^ (lane >> 4)) << 4);
#pragma unroll
        for (int j = 0; j < 4; ++j) {
            voQ[j] = (v0 ^ ((j & 1) * 64)) + (PIMG ? j * 8 : (j & 1) * 16 + (j >> 1) * 4) * (LD * 2) - j * 1024;
            voP[j] = PIMG ? (lw >> 1) * 16384 + (lw & 1) * 4096 + lane * 16 : voQ[j];
        }
        img = PIMG;
    }
    __device__ __forceinline__ void seek(int col, int pof) {
        sq = col; poff = pof;
        sp = (img ? at_tile(sq, par) : sq) + poff;
    }
    __device__ __forceinline__ void step() {
        sq = (sq + BK * 2) & 4095;
        sp = (img ? at_tile(sq, par) : sq) + poff;
        asm volatile("" : "+s"(sq), "+s"(sp));
    }
    __device__ __forceinline__ void sample() { dma_word<true>(rF, (unsigned)(size_t)fl, voF); }
    __device__ __forceinline__ unsigned peek(unsigned addr) {
        unsigned v;
        asm volatile("ds_read_b32 %0, %1\n\ts_waitcnt lgkmcnt(0)" : "=v"(v) : "v"(addr) : "memory");
        return v;
    }
    __device__ __forceinline__ bool seen() { return __builtin_amdgcn_readfirstlane(peek((unsigned)(size_t)fl)) == 1u; }
    __device__ __forceinline__ void gate(unsigned want, unsigned sa, unsigned need, unsigned& have) {
#pragma unroll 1
        for (int spin = 0; spin <= SPIN_CAP && (need & ~have) != 0u; ++spin) {
            if (spin) { sample(); WAIT_VM(0); __builtin_amdgcn_s_sleep(1); }
            const unsigned long long m = __ballot(peek(sa) == want);
            have = (unsigned)m & (unsigned)(m >> 32);
        }
    }
    template <int AUX> __device__ __forceinline__ void issueP(LAS unsigned char* base) { dma_4k<AUX != 0>(rP, (unsigned)(size_t)base, voP, sp); }
    __device__ __forceinline__ void issueQ(LAS unsigned char* base) { dma_4k<false>(rQ, (unsigned)(size_t)base + TILE_BYTES, voQ, sq); }
    __device__ __forceinline__ void issue(LAS unsigned char* lds) {
        issueP<0>(lds + lb);
        issueQ(lds + lb);
        SB();
        lb = (lb + STAGE_BYTES) & (RING_BYTES - 1);
        asm volatile("" : "+s"(lb));
        SB();
    }
    __device__ __forceinline__ void next1() {
        ++ti;
        sp = sq = __builtin_amdgcn_readlane(tq, ti);
        asm volatile("s_nop 4" : "+s"(sq), "+s"(sp), "+s"(ti));
        SB();
    }
    template <int CAT> __device__ __forceinline__ void ramp(LAS unsigned char* lds) {
#pragma unroll 1
        for (int i = 1; i <= CAT + 1; ++i) {
            for (int z = (PSM >> (4 * i)) & 15; z > 0; --z) __builtin_amdgcn_s_sleep(1);
            issue(lds);
            next1();
            if (i == CAT + 1) continue;
            WAIT_VM(8);
            BARRIER();
        }
    }
    template <bool P2, bool SAMP, int W = 16> __device__ __forceinline__ void steady(int cnt) {
        int t, u;
#define LD(v, r, s, o) "buffer_load_dwordx4 " v ", " r ", " s " offen" o " lds\n\t"
        if constexpr (P2) {
#define BODY(SMP) asm volatile("s_mov_b32 m0, %0\n\ts_branch 2f\n1:\n\ts_waitcnt vmcnt(%15)\n\ts_barrier\n2:\n\t" \
            LD("%7", "%6", "%2", " sc1") "s_bitcmp1_b32 %1, 12\n\ts_cselect_b64 s[96:97], %20, %19\n\t" \
            LD("%7", "%6", "%2", " offset:1024 sc1") "s_and_b32 %4, %1, 0xfff\n\ts_add_i32 %3, %3, 1\n\t" \
            LD("%7", "%6", "%2", " offset:2048 sc1") "v_readlane_b32 %1, %12, %3\n\t" LD("%7", "%6", "%2", " offset:3072 sc1") \
            SMP "s_add_i32 m0, %0, 0x4000\n\ts_add_i32 %0, %0, 0x8000\n\t" \
            LD("%8", "%5", "%4", "") "v_readlane_b32 %2, %13, %3\n\ts_and_b32 %0, %0, 0x1ffff\n\t" LD("%9", "%5", "%4", " offset:1024") "s_cmp_lg_u32 %3, %14\n\t" \
            LD("%10", "%5", "%4", " offset:2048") LD("%11", "%5", "%4", " offset:3072") "s_mov_b32 m0, %0\n\ts_cbranch_scc1 1b\n\ts_waitcnt vmcnt(%15)\n\ts_barrier" \
            : "+s"(lb), "+s"(sq), "+s"(sp), "+s"(ti), "=&s"(t), "+{s[96:99]}"(rQ) \
            : "s"(rP), "v"(voP[0]), "v"(voQ[0]), "v"(voQ[1]), "v"(voQ[2]), "v"(voQ[3]), "v"(tq), "v"(tp), "s"(cnt), "n"(W), \
              "s"(rF), "v"(voF), "s"((unsigned)(size_t)fl), "s"(bq[0]), "s"(bq[1]) : "memory", "scc", "m0")
            if constexpr (SAMP) BODY("s_mov_b32 m0, %18\n\ts_nop 0\n\tbuffer_load_dword %17, %16, 0 offen sc1 lds\n\t");
            else BODY("");
#undef BODY
            (void)u;
        } else {
            asm volatile("s_mov_b32 m0, %0\n\ts_branch 2f\n1:\n\ts_waitcnt vmcnt(16)\n\ts_barrier\n2:\n\t"
                LD("%6", "%4", "%1", "") LD("%7", "%4", "%1", " offset:1024") LD("%8", "%4", "%1", " offset:2048") LD("%9", "%4", "%1", " offset:3072")
                "s_add_i32 m0, %0, 0x4000\n\ts_mov_b32 %3, %1\n\t"
                LD("%6", "%5", "%3", "") "s_add_i32 %2, %2, 1\n\ts_add_i32 %0, %0, 0x8000\n\t"
                LD("%7", "%5", "%3", " offset:1024") "s_and_b32 %0, %0, 0x1ffff\n\tv_readlane_b32 %1, %10, %2\n\t"
                LD("%8", "%5", "%3", " offset:2048") "s_cmp_lg_u32 %2, %11\n\t"
                LD("%9", "%5", "%3", " offset:3072") "s_mov_b32 m0, %0\n\ts_cbranch_scc1 1b\n\ts_waitcnt vmcnt(16)\n\ts_barrier"
                : "+s"(lb), "+s"(sq), "+s"(ti), "=&s"(t)
                : "s"(rP), "s"(rQ), "v"(voQ[0]), "v"(voQ[1]), "v"(voQ[2]), "v"(voQ[3]), "v"(tq), "n"(D_LEN / BK)
                : "memory", "scc", "m0");
            (void)cnt;
            (void)u;
        }
#undef LD
    }
};

template <bool PLIN, bool FIRST, bool ODD, class Pre>
__device__ __forceinline__ void compute_loop(const LAS unsigned char* lds, f32x4 (&acc)[4][4], bf16x8 (&e0)[4], bf16x8 (&e1)[4], int NT, const unsigned (&ea)[2],
                                             int wp, int wq, int lane, Pre pre) {
    const int fr = lane & 15, fq = lane >> 4, key = (fr >> 1) & 7;
    const unsigned l0 = (unsigned)(size_t)lds;
    const unsigned oP0 = l0 + (PLIN ? wp * 8192 + lane * 16 : (wp * 64 + fr) * 128 + ((fq ^ key) * 16));
    const unsigned oP1 = l0 + (PLIN ? wp * 8192 + 1024 + lane * 16 : (wp * 64 + fr) * 128 + (((4 + fq) ^ key) * 16));
    const unsigned oQ0 = l0 + TILE_BYTES + (wq * 64 + fr) * 128 + ((fq ^ key) * 16);
    const unsigned oQ1 = l0 + TILE_BYTES + (wq * 64 + fr) * 128 + (((4 + fq) ^ key) * 16);
    unsigned aP0 = oP0, aQ0 = oQ0, aP1 = oP1, aQ1 = oQ1;
    bf16x8 p0[4], p1[4], r0[4], r1[4], q0[4], q1[4];

#pragma unroll
    for (int i = 0; i < 16; ++i) asm volatile("" : "+v"(acc[i >> 2][i & 3]));
    asm volatile("" : "+v"(aP0), "+v"(aQ0), "+v"(aP1), "+v"(aQ1));
#define MM(pp, qq, m, n) do { acc[m][n] = __builtin_amdgcn_mfma_f32_16x16x32_bf16(pp[m], qq[n], acc[m][n], 0, 0, 0); SB(); } while (0)
#define RD(dst, i, a, st) do { dst[i] = *(const LAS bf16x8*)((const LAS unsigned char*)(size_t)(a) + (st) * STAGE_BYTES + (i) * 2048); SB(); } while (0)
#define ADR(a) do { a ^= 2 * STAGE_BYTES; asm volatile("" : "+v"(a)); SB(); } while (0)
#define ADP(on, a, e) do { if (PLIN && (on)) { a = t == 0 ? e : a; asm volatile("" : "+v"(a)); } SB(); } while (0)
    __builtin_amdgcn_s_waitcnt(0xC07F);
    SB();
    if (FIRST) BARRIER();
    SB();
    RD(p0, 0, aP0, 0); RD(q0, 0, aQ0, 0); RD(p1, 0, aP1, 0); RD(q1, 0, aQ1, 0); RD(p1, 1, aP1, 0); RD(p0, 1, aP0, 0);
    RD(p0, 2, aP0, 0); RD(p1, 2, aP1, 0); RD(p1, 3, aP1, 0); RD(p0, 3, aP0, 0);
    pre();
    SB();
    int t = NT & ~1;
#pragma unroll 1
    do {
        MM(p0, q0, 0, 0); MM(p1, q1, 0, 0); RD(q0, 1, aQ0, 0);
        MM(p1, q1, 1, 0); MM(p0, q0, 1, 0); RD(q1, 1, aQ1, 0);
        MM(p0, q0, 2, 0); MM(p1, q1, 2, 0); RD(q0, 2, aQ0, 0);
        MM(p1, q1, 3, 0); MM(p0, q0, 3, 0); RD(q1, 2, aQ1, 0);
        MM(p0, q0, 3, 1); MM(p1, q1, 3, 1); RD(q0, 3, aQ0, 0);
        MM(p1, q1, 2, 1); MM(p0, q0, 2, 1); RD(q1, 3, aQ1, 0);
        MM(p0, q0, 1, 1); MM(p1, q1, 1, 1);
        MM(p1, q1, 0, 1); MM(p0, q0, 0, 1);
        MM(p0, q0, 0, 2); MM(p1, q1, 0, 2); __builtin_amdgcn_s_waitcnt(0xC07F); SB(); BARRIER(); SB();
        MM(p1, q1, 1, 2); RD(r0, 0, aP0, 1); MM(p0, q0, 1, 2); RD(q0, 0, aQ0, 1);
        MM(p0, q0, 2, 2); RD(r1, 0, aP1, 1); MM(p1, q1, 2, 2); RD(q1, 0, aQ1, 1);
        MM(p1, q1, 3, 2); MM(p0, q0, 3, 2); RD(r1, 1, aP1, 1);
        MM(p0, q0, 3, 3); RD(r0, 1, aP0, 1); MM(p1, q1, 3, 3);
        MM(p1, q1, 2, 3); RD(r0, 2, aP0, 1); MM(p0, q0, 2, 3); RD(r1, 2, aP1, 1);
        MM(p0, q0, 1, 3); MM(p1, q1, 1, 3); RD(r1, 3, aP1, 1);
        MM(p1, q1, 0, 3); RD(r0, 3, aP0, 1); MM(p0, q0, 0, 3);

        MM(r0, q0, 0, 0); MM(r1, q1, 0, 0); RD(q0, 1, aQ0, 1);
        MM(r1, q1, 1, 0); MM(r0, q0, 1, 0); RD(q1, 1, aQ1, 1);
        MM(r0, q0, 2, 0); MM(r1, q1, 2, 0); RD(q0, 2, aQ0, 1);
        MM(r1, q1, 3, 0); MM(r0, q0, 3, 0); RD(q1, 2, aQ1, 1);
        MM(r0, q0, 3, 1); MM(r1, q1, 3, 1); RD(q0, 3, aQ0, 1);
        MM(r1, q1, 2, 1); t -= 2; asm volatile("" : "+s"(t)); SB(); MM(r0, q0, 2, 1); RD(q1, 3, aQ1, 1);
        MM(r0, q0, 1, 1); ADR(aP0); MM(r1, q1, 1, 1); ADR(aP1);
        MM(r1, q1, 0, 1); ADP(!ODD, aP0, ea[0]); ADR(aQ0); MM(r0, q0, 0, 1); ADP(!ODD, aP1, ea[1]);
        MM(r0, q0, 0, 2); ADR(aQ1); MM(r1, q1, 0, 2); __builtin_amdgcn_s_waitcnt(0xC07F); SB(); BARRIER(); SB();
        MM(r1, q1, 1, 2); RD(p0, 0, aP0, 0); MM(r0, q0, 1, 2); RD(q0, 0, aQ0, 0);
        MM(r0, q0, 2, 2); RD(p1, 0, aP1, 0); MM(r1, q1, 2, 2); RD(q1, 0, aQ1, 0);
        MM(r1, q1, 3, 2); MM(r0, q0, 3, 2); RD(p1, 1, aP1, 0);
        MM(r0, q0, 3, 3); RD(p0, 1, aP0, 0); MM(r1, q1, 3, 3);
        MM(r1, q1, 2, 3); RD(p0, 2, aP0, 0); MM(r0, q0, 2, 3); RD(p1, 2, aP1, 0);
        MM(r0, q0, 1, 3); MM(r1, q1, 1, 3); RD(p1, 3, aP1, 0);
        MM(r1, q1, 0, 3); RD(p0, 3, aP0, 0); MM(r0, q0, 0, 3);
    } while (t != 0);
    SB();
    if constexpr (ODD) {
        MM(p0, q0, 0, 0); MM(p1, q1, 0, 0); RD(q0, 1, aQ0, 0);
        MM(p1, q1, 1, 0); MM(p0, q0, 1, 0); RD(q1, 1, aQ1, 0);
        MM(p0, q0, 2, 0); MM(p1, q1, 2, 0); RD(q0, 2, aQ0, 0);
        MM(p1, q1, 3, 0); MM(p0, q0, 3, 0); RD(q1, 2, aQ1, 0);
        MM(p0, q0, 3, 1); MM(p1, q1, 3, 1); RD(q0, 3, aQ0, 0);
        MM(p1, q1, 2, 1); MM(p0, q0, 2, 1); RD(q1, 3, aQ1, 0);
        MM(p0, q0, 1, 1); MM(p1, q1, 1, 1);
        MM(p1, q1, 0, 1); MM(p0, q0, 0, 1);
        MM(p0, q0, 0, 2); MM(p1, q1, 0, 2); __builtin_amdgcn_s_waitcnt(0xC07F); SB(); BARRIER(); SB();
        MM(p1, q1, 1, 2); RD(r0, 0, ea[0], 0); MM(p0, q0, 1, 2);
        MM(p0, q0, 2, 2); RD(r1, 0, ea[1], 0); MM(p1, q1, 2, 2);
        MM(p1, q1, 3, 2); MM(p0, q0, 3, 2); RD(r0, 1, ea[0], 0);
        MM(p0, q0, 3, 3); RD(r1, 1, ea[1], 0); MM(p1, q1, 3, 3);
        MM(p1, q1, 2, 3); RD(r0, 2, ea[0], 0); MM(p0, q0, 2, 3); RD(r1, 2, ea[1], 0);
        MM(p0, q0, 1, 3); MM(p1, q1, 1, 3); RD(r0, 3, ea[0], 0);
        MM(p1, q1, 0, 3); RD(r1, 3, ea[1], 0); MM(p0, q0, 0, 3);
    }
#pragma unroll
    for (int i = 0; i < 4; ++i) { e0[i] = ODD ? r0[i] : p0[i]; e1[i] = ODD ? r1[i] : p1[i]; }
#undef MM
#undef RD
#undef ADR
#undef ADP
}

struct Args {
    const bf16_t* x; const bf16_t* wre; const bf16_t* wim; char* ws; const bf16_t* cr; const bf16_t* ci;
    const bf16_t* w_r; const bf16_t* wg; const bf16_t* wu; const bf16_t* wd; bf16_t* out;
};

__device__ __forceinline__ unsigned poll_winner(const char* ws, int rep, int lane, unsigned& mine) {
    GAS unsigned* kp = (GAS unsigned*)(ws + WS_KEYS + (size_t)(rep >> 2) * 8192 + (rep & 3) * 256) + (lane & 31);
    unsigned kw;
    for (int spin = 0;; ++spin) {
            asm volatile("" ::: "memory");
        kw = __hip_atomic_load(kp, __ATOMIC_RELAXED, __HIP_MEMORY_SCOPE_AGENT);
        if (__all(kw != 0u) || spin > SPIN_CAP) break;
        __builtin_amdgcn_s_sleep(POLL_SLEEP);
    }
    mine = kw;
    return __builtin_amdgcn_readfirstlane(wave_max_u32(kw));
}

__device__ __forceinline__ void stage2(char* w2, int slot, int lw, LAS unsigned char* img0, LAS unsigned char* img1, LAS unsigned char* abuf, int lane) {
    const int g = lane >> 4, n = lane & 15;
    const __amdgpu_buffer_rsrc_t rPi = mk_rsrc(w2 + WS_PRE + pre_off(slot), 2048);
    unsigned pp[8];
    auto request = [&]() {
        asm volatile("" ::: "memory");
#pragma unroll
        for (int k = 0; k < 8; ++k) pp[k] = __builtin_amdgcn_raw_buffer_load_b32(rPi, k * 256 + lane * 4, 0, 16);
        SB();
    };
    request();
    bf16x8 wf[16];
    {
        const int q = (lane >> 2) & 3, ky = q | ((g & 1) << 2);
#pragma unroll
        for (int j = 0; j < 16; ++j) {
            LAS unsigned char* t = (g < 2 ? img0 : img1) + (8 * (g & 1) + q) * 512 + (((j & 7) ^ ky) + (j & 8)) * 32 + (lane & 3) * 8;
            const bf16x4 w0 = __builtin_amdgcn_ds_read_tr16_b64_v4i16((LAS bf16x4*)t);
            const bf16x4 w1 = __builtin_amdgcn_ds_read_tr16_b64_v4i16((LAS bf16x4*)(t + 2048));
            wf[j] = __builtin_shufflevector(w0, w1, 0, 1, 2, 3, 4, 5, 6, 7);
        }
        SB();
#pragma unroll
        for (int j = 0; j < 16; ++j) asm volatile("" : "+v"(wf[j]));
    }
    unsigned aa[5], pto = lane * 16;
#pragma unroll
    for (int m = 0; m < 4; ++m) aa[m] = (unsigned)(size_t)abuf + ((n >> 2) == m ? 0 : 64) + g * 16;
    aa[4] = (unsigned)(size_t)abuf + (lane & 31) * 2;
    __amdgpu_buffer_rsrc_t rPt = mk_rsrc(w2 + WS_PART + __builtin_amdgcn_readfirstlane((slot >> 2) * 16384 + (lw >> 1) * 8192 + (slot & 3) * 2048 + (lw & 1) * 1024), 1024);
    asm volatile("" : "+v"(aa[0]), "+v"(aa[1]), "+v"(aa[2]), "+v"(aa[3]), "+v"(aa[4]), "+v"(pto));
    asm volatile("" : "+s"(rPt));
    SB();
#pragma unroll 1
    for (int spin = 0; spin <= SPIN_CAP && !__all(max(max(max(pp[0], pp[1]), max(pp[2], pp[3])), max(max(pp[4], pp[5]), max(pp[6], pp[7]))) != SENT); ++spin) request();
    const float sv = ((__uint_as_float(pp[0]) + __uint_as_float(pp[1])) + (__uint_as_float(pp[2]) + __uint_as_float(pp[3]))) +
                     ((__uint_as_float(pp[4]) + __uint_as_float(pp[5])) + (__uint_as_float(pp[6]) + __uint_as_float(pp[7])));
    unsigned ug, uu;
    swap32(__float_as_uint(sv), ug, uu);
    const float gg = rbf(__uint_as_float(ug)), up = rbf(__uint_as_float(uu));
    const unsigned act = pk_bf16(rbf(gg * __builtin_amdgcn_rcpf(1.0f + __expf(-gg))) * up, 0.f);
    *(volatile LAS unsigned short*)(size_t)aa[4] = (unsigned short)act;
    bf16x8 af[4];
#pragma unroll
    for (int m = 0; m < 4; ++m) af[m] = *(volatile LAS bf16x8*)(size_t)aa[m];
    u32x4 pv;
#pragma unroll
    for (int q = 0; q < 4; ++q) {
        f32x4 d = {0.f, 0.f, 0.f, 0.f};
#pragma unroll
        for (int m = 0; m < 4; ++m) d = __builtin_amdgcn_mfma_f32_16x16x32_bf16(af[m], wf[4 * q + m], d, 0, 0, 0);
        const float e = d[0];
        pv[q] = min(__float_as_uint(e), SENT - 1u);
    }
    ST16_SC1(pv, rPt, pto);
}
__device__ __forceinline__ int wd_off(int lw, int i, int lane) {
    const int r = 2 * (i & 5) + (lane >> 5), ky = (r & 3) | ((r >> 1) & 4), c = lane & 31;
    return (lane >> 5) * (D_LEN * 2) + lw * 512 + (((c >> 1) ^ ky) * 2 + (c & 1)) * 16;
}

__global__ __launch_bounds__(768, 3) void fused_kernel(const Args a) {
    extern __shared__ __attribute__((aligned(16))) unsigned char smem[];
    constexpr int K = S_LEN, NT1 = D_LEN / BK;
    const int tid = threadIdx.x, wid = __builtin_amdgcn_readfirstlane(tid >> 6), lane = tid & 63;
    const int xcd = blockIdx.x & 7, idx = blockIdx.x >> 3;
    TSV;
    LAS unsigned char* lds = (LAS unsigned char*)smem;
    if (idx < CODE_WG && wid < 4) dma_word<false>(code_rsrc(), (unsigned)(size_t)lds + L_DUMMY, tid * 128);
    SB();
    const int h = xcd & 1, xq = xcd >> 1, rt = xq * 4 + (idx & 3), ct = idx >> 2;
    volatile LAS unsigned* ctl1 = (volatile LAS unsigned*)(lds + L_CTL);
#define NBR(k) ((rt + (k)) & 3)
    unsigned kx;
    Ring ring;
    if (__builtin_expect((wid & 12) == 4, 1)) {
        __builtin_amdgcn_s_setprio(3);
        ring.template setup<D_LEN, false>(wid - 4, lane);
        ring.lb = (wid - 4) * 4096;
        auto ord1 = [&](int i) {
            const int q = (i - idx - (idx >> 2)) & 3;
            return ((xq * 4 + h * 8 + i + 4 - (q == 0 ? 0 : q == 2 ? 4 : 2)) & 15) * 128;
        };
        ring.poff = 0;
        ring.sp = ring.sq = ord1(0);
        unsigned ox = (unsigned)(rt * 4 + wid - 4) * (32 * D_LEN * 2), ow = (unsigned)(ct * 4 + wid - 4) * (32 * D_LEN * 2);
        asm volatile("" : "+s"(ring.sq), "+s"(ring.lb), "+s"(ox), "+s"(ow), "+v"(ring.voQ[0]), "+v"(ring.voQ[1]), "+v"(ring.voQ[2]), "+v"(ring.voQ[3]));
        SB();
        ring.sp = ring.sq;
#pragma unroll
        for (int j = 0; j < 4; ++j) ring.voP[j] = ring.voQ[j];
        ring.rP = mk_rsrc((const char*)a.x + ox, 32 * D_LEN * 2);
        ring.rQ = mk_rsrc((const char*)(h == 0 ? a.wre : a.wim) + ow, 32 * D_LEN * 2);
        ring.issue(lds);
        __builtin_amdgcn_s_setprio(0);
        ring.tq = ord1(lane);
        ring.ti = 0;
        ring.next1();
        ring.par = (ct & 4) << 11;
        ring.template ramp<CATCH1>(lds);
        ring.template steady<false, false>(NT1 - 2 - CATCH1);
        ring.template setup<K, true>(wid - 4, lane);
        ring.rQ = mk_rsrc((h == 0 ? a.cr : a.ci) + (size_t)(rt * 4 + wid - 4) * 32 * K, 32 * K * 2);
        ring.seek(rt * 256, 0);
        LAS unsigned char* l0 = lds + (wid - 4) * 4096;
        ring.issueQ(l0); ring.step();
        WAIT_VM(12); BARRIER();
        ring.issueQ(l0 + STAGE_BYTES);
        ring.seek((xq * 4 + NBR(1)) * 256, 0);
        WAIT_VM(8); BARRIER();
        ring.issueQ(l0 + 2 * STAGE_BYTES);
        ring.seek((xq * 4 + NBR(2)) * 256, 0);
        BARRIER();
        ring.issueQ(l0 + 3 * STAGE_BYTES);
        kx = (unsigned)__builtin_amdgcn_readfirstlane((int)ctl1[4]);
        SB();
    }
    char* ws = a.ws + ((0ull - (unsigned long long)a.ws) & 0x1fffffull);
    const int g = lane >> 4, n = lane & 15;
    LAS float* tab = (LAS float*)(lds + L_RED);
    if (wid >= 8) {
        const int aw = wid - 8;
        GAS u64* tagp = (GAS u64*)(ws + WS_TAG);
        if (AUX_SLEEP) __builtin_amdgcn_s_sleep(AUX_SLEEP);
        u64 tag0 = __hip_atomic_load(tagp, __ATOMIC_RELAXED, __HIP_MEMORY_SCOPE_AGENT);
        SB();
        const int rho = (aw < 2 ? ct : (xq & 1) * 4 + (idx & 3)) * 16 + (aw & 1) * 8 + (n >> 1);
        unsigned fa = (unsigned)(size_t)lds + (aw >> 1) * TILE_BYTES + rho * 128 + ((((n & 1) * 4 + g) ^ ((rho >> 1) & 7)) * 16);
        constexpr int NIL = 1 << 30;
        const int at = tid - 512;
        __amdgpu_buffer_rsrc_t rZ = mk_rsrc(ws, 18u << 20);
        int z0 = (int)WS_STATS + blockIdx.x * (int)(FILL_BYTES / 256) + at * 16;
        int z1 = aw < 2 ? (at < (int)(FILL_BYTES / 4096) - 256 ? z0 + 4096 : NIL) : aw == 2 ? (lane < 10 ? blockIdx.x * 160 + lane * 16 : NIL)
               : lane < 32 ? (int)WS_FLAG1 + (lane >> 4) * 65536 + h * 32768 + (rt >> 3) * 16384 + ((lane >> 3) & 1) * 8192 + ct * 1024 + (rt & 7) * 128 + (lane & 7) * 16
               : NIL;
        u32x4 zv = {SENT, SENT, SENT, SENT};
        asm volatile("" : "+v"(fa), "+v"(z0), "+v"(z1), "+v"(zv), "+s"(rZ));
        f32x4 d1 = {0.f, 0.f, 0.f, 0.f}, d2 = {0.f, 0.f, 0.f, 0.f};
        u32x4 ones = {0x3F803F80u, 0x3F803F80u, 0x3F803F80u, 0x3F803F80u};
        asm volatile("" : "+v"(ones), "+v"(d1), "+v"(d2));
        if (aw == 0) {
            const int j = lane < 4 ? ct * 4 + lane : (idx & 3) * 8 + lane - 4;
            dma_word<true>(rZ, (unsigned)(size_t)lds + L_DUMMY, lane >= 12 ? NIL : (int)WS_XRD + (j >> 4) * 65536 + (lane < 4 ? rt : 16 + h * 8 + ct) * 2048 + (j & 15) * 128);
        }
        const char* cb;
        __amdgpu_buffer_rsrc_t rR, rS, rV;
        unsigned mask, tb, wl, wh;
        int ti, ro, o0, o1;
        auto warm = [&](int t) { if (t >= 4 && ((mask >> t) & 1)) { touch_line(cb + (ti & 7) * 512, L_DUMMY); ti += 2; } };
        u32x4 w0v, w1v;
        float wsr = 0.f;
#pragma unroll
        for (int t = 0; t < 8; ++t) {
            SB();
            BARRIER(); SB();
            const bf16x8 f = *(const LAS bf16x8*)((const LAS unsigned char*)(size_t)fa + (t & 1) * STAGE_BYTES);
            SB();
            if (aw == 1) { STAMP(40 + t); if (!(t & 1)) CSTAMP(57 + t / 2); }
            if (t == 0) {
                rR = mk_rsrc(a.w_r, aw == 3 && h == 0 ? D_LEN * 2 : 0);
                rS = mk_rsrc(ws + WS_XRD, 131072);
                rV = mk_rsrc(ws + WS_VOTE, 512);
                asm volatile("" : "+s"(rR), "+s"(rS), "+s"(rV));
            }
            if (t == 1) {
                cb = (const char*)(h ? a.ci : a.cr) + (size_t)rt * (512u << 10) + ct * 4096 + (lane >> 2) * 32768 + (lane & 3) * 128;
                mask = aw == 1 ? CPF_MASK1 : aw == 2 ? CPF_MASK2 : 0u;
                mask = (mask & CPF_FIX) | ((mask & ~CPF_FIX) << (idx % CPF_STAG));
                ti = 2 * xq + aw - 1;
                asm volatile("" : "+v"(cb), "+s"(mask), "+s"(ti));
            }
            if (t == 2) {
                w0v = __builtin_amdgcn_raw_buffer_load_b128(rR, lane * 16, 0, 0); w1v = __builtin_amdgcn_raw_buffer_load_b128(rR, lane * 16, 1024, 0);
                ro = (n | 2) != g * 4 + 2 ? 131072 : (aw < 2 ? h * 65536 + rt * 2048 : (xq >> 1) * 65536 + (16 + h * 8 + ct) * 2048) + rho * 16;
                asm volatile("" : "+v"(ro));
            }
            if (t == 3) {
                const int hi = aw == 3 ? 64 : 0;
                o0 = rt * 2048 + (hi + lane) * 16;
                o1 = o0 + (16 + h * 8 + ct - rt) * 2048;
                tb = (unsigned)(size_t)(tab + prow(hi + lane));
                asm volatile("" : "+v"(o0), "+v"(o1), "+v"(tb));
            }
            if (t == RESET_T) ST16_SC1(zv, rZ, z0);
            if (t == RESET_U) { if (aw >= 2) zv = (u32x4){0u, 0u, 0u, 0u}; ST16_SC1(zv, rZ, z1); }
            warm(t);
            if (t & 1) fa ^= 2 * STAGE_BYTES;
            d1 = __builtin_amdgcn_mfma_f32_16x16x32_bf16(f, __builtin_bit_cast(bf16x8, ones), d1, 0, 0, 0);
            if (aw < 2) d2 = __builtin_amdgcn_mfma_f32_16x16x32_bf16(f, f, d2, 0, 0, 0);
            if (t == 5) {
#pragma unroll
                for (int i = 0; i < 4; ++i) { wsr = dot2bf(w0v[i], 0x3F803F80u, wsr); wsr = dot2bf(w1v[i], 0x3F803F80u, wsr); }
                asm volatile("" : "+v"(wsr));
            }
            if (t == 1) {
                asm volatile("" : "+v"(tag0));
                const u64 want = tag0 * 6364136223846793005ull + 1442695040888963407ull;
                wl = (unsigned)want; wh = (unsigned)(want >> 32);
                asm volatile("" : "+v"(wl), "+v"(wh));
            }
            if (t == VOTE_T && aw == 3 && idx < 8) __builtin_amdgcn_raw_buffer_store_b64((u32x2){ctl1[5], wl}, rV, blockIdx.x * 8 + lane * 512, 0, 16);
            if (t == 6) WAIT_VM0();
            asm volatile("" : "+v"(d1), "+v"(d2), "+v"(fa));
        }
        SB(); BARRIER(); SB();
        {
            const float sa = d1[0] + d1[1], sb = d1[2] + d1[3];
            float sq = sel4(n, d2[0], d2[1], d2[2], d2[3]);
            sq += dppf<0xB1>(sq);
            const u32x4 rec = {__float_as_uint((n & 2) ? sb : sa), wl, __float_as_uint(sq), wh};
            ST16_SC1(rec, rS, ro);
        }
        if (aw == 1) { STAMP(48); CSTAMP(61); }
        if (aw == 1 || aw == 2) {
            warm(8);
#pragma unroll 1
            for (int t = 9; t <= NT1 + AUX_STAY; ++t) {
                BARRIER();
                if (aw == 1 && t <= 16) { STAMP(40 + t); if (!(t & 1)) CSTAMP(t <= 12 ? 57 + t / 2 : 25 + t / 2); }
                warm(t);
            }
            FLUSH();
            return;
        }
        SB(); BARRIER(); SB();
        if (aw == 3) {
            wsr = wave_sum(wsr);
            if (lane == 0) ctl1[1] = __float_as_uint(wsr);
        } else {
            if (lane == 0) { ctl1[0] = wl | 2u; ctl1[2] = wl; ctl1[3] = wh; }
            *(LAS u32x2*)(lds + L_YB + 6144 + 256 + lane * 8) = (u32x2){0u, 0u};
        }
#pragma unroll
        for (int t = 10; t <= POLL_T; ++t) BARRIER();
        SB();
        u32x4 q0, q1, q2, q3;
        u32x2 qv;
        auto request = [&]() {
            asm volatile("" ::: "memory");
            q0 = LD16_SC1(rS, o0); q1 = LD16_SC1(rS, o0 + 65536); q2 = LD16_SC1(rS, o1); q3 = LD16_SC1(rS, o1 + 65536);
            qv = __builtin_amdgcn_raw_buffer_load_b64(rV, lane * 8, 0, 16);
            SB();
        };
        if (FIN_PRIO) __builtin_amdgcn_s_setprio(FIN_PRIO);
        request();
        int b = POLL_T + 1;
        for (int spin = 0; spin <= SPIN_CAP; ++spin) {
            if (b < NT1) { BARRIER(); ++b; }
            SB();
            unsigned d = ((q0[1] ^ wl) | (q1[1] ^ wl)) | ((q2[1] ^ wl) | (q3[1] ^ wl)) | ((q0[3] ^ wh) | (q1[3] ^ wh)) | ((q2[3] ^ wh) | (q3[3] ^ wh)) | (qv[1] ^ wl);
            asm volatile("" : "+v"(d));
            if (__all(d == 0u)) break;
            request();
        }
        {
            const unsigned a0 = q0[0], a1 = q1[0], b0 = q0[2], b1 = q1[2], c0 = q2[0], c1_ = q3[0];
            const float mean = (__uint_as_float(a0) + __uint_as_float(a1)) * (1.0f / D_LEN);
            const float var = fmaxf((__uint_as_float(b0) + __uint_as_float(b1)) * (1.0f / D_LEN) - mean * mean, 0.f);
            float rden = __builtin_amdgcn_rcpf(rbf(__builtin_amdgcn_sqrtf(rbf(rbf(var) + 1e-5f))));
            if (h != 0) rden = -rden;
            float mr = rbf(mean) * rden;
            float wv = __uint_as_float(c0) + __uint_as_float(c1_);
            const unsigned vt = qv[0];
            unsigned kv = __popcll(__ballot(vt == 2u)) > __popcll(__ballot(vt == 1u)) ? 8192u : 0u;
            asm volatile("" : "+v"(rden), "+v"(mr), "+v"(wv), "+v"(kv));
            SB();
            if (b < NT1) BARRIER();
            SB();
            LAS float* tp = (LAS float*)(size_t)tb;
            tp[0] = rden; tp[128] = mr; tp[256] = wv;
            ctl1[4] = kv;
        }
        WAIT_LGKM0();
        return;
    }
#define CTL(i) ((unsigned)__builtin_amdgcn_readfirstlane((int)ctl[i]))
    const int wp = wid & 1, wq = (wid >> 1) & 1;
#define Y_OFF ((int)WS_Y + ((rt & 7) * 8 + ct) * 65536 + (wid & 3) * 16384 + (((rt >> 3) ^ (rt >> 2)) & 1) * 8192)
#define P2_DEFS \
    char* w2 = ws + kx; \
    const int sidx = idx < 28 ? xq * 28 + idx : 112 + xq * 4 + idx - 28;     \
    volatile LAS unsigned* ctl = (volatile LAS unsigned*)(lds + (h == 0 ? L_ATAB : L_CTL)); \
    const int NT = h == 0 ? 32 + KSHIFT : 32 - KSHIFT; \
    LAS float* rstat = (LAS float*)(lds + L_ATAB + 256);     \
    const int hp_ = rt * 2 + wq;                             \
    (void)sidx; (void)ctl; (void)NT; (void)rstat; (void)hp_; (void)w2
    if (__builtin_expect(wid < 4, 1)) {
        if (wid == 3 && idx < 8) {
            const unsigned r = (((unsigned)__builtin_amdgcn_s_memrealtime() * 2654435761u + blockIdx.x * 40503u) >> 9) & 0x1fdf80u;
            const unsigned first = (idx & 1) << 13;
            const char* pb = ws + WS_PROBE;
            unsigned va, vb, left;
            asm volatile("s_waitcnt lgkmcnt(0)\n\ts_load_dword %0, %3, %4\n\ts_load_dword %1, %3, %5\n\ts_waitcnt lgkmcnt(1)\n\t"
                         "s_nop 15\n\ts_getreg_b32 %2, hwreg(HW_REG_IB_STS, 8, 4)\n\ts_waitcnt lgkmcnt(0)"
                         : "=&s"(va), "=&s"(vb), "=&s"(left) : "s"(pb), "s"(r | first), "s"(r | (first ^ 8192u)) : "memory");
            const unsigned nearbit = (left != 0u ? first : first ^ 8192u) >> 13;
            if (lane == 0) ctl1[5] = nearbit == (unsigned)(xcd >> 2) ? 1u : 2u;
        }
        f32x4 acc[4][4];
#pragma unroll
        for (int i = 0; i < 16; ++i) acc[i >> 2][i & 3] = (f32x4){0.f, 0.f, 0.f, 0.f};
        unsigned tc = (unsigned)(size_t)(tab + wp * 64 + g * 8), tw = (unsigned)(size_t)(tab + 256 + prow(wq * 64 + n));
        unsigned ta = (unsigned)(size_t)lds + wp * STAGE_BYTES + wq * 8192 + lane * 16;
        constexpr int NIL = 1 << 30;
        __amdgpu_buffer_rsrc_t rW = mk_rsrc(ws, 18u << 20);
        const int hpan = rt * 2 + wq;
        int sk = (int)WS_AT + at_panel(h * 8 + ct) + at_tile((rt * 2 + wp) * 128, (ct & 4) << 11) + wq * 16384 + lane * 16;
        int st = lane != 0 ? NIL : (int)WS_FLAG1 + (1 - wp) * 65536 + h * 32768 + (rt >> 3) * 24576 + ct * 1024 + (rt & 7) * 128 + wq * 4;
        int so = h != 0 ? Y_OFF + lane * 16 : g != (n >> 2) ? NIL
               : (int)WS_STATS + (hpan & 15) * 32768 + (ct >> 2) * 16384 + (hpan >> 4) * 8192 + ((ct & 3) * 2 + wp) * 1024 + n * 16;
        int sf = lane != 0 ? NIL : (int)WS_FLAGS + (rt * 8 + ct) * 128 + wid * 4;
        u32x4 sel[2], aw[2];
        unsigned one[2];
#pragma unroll
        for (int i = 0; i < 4; ++i) {
            one[i & 1] = g == (n >> 2) && (i & 1) == ((n >> 1) & 1) ? ((n & 1) ? 0x3F800000u : 0x00003F80u) : 0u;
            aw[0][i] = aw[1][i] = (n & 3) == 0 ? 0x3F803F80u : 0u;
        }
        if ((n & 3) == 1) {
            const bf16_t* wr = a.w_r + ct * BT + wp * 64 + g * 8;
            aw[0] = *(const u32x4*)wr; aw[1] = *(const u32x4*)(wr + 32);
        }
        bf16x8 e0[4], e1[4];
        unsigned ea[2];
#pragma unroll
        for (int mp = 0; mp < 2; ++mp)
            ea[mp] = (unsigned)(size_t)lds + L_YB + wq * 16384 + wp * 8192 + (h == 0 ? mp * 1024 + lane * 16 : n * 128 + (((mp * 4 + g) ^ (n >> 1)) * 16));
        asm volatile("" : "+v"(ea[0]), "+v"(ea[1]));
        asm volatile("" : "+v"(tc), "+v"(tw), "+v"(ta), "+v"(sk), "+v"(st), "+v"(so), "+v"(sf), "+v"(one[0]), "+v"(one[1]));
        asm volatile("" : "+s"(rW));
        unsigned tg;
        if (wid == 0) STAMP(0);
        compute_loop<false, true, false>(lds, acc, e0, e1, NT1, ea, wp, wq, lane, [&]() { if (wid == 0) STAMP(24); if (wid == 0) STAMP(31); });
        if (wid == 0) STAMP(25);
        __builtin_amdgcn_s_waitcnt(0x0F70);
        {
            f32x4 rd[4], mr[4];
            float wsm[4];
#pragma unroll
            for (int m = 0; m < 4; ++m) {
                rd[m] = *(const LAS f32x4*)((const LAS float*)(size_t)tc + (m >> 1) * 32 + (m & 1) * 4);
                mr[m] = *(const LAS f32x4*)((const LAS float*)(size_t)tc + 128 + (m >> 1) * 32 + (m & 1) * 4);
                wsm[m] = *((const LAS float*)(size_t)tw + (m >> 1) * 32 + (m & 1) * 4);
            }
            const unsigned kv = ctl1[4];
            tg = ctl1[0];
            SB();
            sk += (int)kv; st ^= (int)kv; so += (int)kv; sf += (int)kv;
            kx = (unsigned)__builtin_amdgcn_readfirstlane((int)kv);
#pragma unroll
            for (int nn = 0; nn < 4; ++nn)
#pragma unroll
                for (int mp = 0; mp < 2; ++mp) {
                    const f32x4 u = acc[2 * mp][nn] * rd[2 * mp] - mr[2 * mp] * wsm[nn];
                    const f32x4 v = acc[2 * mp + 1][nn] * rd[2 * mp + 1] - mr[2 * mp + 1] * wsm[nn];
                    u32x4 w;
                    w[0] = pk_bf16(u[0], u[1]); w[1] = pk_bf16(u[2], u[3]);
                    w[2] = pk_bf16(v[0], v[1]); w[3] = pk_bf16(v[2], v[3]);
                    ST16_SC1(w, rW, sk + (nn * 2 + mp) * 1024);
                    *(LAS u32x4*)((LAS unsigned char*)(size_t)ta + (nn * 2 + mp) * 1024) = w;
                }
        }
        if (wid == 0) STAMP(26);
        WAIT_LGKM0();
        BARRIER();
        SB();
        {
#pragma unroll
            for (int i = 0; i < 16; ++i) { acc[i >> 2][i & 3] = (f32x4){0.f, 0.f, 0.f, 0.f}; asm volatile("" : "+v"(acc[i >> 2][i & 3])); }
            sel[0] = (u32x4){one[0], one[1], 0u, 0u}; sel[1] = (u32x4){0u, 0u, one[0], one[1]};
            asm volatile("" : "+v"(st), "+v"(so), "+v"(sf), "+v"(tg), "+v"(sel[0]), "+v"(sel[1]));
            SB();
            WAIT_VM(0);
            __builtin_amdgcn_raw_buffer_store_b32(tg, rW, st, 0, 16);
            SB();
            if (wid == 0) STAMP(27);
        }
        compute_loop<true, false, (KSHIFT & 1) != 0>(lds, acc, e0, e1, h == 0 ? 32 + KSHIFT : 32 - KSHIFT, ea, wp, wq, lane, [&]() {
            if (wid == 0) { STAMP(h == 0 ? 1 : 19); CSTAMP(22); }
        });
        P2_DEFS;
        if (wid == 0) { STAMP(h == 0 ? 2 : 12); CSTAMP(23); }
        u32x4 hq[4][2];
#define ADD(nn, m) do { acc[m][nn] = __builtin_amdgcn_mfma_f32_16x16x32_bf16(__builtin_bit_cast(bf16x8, sel[(m) & 1]), (m) < 2 ? e0[nn] : e1[nn], acc[m][nn], 0, 0, 0); SB(); } while (0)
#define PK(nn, m) do { const f32x4 c_ = acc[m][nn]; hq[nn][(m) >> 1][((m) & 1) * 2] = pk_bf16(c_[0], c_[1]); hq[nn][(m) >> 1][((m) & 1) * 2 + 1] = pk_bf16(c_[2], c_[3]); SB(); } while (0)
        if (__builtin_expect(h == 0, 1)) {
            f32x4 d1[4], d2[4];
            const f32x4 z4 = {0.f, 0.f, 0.f, 0.f};
#define S1(nn, mp) do { d1[nn] = __builtin_amdgcn_mfma_f32_16x16x32_bf16(__builtin_bit_cast(bf16x8, aw[mp]), __builtin_bit_cast(bf16x8, hq[nn][mp]), (mp) ? d1[nn] : z4, 0, 0, 0); SB(); } while (0)
#define S2(nn, mp) do { const bf16x8 f_ = __builtin_bit_cast(bf16x8, hq[nn][mp]); d2[nn] = __builtin_amdgcn_mfma_f32_16x16x32_bf16(f_, f_, (mp) ? d2[nn] : z4, 0, 0, 0); SB(); } while (0)
#define OUT(nn) do { u32x3 sv; sv[0] = min(__float_as_uint(d1[nn][0]), SENT - 1u); \
                sv[1] = min(__float_as_uint(sel4(n, d2[nn][0], d2[nn][1], d2[nn][2], d2[nn][3])), SENT - 1u); \
                sv[2] = min(__float_as_uint(d1[nn][1]), SENT - 1u); \
                __builtin_amdgcn_raw_buffer_store_b96(sv, rW, so + (nn) * 256, 0, 16); SB(); } while (0)
            ADD(0, 0); ADD(0, 1); ADD(0, 2); ADD(0, 3);
            ADD(1, 0); PK(0, 0); ADD(1, 1); PK(0, 1); ADD(1, 2); PK(0, 2); ADD(1, 3); PK(0, 3);
            ADD(2, 0); PK(1, 0); ADD(2, 1); PK(1, 1); ADD(2, 2); PK(1, 2); ADD(2, 3); PK(1, 3);
            ADD(3, 0); PK(2, 0); ADD(3, 1); PK(2, 1); ADD(3, 2); PK(2, 2); ADD(3, 3); PK(2, 3);
            S1(0, 0); PK(3, 0); S2(0, 0); PK(3, 1); S1(0, 1); PK(3, 2); S2(0, 1); PK(3, 3);
            S1(1, 0); S2(1, 0); S1(1, 1); S2(1, 1); S1(2, 0); OUT(0); S2(2, 0); S1(2, 1); S2(2, 1);
            S1(3, 0); OUT(1); S2(3, 0); S1(3, 1); S2(3, 1); OUT(2); OUT(3);
#undef S1
#undef S2
#undef OUT
            if (wid == 0) STAMP(3);
#pragma unroll
            for (int nn = 0; nn < 4; ++nn)
#pragma unroll
                for (int mp = 0; mp < 2; ++mp)
                    *(LAS u32x4*)(lds + L_HT + (wq * 64 + nn * 16 + n) * 256 + (((wp * 8 + mp * 4 + g) ^ n) * 16)) = hq[nn][mp];
            WAIT_LGKM0();
            BARRIER();
            BARRIER();
            if (wid == 0) STAMP(6);
            const int crow = (int)CTL(2 + wq) + wq * 64;
            const __amdgpu_buffer_rsrc_t rOut = mk_rsrc(a.out + (size_t)rt * BT * D_LEN, BT * D_LEN * 2);
#pragma unroll 2
            for (int j = 0; j < 8; ++j) {
                const int r = wq * 64 + wp * 32 + j * 4 + g;
                const float m_ = rstat[r * 2], d_ = rstat[r * 2 + 1];
                const u32x4 hh = *(const LAS u32x4*)(lds + L_HT + r * 256 + ((n ^ (r & 15)) * 16));
                u32x4 ov;
#pragma unroll
                for (int i = 0; i < 4; ++i) {
                    const unsigned u = hh[i];
                    const float h0 = bflo(u), h1 = bfhi(u);
                    const unsigned dd = pk_bf16(h0 - m_, h1 - m_);
                    const unsigned q = pk_bf16(bflo(dd) * d_, bfhi(dd) * d_);
                    ov[i] = pk_bf16(h0 + bflo(q), h1 + bfhi(q));
                }
                __builtin_amdgcn_raw_buffer_store_b128(ov, rOut, r == crow ? BT * D_LEN * 2 : (r * D_LEN + ct * BT + n * 8) * 2, 0, OUT_AUX);
            }
            FLUSH();
            return;
        }
#define YS(i) do { __builtin_amdgcn_raw_buffer_store_b128(hq[(i) >> 1][(i) & 1], rW, so, (i) * 1024, 16); SB(); } while (0)
        ADD(0, 0); ADD(0, 1); ADD(0, 2); ADD(0, 3);
        ADD(1, 0); PK(0, 0); ADD(1, 1); PK(0, 1); ADD(1, 2); PK(0, 2); YS(0); ADD(1, 3); PK(0, 3); YS(1);
        ADD(2, 0); PK(1, 0); ADD(2, 1); PK(1, 1); ADD(2, 2); PK(1, 2); YS(2); ADD(2, 3); PK(1, 3); YS(3);
        ADD(3, 0); PK(2, 0); ADD(3, 1); PK(2, 1); ADD(3, 2); PK(2, 2); YS(4); ADD(3, 3); PK(2, 3); YS(5);
        PK(3, 0); PK(3, 1); YS(6); PK(3, 2); PK(3, 3); YS(7);
#undef YS
#undef ADD
#undef PK
        WAIT_VM0();
        __builtin_amdgcn_raw_buffer_store_b32(1u, rW, sf, 0, 16);
        if (wid == 0) STAMP(13);
    } else {
        LAS unsigned char* l0 = lds + (wid - 4) * 4096;
        P2_DEFS;
        const __amdgpu_buffer_rsrc_t rD = mk_rsrc(a.wd + (size_t)sidx * 32 * D_LEN, h == 0 && sidx < 112 && wid != 4 + BQ ? 32 * D_LEN * 2 : 0);
        auto load_wd = [&](int i) {
            dma_nt(rD, (unsigned)(size_t)lds + (i < 8 ? L_WD0 : L_WD1) + (wid & 3) * 8192 + (i & 7) * 1024, wd_off(wid & 3, i, lane), i * (4 * D_LEN));
        };
        const int lw = wid - 4;
        constexpr int NIL = 1 << 30;
        ring.rP = ring.rF = mk_rsrc(w2, (unsigned)WS_STATS);
        ring.voP[0] += (int)WS_AT + at_panel(h * 8 + ct);
        ring.voP[1] = ring.voP[2] = ring.voP[3] = ring.voP[0];
#define FLINE(k) (((k) >> 3) * (24576 - 2 * (int)kx) + ((k) & 7) * 128)
        const int ft = (g & 1) * 65536 + (g >> 1) * 4 + (int)WS_FLAG1 + (lw == 0 ? ct * 1024 : NIL);
        int vall = FLINE(n) + ft + h * 32768;
        ring.voF = (n >> 2) == xq ? vall : NIL;
        ring.fl = lds + L_YB + wq * 16384 + wp * 8192 + 6144 + 256;
        unsigned sa = (unsigned)(size_t)lds + L_YB + 6144 + 256 + lane * 4;
        const unsigned tg = (unsigned)__builtin_amdgcn_readfirstlane((int)ctl1[0]);
        float wsum = __uint_as_float(ctl1[1]);
        ring.lb = lw * 4096;
        int cq[3], cp[3];
        unsigned mk[3];
#pragma unroll
        for (int k = 0; k < 3; ++k) {
            const int nb = xq * 4 + NBR(k + 1);
            cq[k] = nb * 256; cp[k] = at_tile(cq[k], ring.par); mk[k] = 0x10000u << nb;
            asm volatile("" : "+s"(cq[k]), "+s"(cp[k]), "+s"(mk[k]));
        }
        unsigned m2 = (mk[0] | mk[1] | mk[2]) >> 16;
        asm volatile("" : "+s"(m2), "+s"(ring.rP));
        asm volatile("" : "+v"(vall), "+v"(ring.voF), "+v"(ring.voP[0]), "+v"(sa), "+v"(wsum));
        ring.rF = ring.rP;
        ring.voP[1] = ring.voP[2] = ring.voP[3] = ring.voP[0];
        WAIT_VM(8);
        BARRIER();
        SB();
        __amdgpu_buffer_rsrc_t rE = mk_rsrc(h == 0 ? (const void*)w2 : (const void*)a.x, (unsigned)WS_STATS);
        int ve[2];
        auto fetch = [&]() {
            if (wid == 4 && h == 0) STAMP(21);
            LAS unsigned char* base = lds + L_YB + wq * 16384 + wp * 8192;
            const int s2 = h == 0 ? 2048 : 16 * D_LEN * 2;
#pragma unroll
            for (int k = 0; k < 4; ++k) dma_2k(rE, (unsigned)(size_t)base + k * 2048, ve[0], ve[1], k * s2);
        };
#pragma unroll
        for (int j = 0; j < 2; ++j)
            ve[j] = h == 0 ? Y_OFF + lane * 16 : ((rt * BT + wq * 64 + j * 8 + (lane >> 3)) * D_LEN + ct * BT + wp * 64 + ((lane & 7) ^ (j * 4 + g)) * 8) * 2 - j * 1024;
        ring.bq[0] = (u64)((h == 0 ? a.cr : a.ci) + (size_t)(rt * 4 + lw) * 32 * K) & 0xffffffffffffull;
        ring.bq[1] = (u64)(a.ci + (size_t)(rt * 4 + lw) * 32 * K) & 0xffffffffffffull;
        int vT2 = h == 0 && n < (KSHIFT + 1) / 2 ? FLINE((xq * 4 - 1 - n) & 15) + ft + 32768 : NIL;
        int vY = lane == 0 ? (int)WS_FLAGS + (rt * 8 + ct) * 128 + lw * 4 : NIL;
        int n1 = h == 0 ? NT - 8 - NSAMP : NT - 8;
        {
            const int N = NT - 8, c = (idx + (idx >> 2)) & 3, r = (lane - 2 - c) & 3;
            int j = r == 0 ? lane + 2 : r == 3 ? lane : lane - 1;
            if (lane < 4 && !(lane >= 2 && r == 0)) j = (lane & 1) * 2 + ((c >> 1) ^ (lane >> 1)) - (lane == 3 && c == 0 ? 2 : 0);
            if (j >= N) j = lane; else if (lane == N - 1 && r == 1) j = lane;
            if (ct >= 4 && j >= 4) j = N + 3 - j;
            const int im = j >= 24;
            const int col = (xq * 1024 + 1024 + (j - im * KSHIFT) * 128) & 4095;
            ring.tq = col | (im << 12);
            ring.tp = at_tile(col, ring.par) + im * at_panel(8);
        }
        int c8 = __builtin_amdgcn_readlane(ring.tq, 0), p8 = __builtin_amdgcn_readlane(ring.tp, 0);
        asm volatile("" : "+s"(rE), "+s"(ring.bq[0]), "+s"(ring.bq[1]), "+s"(n1), "+s"(c8), "+s"(p8));
        asm volatile("" : "+v"(ring.tq), "+v"(ring.tp));
        asm volatile("" : "+v"(ve[0]), "+v"(ve[1]), "+v"(vT2), "+v"(vY));
        unsigned have = 0u;
        SB();
        if (T_DELAY) __builtin_amdgcn_s_sleep(T_DELAY);
#define GATE(m) do { if (__builtin_expect(((m) & ~have) != 0u, 0)) ring.gate(tg, sa, m, have); SB(); } while (0)
#define TILE(k, o, st) do { ring.sp = cp[k] + (o) * 256; ring.sq = cq[k] + (o); ring.template issueP<16>(l0 + (st) * STAGE_BYTES); \
                            ring.issueQ(l0 + (st) * STAGE_BYTES); ring.sample(); SB(); } while (0)
        BARRIER();
        ring.sample();
        ring.sq = cq[2]; ring.issueQ(l0);
        WAIT_VM(4);
        SB();
        ring.gate(tg, sa, mk[0], have);
        if (wid == 4) STAMP(28);
        ring.sp = cp[0]; ring.template issueP<16>(l0 + 2 * STAGE_BYTES); ring.sample();
        GATE(mk[1]);
        ring.sp = cp[1]; ring.template issueP<16>(l0 + 3 * STAGE_BYTES); ring.sample();
        WAIT_VM(6); BARRIER();
        if (wid == 4) STAMP(30);
        GATE(mk[2]);
        ring.sp = cp[2]; ring.template issueP<16>(l0); ring.sample();
        GATE(m2);
        ring.voF = vall;
        TILE(0, 128, 1);
        WAIT_VM(15); BARRIER();
        TILE(1, 128, 2);
        WAIT_VM(19); BARRIER();
        TILE(2, 128, 3);
        WAIT_VM(19); BARRIER();
#undef GATE
#undef TILE
        if (wid == 4) STAMP(29);
        if (have != 0xffffffffu) ring.gate(tg, sa, 0xffffffffu, have);
        ring.sq = c8; ring.sp = p8; ring.ti = 0;
        ring.voF = vT2;
        ring.fl += 256;
        ring.sample();
        ring.template steady<true, false, 17>(2);
        if (__builtin_expect(blockIdx.x == 255, 0) && tid == 256) {
            const u64 want = ((u64)ctl1[3] << 32) | ctl1[2];
            __hip_atomic_store((GAS u64*)(ws + WS_TAG), want, __ATOMIC_RELAXED, __HIP_MEMORY_SCOPE_AGENT);
        }
        if (h == 0) {
            have = 0u;
            ring.gate(tg, sa + 256, ((1u << ((KSHIFT + 1) / 2)) - 1u) * 0x10001u, have);
        }
        if (wid == 4) { CSTAMP(34); STAMP(36); }
        ring.template steady<true, false>(n1);
        if (wid == 4) { CSTAMP(35); STAMP(37); }
        if (h == 0) {
            ring.voF = vY;
            ring.fl -= 512;
            ring.template steady<true, true>(24 + KSHIFT);
        }
        if (h != 0 || ring.seen()) {
            fetch();
            WAIT_VM(16); BARRIER();
            WAIT_VM(8); BARRIER();
        } else {
            ring.sample();
            WAIT_VM(10); BARRIER();
            const bool have = ring.seen();
            if (have) { fetch(); WAIT_VM(9); } else { ring.sample(); WAIT_VM(2); }
            BARRIER();
            if (!have) {
#pragma unroll 1
                for (int spin = 0; spin <= SPIN_CAP && !ring.seen(); ++spin) { ring.sample(); WAIT_VM(0); }
                fetch();
            }
        }
        if (wid == 4 && h == 0) STAMP(20);
        WAIT_VM0();
        BARRIER();
        if (h == 0) {
            const int slot = sidx;
            LAS unsigned char* abuf = lds + L_ATAB + 2048 + lw * 128;
#pragma unroll
            for (int i = 0; i < WD_EARLY; ++i) load_wd(i);
            SB();
            BARRIER();
            __builtin_amdgcn_s_setprio(TAIL_PRIO);
            if (lane < 16) *(LAS unsigned*)(abuf + 64 + lane * 4) = 0u;
            __builtin_amdgcn_s_sleep(STAT_DELAY);
            if (wp == 0) {
                const __amdgpu_buffer_rsrc_t rS = mk_rsrc(w2 + WS_STATS + (hp_ & 15) * 32768 + (hp_ >> 4) * 8192, 24576);
                __amdgpu_buffer_rsrc_t rC = mk_rsrc(w2 + WS_CAND + (size_t)hp_ * 4096, 4096);
                __amdgpu_buffer_rsrc_t rKy = mk_rsrc(w2 + WS_KEYS + hp_ * 4, ct == 0 ? 8192 + 3 * 256 + 4 : 0);
                asm volatile("" : "+s"(rC), "+s"(rKy));
                float s1, s2, s3;
#pragma unroll 1
                for (int spin = 0;; ++spin) {
                    asm volatile("" ::: "memory");
                    u32x3 sv[16];
#pragma unroll
                    for (int j = 0; j < 16; ++j) sv[j] = __builtin_amdgcn_raw_buffer_load_b96(rS, lane * 16, (j >> 3) * 16384 + (j & 7) * 1024, 16);
                    SB();
                    s1 = 0.f; s2 = 0.f; s3 = 0.f;
#pragma unroll
                    for (int j = 0; j < 16; ++j) {
                        const unsigned u0 = sv[j][0], u1 = sv[j][1], u2 = sv[j][2];
                        s1 += __uint_as_float(u0); s2 += __uint_as_float(u1); s3 += __uint_as_float(u2);
                    }
                    const float z = (s1 - s1) + (s2 - s2) + (s3 - s3);
                    if (__builtin_expect(__all(z == 0.f), 1) || spin > SPIN_CAP) break;
                    unsigned mx = 0u;
#pragma unroll
                    for (int j = 0; j < 16; ++j) mx = max(max(mx, sv[j][0]), max(sv[j][1], sv[j][2]));
                    if (__all(mx != SENT)) break;
                }
                if (wid == 4) STAMP(4);
                const float mean = s1 * (1.0f / D_LEN);
                const float var = fmaxf(s2 * (1.0f / D_LEN) - mean * mean, 0.f);
                const float mb = rbf(mean), rden = __builtin_amdgcn_rcpf(rbf(__builtin_amdgcn_sqrtf(rbf(rbf(var) + 1e-5f))));
                const unsigned kb = wave_max_u32(r2key((s3 - mb * wsum) * rden, hp_ * 64 + lane));
                __builtin_amdgcn_raw_buffer_store_b32(kb, rKy, (lane >> 2) * 8192 + (lane & 3) * 256, 0, 16);
                {
                    const int cr_ = __builtin_amdgcn_readfirstlane((int)((0xffffu - (kb & 0xffffu)) & 63u));
                    const float m_ = __int_as_float(__builtin_amdgcn_readlane(__float_as_int(mb), cr_));
                    const float d_ = __int_as_float(__builtin_amdgcn_readlane(__float_as_int(rden), cr_));
                    u32x2 c = *(const LAS u32x2*)(lds + L_HT + (wq * 64 + cr_) * 256 + (((lane & 31) * 8) ^ ((cr_ & 15) * 16)));
#pragma unroll
                    for (int i = 0; i < 2; ++i) {
                        const unsigned u = c[i];
                        const unsigned dd = pk_bf16(bflo(u) - m_, bfhi(u) - m_);
                        const unsigned q = pk_bf16(bflo(dd) * d_, bfhi(dd) * d_);
                        c[i] = min(lane < 32 ? q : u, SENT - 1u);
                    }
                    __builtin_amdgcn_raw_buffer_store_b64(c, rC, (lane < 32 ? 2048 : 0) + ct * 256 + (lane & 31) * 8, 0, 16);
                }
                rstat[(wq * 64 + lane) * 2] = mb; rstat[(wq * 64 + lane) * 2 + 1] = rden;
                if (lane == 0) ctl[2 + wq] = (0xffffu - (kb & 0xffffu)) & 63u;
                WAIT_LGKM0();
                if (wid == 4) STAMP(5);
            }
            BARRIER();
#pragma unroll
            for (int i = WD_EARLY; i < 16; ++i) load_wd(i);
            if (slot < 112 && wid != 4 + BQ) {
                WAIT_VM0();
                if (wid == 5) STAMP(7);
                stage2(w2, slot, lw, lds + L_WD0 + lw * 8192, lds + L_WD1 + lw * 8192, abuf, lane);
                if (wid == 5) STAMP(11);
            }
            FLUSH();
            return;
        }
    }

    P2_DEFS;
    LAS float* red = (LAS float*)(lds + L_RED);
    if (sidx < 112) {
        const int slot = sidx, grp = slot >> 3, kq = slot & 7;
        const __amdgpu_buffer_rsrc_t rG = mk_rsrc(a.wg, D_LEN * F_LEN * 2), rU = mk_rsrc(a.wu, D_LEN * F_LEN * 2);
        auto pair = [&](int i) {
            const int d = wid * 16 + i * 2 + (lane >> 5), ky = (d & 3) | ((d >> 1) & 4), c = lane & 31;
            const int vgu = ((kq * 128 + d) * F_LEN + grp * 256 + (((c >> 1) ^ ky) * 16) + (c & 1) * 8) * 2;
            dma_nt(rG, (unsigned)(size_t)lds + wid * 8192 + i * 1024, vgu, 0);
            dma_nt(rU, (unsigned)(size_t)lds + 65536 + wid * 8192 + i * 1024, vgu ^ 512, 0);
        };
        if (wid >= 4) {
            __builtin_amdgcn_s_sleep(W_PRE_SL);
#pragma unroll
            for (int i = 0; i < W_PRE; ++i) pair(i);
        }
        BARRIER();
        if (wid < 4) {
#pragma unroll
            for (int i = 0; i < W_PRE; ++i) pair(i);
        }
#pragma unroll
        for (int i = W_PRE; i < 8; ++i) pair(i);
        if (wid == 7) {
            const __amdgpu_buffer_rsrc_t rD = mk_rsrc(a.wd + (size_t)slot * 32 * D_LEN, 32 * D_LEN * 2);
#pragma unroll
            for (int i = 0; i < 16; ++i)
                dma_nt(rD, (unsigned)(size_t)lds + L_YB + 16384 + i * 1024, wd_off(BQ, i, lane), i * (4 * D_LEN));
            if (lane < 16) *(LAS unsigned*)(lds + L_YB + 4096 + 64 + lane * 4) = 0u;
        }
        const __amdgpu_buffer_rsrc_t rK = mk_rsrc(w2, wid == 0 ? (unsigned)(WS_CAND + 32 * 4096) : 0u);
        const int ok_ = lane < 40 ? (int)WS_KEYS + (xcd >> 2) * 8192 + (xcd & 3) * 256 + (lane - 32) * 16 : (int)(WS_CAND + 32 * 4096);
        const int oc = (int)WS_CAND + 2048 + kq * 256 + n * 16;
        int best = 0;
        u32x4 v = {0u, 0u, 0u, 0u};
        auto request = [&]() {
            asm volatile("" ::: "memory");
            v = LD16_SC1(rK, lane < 32 ? oc + best * 4096 : ok_);
            SB();
        };
        auto settled = [&]() {
            const unsigned mx = umax4(v), mn = min(min(v[0], v[1]), min(v[2], v[3]));
            unsigned fv = lane < 32 ? ~mx : mn | (unsigned)(lane >> 3 ^ 4);
            asm volatile("" : "+v"(fv));
            const bool fine = fv != 0u;
            unsigned km = max(mx, dppu<0xB1>(mx));
            km = max(km, dppu<0x4E>(km));
            km = max(km, dppu<0x141>(km));
            const int nb = (int)(((0xffffu - ((unsigned)__builtin_amdgcn_readlane((int)km, 32) & 0xffffu)) >> 6) & 31u);
            const bool same = nb == best;
            best = nb;
            return __all(fine) && same;
        };
        if (wid == 0) {
            request();
            WAIT_VM(0);
            settled();
            request();
        } else if (wid == 7) WAIT_VM(16);
        else WAIT_VM(0);
        BARRIER();
        if (wid == 0) STAMP(8);
        bf16x8 wfr[4][4];
        {
            const int q = (lane >> 2) & 3, ky = q | ((g & 1) << 2);
#pragma unroll
            for (int u = 0; u < 4; ++u) {
                const int tro = (u >> 1) * 65536 + (8 * g + q) * 512 + (((wid * 2 + (u & 1)) ^ ky) * 32) + (lane & 3) * 8;
#pragma unroll
                for (int ks = 0; ks < 4; ++ks) {
                    const bf16x4 w0 = __builtin_amdgcn_ds_read_tr16_b64_v4i16((LAS bf16x4*)(lds + tro + ks * 16384));
                    const bf16x4 w1 = __builtin_amdgcn_ds_read_tr16_b64_v4i16((LAS bf16x4*)(lds + tro + ks * 16384 + 2048));
                    wfr[u][ks] = __builtin_shufflevector(w0, w1, 0, 1, 2, 3, 4, 5, 6, 7);
                }
            }
            SB();
#pragma unroll
            for (int i = 0; i < 16; ++i) asm volatile("" : "+v"(wfr[i >> 2][i & 3]));
        }
        const __amdgpu_buffer_rsrc_t rPo = mk_rsrc(w2 + WS_PRE, 112 * 2048);
        int po = n < 4 ? pre_off((grp ^ (n >> 1)) * 8 + wid) + kq * 256 + n * 64 + g * 16 : 112 * 2048;
        asm volatile("" : "+v"(po));
        if (wid == 0) {
#pragma unroll 1
            for (int spin = 0; spin <= SPIN_CAP && !settled(); ++spin) {
                __builtin_amdgcn_s_sleep(1);
                request();
            }
            __builtin_amdgcn_s_waitcnt(0x0F70);
            if (lane < 16) *(LAS u32x4*)(lds + L_RED + lane * 16) = v;
            WAIT_LGKM0();
            STAMP(9);
        }
        BARRIER();
        {
            f32x4 ac[4];
            bf16x8 xf[4];
#pragma unroll
            for (int ks = 0; ks < 4; ++ks) xf[ks] = *(const LAS bf16x8*)(lds + L_RED + ks * 64 + g * 16);
            SB();
#pragma unroll
            for (int u = 0; u < 4; ++u) ac[u] = (f32x4){0.f, 0.f, 0.f, 0.f};
#pragma unroll
            for (int ks = 0; ks < 4; ++ks)
#pragma unroll
                for (int u = 0; u < 4; ++u) ac[u] = __builtin_amdgcn_mfma_f32_16x16x32_bf16(wfr[u][ks], xf[ks], ac[u], 0, 0, 0);
            u32x4 sv;
#pragma unroll
            for (int r = 0; r < 4; ++r) sv[r] = min(__float_as_uint(sel4(n, ac[0][r], ac[1][r], ac[2][r], ac[3][r])), SENT - 1u);
            ST16_SC1(sv, rPo, po);
            SB();
            asm volatile("" :: "v"(v));
        }
        if (wid == 0) STAMP(14);
        if (wid == 7) {
            WAIT_VM(1);
            STAMP(10);
            stage2(w2, slot, BQ, lds + L_YB + 16384, lds + L_YB + 24576, lds + L_YB + 4096, lane);
            STAMP(15);
        }
    } else {
        const int rj = sidx - 112;
        if (wid == 4) {
            unsigned kk;
            const unsigned k = poll_winner(w2, xcd, lane, kk);
            if (lane == 0) ctl[4] = k;
            if (lane < 32) ctl[8 + lane] = kk;
            WAIT_LGKM0();
        }
        BARRIER();
        const unsigned kw = CTL(4);
        if (wid == 0) STAMP(16);
        {
            const unsigned kc = ctl[8 + wid * 4 + g];
            const __amdgpu_buffer_rsrc_t rL = mk_rsrc(w2 + WS_CAND + (size_t)wid * 16384, 16384);
            u32x2 hv2, nv2;
            for (int spin = 0;; ++spin) {
                asm volatile("" ::: "memory");
                hv2 = __builtin_amdgcn_raw_buffer_load_b64(rL, g * 4096 + (rj * 64 + n * 4) * 2, 0, 16);
                nv2 = __builtin_amdgcn_raw_buffer_load_b64(rL, g * 4096 + 2048 + (rj * 64 + n * 4) * 2, 0, 16);
                if (__all(max(max(hv2[0], hv2[1]), max(nv2[0], nv2[1])) != SENT) || spin > SPIN_CAP) break;
                __builtin_amdgcn_s_sleep(1);
            }
            u32x2 ov;
#pragma unroll
            for (int i = 0; i < 2; ++i) {
                const unsigned u = hv2[i], q = nv2[i];
                ov[i] = pk_bf16(bflo(u) + bflo(q), bfhi(u) + bfhi(q));
            }
            const int crow = (int)(0xffffu - (kc & 0xffffu)) & (S_LEN - 1);
            if (kc != kw) *(u32x2*)(a.out + (size_t)crow * D_LEN + rj * 64 + n * 4) = ov;
        }
        const int wrow = (int)(0xffffu - (kw & 0xffffu)) & (S_LEN - 1);
        const __amdgpu_buffer_rsrc_t rC = mk_rsrc(w2 + WS_CAND + (size_t)(wrow >> 6) * 4096, 4096);
        const __amdgpu_buffer_rsrc_t rPt = mk_rsrc(w2 + WS_PART, 112 * 4096);
        const int ocol = (rj >> 2) * 256 + ((lane >> 3) & 3) * 64 + (rj & 3) * 16 + (lane & 7) * 2, opos = (lane & 7) * 8 + ((lane >> 3) & 3);
        unsigned hv, hnv;
        for (int spin = 0;; ++spin) {
            asm volatile("" ::: "memory");
            hv = __builtin_amdgcn_raw_buffer_load_b32(rC, ocol * 2, 0, 16);
            hnv = __builtin_amdgcn_raw_buffer_load_b32(rC, 2048 + ocol * 2, 0, 16);
            if (__all(max(hv, hnv) != SENT) || spin > SPIN_CAP) break;
        }
        float sgm = rbf(1.0f / (1.0f + __expf(-key2r(kw))));
        GAS unsigned* optr = (GAS unsigned*)(a.out + (size_t)wrow * D_LEN + ocol);
        asm volatile("" : "+v"(sgm), "+v"(optr));
        u32x4 pv[4];
#pragma unroll 1
        for (int spin = 0;; ++spin) {
            asm volatile("" ::: "memory");
#pragma unroll
            for (int i = 0; i < 4; ++i) pv[i] = LD16_SC1(rPt, (wid + 8 * i) * 16384 + (rj >> 3) * 8192 + g * 2048 + ((rj & 7) * 64 + n * 4) * 4);
            if (__all(max(max(umax4(pv[0]), umax4(pv[1])), max(umax4(pv[2]), umax4(pv[3]))) != SENT) || spin > SPIN_CAP) break;
            __builtin_amdgcn_s_sleep(1);
        }
        if (wid == 0) STAMP(17);
        f32x4 sm;
#pragma unroll
        for (int j = 0; j < 4; ++j) {
            const unsigned u0 = pv[0][j], u1 = pv[1][j], u2 = pv[2][j], u3 = pv[3][j];
            sm[j] = sum_rows((__uint_as_float(u0) + __uint_as_float(u1)) + (__uint_as_float(u2) + __uint_as_float(u3)));
        }
        if (lane < 16) *(LAS f32x4*)(red + wid * 64 + lane * 4) = sm;
        WAIT_LGKM0();
        BARRIER();
        if (wid == 0) {
            float t0 = 0.f, t1 = 0.f;
#pragma unroll
            for (int w = 0; w < 8; ++w) { t0 += red[w * 64 + opos]; t1 += red[w * 64 + opos + 4]; }
            const float y0 = rbf(rbf(t0) * sgm), y1 = rbf(rbf(t1) * sgm);
            const unsigned o = pk_bf16(bflo(hv) + rbf(bflo(hnv) + y0), bfhi(hv) + rbf(bfhi(hnv) + y1));
            if (lane < 32) *optr = o;
            STAMP(18);
        }
        __builtin_amdgcn_s_waitcnt(0x0F70);
    }
    FLUSH();
}

extern "C" void kernel_launch(void* const* d_in, const int* in_sizes, int n_in, void* d_out, int out_size, void* d_ws,
                              size_t ws_size, hipStream_t stream) {
    Args a;
    a.x = (const bf16_t*)d_in[0];
    a.cr = (const bf16_t*)d_in[1];
    a.ci = (const bf16_t*)d_in[2];
    a.wre = (const bf16_t*)d_in[3];
    a.wim = (const bf16_t*)d_in[4];
    a.w_r = (const bf16_t*)d_in[6];
    a.wg = (const bf16_t*)d_in[7];
    a.wu = (const bf16_t*)d_in[8];
    a.wd = (const bf16_t*)d_in[9];
    a.out = (bf16_t*)d_out;
    a.ws = (char*)d_ws + (WS_END ? ((ws_size - ((size_t)24 << 20)) & ~(size_t)255) : 0);
    (void)hipFuncSetAttribute((const void*)fused_kernel, hipFuncAttributeMaxDynamicSharedMemorySize, LDS_BYTES);
    fused_kernel<<<256, 768, LDS_BYTES, stream>>>(a);
}
```
